```python
import torch

def _softmax(x, axis=-1):
    return torch.softmax(x, dim=axis)
def _gelu(x):
    return torch.nn.functional.gelu(x, approximate="tanh")
def _silu(x):
    return torch.nn.functional.silu(x)
def _sigmoid(x):
    return torch.sigmoid(x)
def _layernorm(x, axis=-1, eps=1e-5):
    m = x.mean(dim=axis, keepdim=True)
    v = x.var(dim=axis, keepdim=True, unbiased=False)
    return (x - m) / torch.sqrt(v + eps)
def _scatter_add(tgt, idx, src, axis=0):
    return tgt.clone().index_add_(axis, idx, src)
def _segment_sum(src, seg, n):
    out = torch.zeros((n,) + src.shape[1:], dtype=src.dtype)
    return out.index_add_(0, seg, src)
def _pad(x, pw):
    flat = tuple(v for pair in reversed(pw) for v in pair)
    return torch.nn.functional.pad(x, flat)

kernel_name = 'gated_mix__softmax_full_mha__dense_activation_gelu__prompt'

def setup_inputs():

    _w = (lambda a: (lambda t: t.to(torch.bfloat16) if t.is_floating_point() else t)(torch.as_tensor(a)))
    def _A_setup():
        B, S, D = 1, 2048, 512
        return {
            "x": _w((lambda *s: torch.randn(*s, dtype=torch.bfloat16) * 0.05)(B, S, D)),
            "wqkv": _w((lambda *s: torch.randn(*s, dtype=torch.bfloat16) * 0.8)(D, 3 * D)),
            "wo": _w((lambda *s: torch.randn(*s, dtype=torch.bfloat16) * 0.05)(D, D)),
        }
    _A_in = _A_setup()

    def _B_setup():
        B, S, D, F = 1, 2048, 512, 1792
        return {
            "x": _w((lambda *s: torch.randn(*s, dtype=torch.bfloat16) * 0.05)(B, S, D)),
            "w1": _w((lambda *s: torch.randn(*s, dtype=torch.bfloat16) * 3.2)(D, F) * (D ** -0.5)),
            "b1": _w((lambda *s: torch.randn(*s, dtype=torch.bfloat16) * 3.2)(F) * 0.01),
            "w2": _w((lambda *s: torch.randn(*s, dtype=torch.bfloat16) * 0.8)(F, D) * (F ** -0.5)),
            "b2": _w((lambda *s: torch.randn(*s, dtype=torch.bfloat16) * 1.6)(D) * 0.01),
        }
    _B_in = _B_setup()

    return {
        "A_x": _A_in["x"],
        "A_wqkv": _A_in["wqkv"],
        "A_wo": _A_in["wo"],
        "B_x": _B_in["x"],
        "B_w1": _B_in["w1"],
        "B_b1": _B_in["b1"],
        "B_w2": _B_in["w2"],
        "B_b2": _B_in["b2"],
        "gate_w": _w((lambda *s: torch.randn(*s, dtype=torch.bfloat16) * 0.05)(512) * 16.0),
    }

def reference(A_x, A_wqkv, A_wo, B_x, B_w1, B_b1, B_w2, B_b2, gate_w):
    def _A_ref(x, wqkv, wo):
        B, S, D = x.shape
        H = 32; Dh = D // H
        qkv = (lambda x, *d: x.permute(*d))((x @ wqkv).reshape(B, S, 3, H, Dh), 2, 0, 3, 1, 4)
        q, k, v = qkv[0], qkv[1], qkv[2]
        scale = Dh ** -0.5
        scores = torch.einsum("bhqd,bhkd->bhqk", q, k) * scale
        p = _softmax(scores, axis=-1)
        o = torch.einsum("bhqk,bhkd->bhqd", p, v)
        return (lambda x, *d: x.permute(*d))(o, 0, 2, 1, 3).reshape(B, S, D) @ wo

    def _B_ref(x, w1, b1, w2, b2):
        h = torch.einsum("bsd,df->bsf", x, w1) + b1
        h = _gelu(h)
        return torch.einsum("bsf,fd->bsd", h, w2) + b2

    x = A_x
    g = _sigmoid((x * gate_w).sum(-1, keepdims=True))
    a = _A_ref(x, A_wqkv, A_wo)
    b = _B_ref(x, B_w1, B_b1, B_w2, B_b2)
    return g * a + (1 - g) * b
```

```cpp
#include <hip/hip_runtime.h>
#include <hip/hip_bf16.h>
#include <stdint.h>

typedef __attribute__((ext_vector_type(8))) __bf16 bf16x8;
typedef __attribute__((ext_vector_type(16))) __bf16 bf16x16;
typedef __attribute__((ext_vector_type(2))) __bf16 bf16x2;
typedef __attribute__((ext_vector_type(4))) short s16x4;
typedef __attribute__((ext_vector_type(16))) float f32x16;
typedef __attribute__((ext_vector_type(8))) float f32x8;
typedef __attribute__((ext_vector_type(4))) float f32x4;
typedef __attribute__((ext_vector_type(2))) float f32x2;
typedef __attribute__((ext_vector_type(4))) unsigned u32x4;
typedef __attribute__((ext_vector_type(2))) unsigned u32x2;
typedef unsigned long long u64;
#define LAS __attribute__((address_space(3)))
#define GAS __attribute__((address_space(1)))
#define DI __device__ __forceinline__
#define RLX_AGENT __ATOMIC_RELAXED, __HIP_MEMORY_SCOPE_AGENT
#define RLX_WG __ATOMIC_RELAXED, __HIP_MEMORY_SCOPE_WORKGROUP

constexpr int DM = 512, DFF = 1792;
constexpr unsigned WS_FLAGS = 0;
constexpr unsigned WS_FLAG0 = 4096, FLAG_STRIDE = 128;
constexpr unsigned WS_KV = 131072;
constexpr unsigned WS_H = WS_KV + 32u * 8u * 16384u;
constexpr unsigned WS_O = WS_H + 64u * 112u * 1024u;
constexpr unsigned WS_W = WS_O + 64u * 32u * 1024u;
constexpr unsigned WS_END = WS_W + 16u * 144u * 1024u;
constexpr unsigned WS_DIAG = WS_END;
constexpr int XB = 32768, BUFB = XB + 4096 + 2048 + 8192;
constexpr int L_GATE = 3 * BUFB;
constexpr int L_RT = L_GATE + 4096;
constexpr int L_MISC = 163840 - 2048;
constexpr int L_B1 = L_MISC + 272, L_B2 = L_MISC + 512 + 272, L_SYNC = L_MISC + 1024 + 272;
static_assert(L_RT + 10240 <= L_MISC, "lds");
enum { SY_READY = 0  , SY_KV = 8, SY_O = 9, SY_F = 10, SY_PASS = 11, SY_RED = 12, SY_N = 16 };
constexpr unsigned SPIN_CAP = 1u << 18;

#define MFMA(a, b, c) __builtin_amdgcn_mfma_f32_32x32x16_bf16((a), (b), (c), 0, 0, 0)
#define SBAR() __builtin_amdgcn_sched_barrier(0)
#define CFENCE() asm volatile("" ::: "memory")

#define STAMP(i) do {} while (0)

DI unsigned pack2(float lo, float hi) { f32x2 v = {lo, hi}; return __builtin_bit_cast(unsigned, __builtin_convertvector(v, bf16x2)); }
DI u32x4 pack8(const f32x16& a, int o, float s) {
    u32x4 r; r[0] = pack2(a[o] * s, a[o + 1] * s); r[1] = pack2(a[o + 2] * s, a[o + 3] * s); r[2] = pack2(a[o + 4] * s, a[o + 5] * s); r[3] = pack2(a[o + 6] * s, a[o + 7] * s); return r; }
DI u32x4 pack8(const f32x16& a, int o) {
    u32x4 r; r[0] = pack2(a[o], a[o + 1]); r[1] = pack2(a[o + 2], a[o + 3]); r[2] = pack2(a[o + 4], a[o + 5]); r[3] = pack2(a[o + 6], a[o + 7]); return r; }
DI float bfround(float v) { return (float)(__bf16)v; }
DI float bf2f(unsigned short u) { return __uint_as_float((unsigned)u << 16); }
DI bf16x8 ldsr(const LAS char* p) { return *(const LAS bf16x8*)p; }
DI bf16x8 ldtr(const LAS char* p0, const LAS char* p1) {
    s16x4 a = __builtin_amdgcn_ds_read_tr16_b64_v4i16((LAS s16x4*)p0), b = __builtin_amdgcn_ds_read_tr16_b64_v4i16((LAS s16x4*)p1);
    return __builtin_bit_cast(bf16x8, __builtin_shufflevector(a, b, 0, 1, 2, 3, 4, 5, 6, 7)); }
DI void glds16(unsigned voff, const void* sbase, unsigned lds) {
    unsigned keep; asm volatile("s_mov_b32 %0, m0\n\ts_mov_b32 m0, %3\n\ts_nop 0\n\tglobal_load_lds_dwordx4 %1, %2\n\ts_mov_b32 m0, %0" : "=&s"(keep) : "v"(voff), "s"(sbase), "s"(lds) : "memory"); }
DI void glds4(unsigned voff, const void* sbase, unsigned lds) {
    unsigned keep; asm volatile("s_mov_b32 %0, m0\n\ts_mov_b32 m0, %3\n\ts_nop 0\n\tglobal_load_lds_dword %1, %2\n\ts_mov_b32 m0, %0" : "=&s"(keep) : "v"(voff), "s"(sbase), "s"(lds) : "memory"); }
DI u32x4 ld1(__amdgpu_buffer_rsrc_t rs, unsigned voff, unsigned soff) { return __builtin_amdgcn_raw_buffer_load_b128(rs, voff, soff, 16); }
DI void st1(u32x4 v, __amdgpu_buffer_rsrc_t rs, unsigned voff, unsigned soff) { __builtin_amdgcn_raw_buffer_store_b128(v, rs, voff, soff, 16); }
DI void drain() { asm volatile("s_waitcnt vmcnt(0)" ::: "memory"); }
DI unsigned lds_arrive(LAS unsigned* w, int lane) {
    unsigned old = 0; if (lane == 0) old = __hip_atomic_fetch_add(w, 1u, RLX_WG); return __builtin_amdgcn_readfirstlane(old); }
DI void lds_wait_ge(LAS unsigned* w, unsigned want) {
    for (unsigned s = 0; s < (SPIN_CAP << 4); ++s) { if ((unsigned)__builtin_amdgcn_readfirstlane(__hip_atomic_load(w, RLX_WG)) >= want) break; __builtin_amdgcn_s_sleep(1); }
    CFENCE(); }
DI void poll_tags(const char* ws, unsigned flag_off, bool active, u64 tag) {
    for (unsigned s = 0; s < SPIN_CAP; ++s) {
        u64 v = tag; if (active) v = __hip_atomic_load((GAS u64*)(ws + flag_off), RLX_AGENT);
        if (__all(v == tag)) break; __builtin_amdgcn_s_sleep(1); }
    __builtin_amdgcn_fence(__ATOMIC_ACQUIRE, "wavefront"); CFENCE(); }
DI unsigned flag_at(int kind, int hd, int t) { return WS_FLAG0 + (unsigned)(kind * 256 + hd * 8 + t) * FLAG_STRIDE; }
DI float gate_of(const LAS char* lds, int i) {
    const float z = bfround(*(const LAS float*)(lds + L_MISC + (i >> 6) * 512 + 16 + (i & 63) * 4));
    return bfround(__builtin_amdgcn_rcpf(1.f + __builtin_amdgcn_exp2f(-1.4426950408889634f * z))); }
DI f32x16 exp16(f32x16 s) {
#pragma unroll
    for (int i = 0; i < 16; ++i) s[i] = __builtin_amdgcn_exp2f(s[i]);
    return s; }
DI f32x16 pv(bf16x8 v0, bf16x8 v1, const f32x16& p, f32x16 o, int idx) {
    const f32x8 lo = __builtin_shufflevector(p, p, 0, 1, 2, 3, 4, 5, 6, 7), hi = __builtin_shufflevector(p, p, 8, 9, 10, 11, 12, 13, 14, 15);
    o = __builtin_amdgcn_smfmac_f32_32x32x32_bf16(v0, __builtin_bit_cast(bf16x16, lo), o, idx, 0, 0);
    o = __builtin_amdgcn_smfmac_f32_32x32x32_bf16(v1, __builtin_bit_cast(bf16x16, hi), o, idx, 0, 0);
    return o; }
DI float rowmax16(const f32x16& s) {
    float m = fmaxf(fmaxf(s[0], s[1]), s[2]);
#pragma unroll
    for (int i = 3; i < 15; i += 2) m = fmaxf(fmaxf(m, s[i]), s[i + 1]);
    return fmaxf(m, s[15]); }
DI float xhalf_max(float m) { auto r = __builtin_amdgcn_permlane32_swap(__float_as_uint(m), __float_as_uint(m), false, false); return fmaxf(__uint_as_float(r[0]), __uint_as_float(r[1])); }

__global__ __launch_bounds__(768, 3) void gated_mix_fused(const unsigned short* __restrict__ x, const unsigned short* __restrict__ wqkv, const unsigned short* __restrict__ wo,
                                                         const unsigned short* __restrict__ w1, const unsigned short* __restrict__ b1, const unsigned short* __restrict__ w2,
                                                         const unsigned short* __restrict__ b2, const unsigned short* __restrict__ gate_w, unsigned short* __restrict__ out, char* ws) {
    __shared__ __attribute__((aligned(1024))) char smem[163840];
    LAS char* lds = (LAS char*)smem;
    const unsigned lds0 = (unsigned)(uintptr_t)lds;
    const int lane = threadIdx.x & 63, wid = __builtin_amdgcn_readfirstlane(threadIdx.x >> 6), r32 = lane & 31, hh = lane >> 5;
    const int bx = blockIdx.x, hd = 8 * (bx & 3) + ((bx >> 3) & 7), t = 4 * ((bx >> 2) & 1) + (bx >> 6);
    const __amdgpu_buffer_rsrc_t rs = __builtin_amdgcn_make_buffer_rsrc(ws, 0, WS_END, 0x00020000);
    LAS unsigned* sy = (LAS unsigned*)(lds + L_SYNC);
    const u64 tag = *(const u64*)(ws + WS_FLAGS) + 1ull;
    const bool isA = wid < 8;
    const int f = wid - 8, cp = hd & 7, rq = hd >> 3;
    const int nun = hd < 24 ? 2 : 1, un0 = hd < 24 ? 2 * hd : 24 + hd;
    const int g4 = lane >> 4, cb = g4 & 1, q4 = (lane & 15) >> 2, p4 = lane & 3;
    STAMP(0);

    const int wtr = (8 * hh + q4) * 64 + cb * 32 + p4 * 8;
    f32x16 acc[4];
#pragma unroll
    for (int i = 0; i < 4; ++i) acc[i] = (f32x16){};
#define CHUNK_SYNC(c) do { if ((c) < 7) asm volatile("s_waitcnt vmcnt(4) lgkmcnt(0)\n\ts_barrier" ::: "memory"); else asm volatile("s_waitcnt vmcnt(0) lgkmcnt(0)\n\ts_barrier" ::: "memory"); } while (0)

    if (isA) {
        const int pr = (r32 & ~12) | ((r32 & 4) << 1) | ((r32 & 8) >> 1);
        { const int c0 = (lane & 7) ^ (lane >> 4);
          const unsigned dvo0 = (lane >> 3) * 1024 + c0 * 16, dvo1 = (lane >> 3) * 1024 + (c0 ^ 4) * 16;
          const char* dbase = (const char*)x + (size_t)(256 * t + 32 * wid) * 1024;
#define DMA_X(c) do { _Pragma("unroll") for (int i = 0; i < 4; ++i) glds16((i & 1) ? dvo1 : dvo0, dbase + (c) * 128 + i * 8192, lds0 + ((c) % 3) * BUFB + wid * 4096 + i * 1024); } while (0)
          unsigned gv; asm volatile("global_load_ushort %0, %1, %2" : "=v"(gv) : "v"((wid * 64 + lane) * 2), "s"(gate_w) : "memory");
          DMA_X(0); DMA_X(1);
          if (wid == 0) {
              if (lane < SY_N) sy[lane] = 0;
              if (lane < 4) *(LAS u32x4*)(lds + L_MISC + lane * 512) = (u32x4){0x3F803F80u, 0x3F803F80u, 0x3F803F80u, 0x3F803F80u};
          }
          asm volatile("s_waitcnt vmcnt(8)" : "+v"(gv) :: "memory");
          *(LAS unsigned short*)(lds + L_GATE + (wid * 64 + lane) * 8) = (unsigned short)gv;
          const int xo = (32 * wid + pr) * 128, xsw = hh ^ ((pr >> 1) & 7);
          const int vst = cb ? 8 : 32;
#pragma unroll
          for (int c = 0; c < 8; ++c) {
              CHUNK_SYNC(c);
              if (c + 2 < 8) DMA_X(c + 2);
              const LAS char* buf = lds + (c % 3) * BUFB;
              const LAS char* vb = cb ? lds + L_GATE + (64 * c + 8 * hh + q4) * 8 : buf + XB + 4096 + (8 * hh + q4) * 32 + p4 * 8;
#pragma unroll
              for (int ks = 0; ks < 4; ++ks) {
                  const bf16x8 xf = ldsr(buf + xo + ((xsw ^ (2 * ks)) * 16));
                  const bf16x8 wq = ldtr(buf + XB + wtr + ks * 1024, buf + XB + wtr + ks * 1024 + 256);
                  const bf16x8 wv = ldtr(vb + vst * (16 * ks), vb + vst * (16 * ks + 4));
                  acc[0] = MFMA(wq, xf, acc[0]);
                  acc[1] = MFMA(xf, wv, acc[1]);
              }
          }
#undef DMA_X
        }
        STAMP(1);
        asm volatile("" :: "s"(tag));
        const bf16x8 qf = __builtin_bit_cast(bf16x8, pack8(acc[0], 0, 0.25f * 1.4426950408889634f));
        const unsigned kvg = WS_KV + (unsigned)(hd * 8 + t) * 16384u;
        { const u32x4 kk = pack8(acc[0], 8); const int o = wid * 1024 + hh * 512 + pr * 16;
          *(LAS u32x4*)(lds + o) = kk; st1(kk, rs, o, kvg);
          if (r32 < 16) {
#pragma unroll
              for (int s = 0; s < 2; ++s) { const u32x4 vv = pack8(acc[1], 8 * s); const int ov = 8192 + wid * 1024 + s * 512 + hh * 256 + r32 * 16; *(LAS u32x4*)(lds + ov) = vv; st1(vv, rs, ov, kvg); }
          } else if (r32 == 16) {
#pragma unroll
              for (int s = 0; s < 2; ++s) { const int i = 32 * wid + 16 * s + 8 * hh; LAS f32x4* d = (LAS f32x4*)(lds + L_MISC + (i >> 6) * 512 + 16 + (i & 63) * 4);
                  d[0] = (f32x4){acc[1][8 * s], acc[1][8 * s + 1], acc[1][8 * s + 2], acc[1][8 * s + 3]}; d[1] = (f32x4){acc[1][8 * s + 4], acc[1][8 * s + 5], acc[1][8 * s + 6], acc[1][8 * s + 7]}; }
          } }
        asm volatile("s_waitcnt lgkmcnt(0)" ::: "memory"); __builtin_amdgcn_s_barrier(); CFENCE();
        STAMP(2);
        const int idx = 0xDDDD;
        const LAS char* kp = lds + lane * 16;
        const bool vl = r32 < 16;
        const int vlane = vl ? 8192 + hh * 256 + r32 * 16 : L_MISC, vmul = vl ? 1 : 0;
        f32x16 o = (f32x16){}, negm, sA, sB;
        { const f32x16 s0 = MFMA(ldsr(kp), qf, (f32x16){}); const float m0 = -xhalf_max(rowmax16(s0));
#pragma unroll
          for (int i = 0; i < 16; ++i) negm[i] = m0; }
        sA = MFMA(ldsr(kp), qf, negm);
#define PAIR(OFF, NXT) do { const LAS char* vp_ = lds + vlane + vmul * (OFF); \
            sB = MFMA(ldsr(kp + (OFF) + 1024), qf, negm); \
            { const bf16x8 v0 = ldsr(vp_), v1 = ldsr(vp_ + 512); sA = exp16(sA); o = pv(v0, v1, sA, o, idx); } \
            sA = MFMA(ldsr(kp + (NXT)), qf, negm); \
            { const bf16x8 v0 = ldsr(vp_ + 1024), v1 = ldsr(vp_ + 1536); sB = exp16(sB); o = pv(v0, v1, sB, o, idx); } } while (0)
        PAIR(0, 2048);
        drain();
        if (lds_arrive(sy + SY_KV, lane) == 7 && lane == 0) __hip_atomic_store((GAS u64*)(ws + flag_at(0, hd, t)), tag, RLX_AGENT);
        PAIR(2048, 4096);
        u32x4 stg[16];
        if (wid > 0) {
            const int tc = (t + wid) & 7;
            poll_tags(ws, flag_at(0, hd, tc), lane == 0, tag);
#pragma unroll
            for (int i = 0; i < 16; ++i) stg[i] = ld1(rs, lane * 16 + i * 1024, WS_KV + (unsigned)(hd * 8 + tc) * 16384u);
        }
        PAIR(4096, 6144);
        if (wid > 0) {
#pragma unroll
            for (int i = 0; i < 16; ++i) *(LAS u32x4*)(lds + wid * 16384 + i * 1024 + lane * 16) = stg[i];
            asm volatile("s_waitcnt lgkmcnt(0)" ::: "memory");
            if (lane == 0) __hip_atomic_store(sy + SY_READY + wid, 1u, RLX_WG);
        }
        lds_wait_ge(sy + SY_READY + 1, 1);
        PAIR(6144, 16384);
        STAMP(3);
        for (int j = 1; j < 8; ++j) {
            const int sb = j * 16384;
            PAIR(sb, sb + 2048); PAIR(sb + 2048, sb + 4096); PAIR(sb + 4096, sb + 6144);
            if (j < 7) lds_wait_ge(sy + SY_READY + j + 1, 1);
            PAIR(sb + 6144, j < 7 ? sb + 16384 : sb);
            if (j == 3) { asm volatile("s_waitcnt lgkmcnt(0)" ::: "memory"); lds_arrive(sy + SY_PASS, lane); }
        }
#undef PAIR
        STAMP(4);
        { float chk = o[8] * 0.f;
#pragma unroll
          for (int i = 0; i < 8; ++i) chk = fmaf(o[i], 0.f, chk);
          if (__builtin_expect(__any(!(chk == 0.f) || o[8] > 1.8e19f), 0)) {
              float m = -INFINITY;
              for (int tl = 0; tl < 64; ++tl) { const bf16x8 kf = __builtin_bit_cast(bf16x8, ld1(rs, lane * 16, WS_KV + (unsigned)(hd * 8 + (tl >> 3)) * 16384u + (tl & 7) * 1024));
                  m = fmaxf(m, rowmax16(MFMA(kf, qf, (f32x16){}))); }
              m = -xhalf_max(m);
#pragma unroll
              for (int i = 0; i < 16; ++i) negm[i] = m;
              o = (f32x16){};
              const u32x4 ones = {0x3F803F80u, 0x3F803F80u, 0x3F803F80u, 0x3F803F80u};
              for (int tl = 0; tl < 64; ++tl) { const unsigned so = WS_KV + (unsigned)(hd * 8 + (tl >> 3)) * 16384u + (tl & 7) * 1024;
                  const bf16x8 kf = __builtin_bit_cast(bf16x8, ld1(rs, lane * 16, so));
                  const u32x4 a0 = ld1(rs, 8192 + hh * 256 + (r32 & 15) * 16, so), a1 = ld1(rs, 8192 + 512 + hh * 256 + (r32 & 15) * 16, so);
                  o = pv(__builtin_bit_cast(bf16x8, vl ? a0 : ones), __builtin_bit_cast(bf16x8, vl ? a1 : ones), exp16(MFMA(kf, qf, negm)), o, idx); }
          } }
        const float sc = gate_of(lds, 32 * wid + pr) * __builtin_amdgcn_rcpf(o[8]);
        st1(pack8(o, 0, sc), rs, (hh * 32 + pr) * 16, WS_O + (unsigned)((8 * t + wid) * 32 + hd) * 1024u);
        drain();
        if (lds_arrive(sy + SY_O, lane) == 7 && lane == 0) __hip_atomic_store((GAS u64*)(ws + flag_at(2, hd, t)), tag, RLX_AGENT);
        STAMP(5);
    } else {
        const int i32 = rq + 4 * t;
        {
          const int u = i32 + 32 * f; const char* src = u < 112 ? (const char*)w2 + (size_t)u * 16384 : (const char*)wo + (size_t)(u - 112) * 16384;
          const unsigned vo = (lane >> 3) * 1024 + cp * 128 + (lane & 7) * 16;
          glds16(vo, src, lds0 + L_RT + f * 2048); glds16(vo, src + 8192, lds0 + L_RT + f * 2048 + 1024);
          if (f == 0 && i32 < 16) { const char* s2 = (const char*)wo + (size_t)(i32 + 16) * 16384; glds16(vo, s2, lds0 + L_RT + 8192); glds16(vo, s2 + 8192, lds0 + L_RT + 8192 + 1024); }
          if (f == 1) { if (lane < 16 * nun) glds4(lane * 4, (const char*)b1 + un0 * 64, lds0 + L_B1); if (lane < 32) glds4(lane * 4, (const char*)b2 + cp * 128, lds0 + L_B2); }
          const unsigned pitch = f < 2 ? 3072 : 3584;
          const unsigned dvo = f == 0 ? (lane >> 2) * 3072 + (p4 >> 1) * 1024 + (p4 & 1) * 16 : f == 1 ? (lane >> 1) * 3072 + (lane & 1) * 16 : (lane >> 2) * 3584 + p4 * 16;
          const char* dbase = f == 0 ? (const char*)wqkv + hd * 32 : f == 1 ? (const char*)wqkv + 2048 + hd * 32 : (const char*)w1 + (un0 + (f - 2 < nun ? f - 2 : 0)) * 64;
          const unsigned ddst = lds0 + XB + (f == 0 ? 0 : f == 1 ? 4096 : 6144 + (f - 2) * 4096);
          const unsigned sA_ = f == 1 ? 32 * pitch : 16 * pitch, sB_ = f == 1 ? 0 : 32 * pitch, lB_ = f == 1 ? 0 : 2048;
#define DMA_W(c) do { _Pragma("unroll") for (int i = 0; i < 4; ++i) glds16(dvo, dbase + (c) * 64 * pitch + (i & 1) * sA_ + (i >> 1) * sB_, ddst + ((c) % 3) * BUFB + (i & 1) * 1024 + (i >> 1) * lB_); } while (0)
          DMA_W(0); DMA_W(1);
          const int xo = (64 * f + r32) * 128, xsw = hh ^ ((r32 >> 1) & 7);
#pragma unroll
          for (int c = 0; c < 8; ++c) {
              CHUNK_SYNC(c);
              if (c + 2 < 8) DMA_W(c + 2);
              const LAS char* buf = lds + (c % 3) * BUFB;
#pragma unroll
              for (int ks = 0; ks < 4; ++ks) {
                  const bf16x8 x0 = ldsr(buf + xo + ((xsw ^ (2 * ks)) * 16)), x1 = ldsr(buf + xo + 4096 + ((xsw ^ (2 * ks)) * 16));
                  const bf16x8 wa = ldtr(buf + XB + 6144 + wtr + ks * 1024, buf + XB + 6144 + wtr + ks * 1024 + 256);
                  const bf16x8 wb = ldtr(buf + XB + 10240 + wtr + ks * 1024, buf + XB + 10240 + wtr + ks * 1024 + 256);
                  acc[0] = MFMA(wa, x0, acc[0]); acc[1] = MFMA(wa, x1, acc[1]);
                  acc[2] = MFMA(wb, x0, acc[2]); acc[3] = MFMA(wb, x1, acc[3]);
              }
          }
#undef DMA_W
        }
        STAMP(1);
        asm volatile("" :: "s"(tag));
        asm volatile("s_waitcnt lgkmcnt(0)" ::: "memory"); __builtin_amdgcn_s_barrier(); CFENCE();
        STAMP(2);
        __builtin_amdgcn_s_setprio(2);
        const int rtr = (4 * hh + q4) * 128 + cb * 32 + p4 * 8;
        for (int n = f; n < ((f == 0 && i32 < 16) ? 5 : 4); n += 4) {
            const int u = i32 + 32 * n;
#pragma unroll
            for (int ct = 0; ct < 2; ++ct) { const LAS char* s = lds + L_RT + n * 2048 + rtr + ct * 64;
                st1(__builtin_bit_cast(u32x4, ldtr(s, s + 1024)), rs, lane * 16, WS_W + (unsigned)((2 * cp + ct) * 144 + u) * 1024u); }
        }
#pragma unroll
        for (int u = 0; u < 2; ++u) if (u < nun) {
            float bb[16];
#pragma unroll
            for (int g = 0; g < 4; ++g) { const u32x2 w = *(const LAS u32x2*)(lds + L_B1 + u * 64 + g * 16 + hh * 8);
                bb[4 * g] = __uint_as_float(w[0] << 16); bb[4 * g + 1] = __uint_as_float(w[0] & 0xffff0000u); bb[4 * g + 2] = __uint_as_float(w[1] << 16); bb[4 * g + 3] = __uint_as_float(w[1] & 0xffff0000u); }
#pragma unroll
            for (int rt = 0; rt < 2; ++rt) {
                f32x16 hv;
#pragma unroll
                for (int i = 0; i < 16; ++i) { const float v = acc[2 * u + rt][i] + bb[i];
                    const float a = v * fmaf(v * v, -2.f * 0.7978845608028654f * 1.4426950408889634f * 0.044715f, -2.f * 0.7978845608028654f * 1.4426950408889634f);
                    hv[i] = v * __builtin_amdgcn_rcpf(1.f + __builtin_amdgcn_exp2f(a)); }
#pragma unroll
                for (int s = 0; s < 2; ++s) st1(pack8(hv, 8 * s), rs, lane * 16, WS_H + (unsigned)((8 * t + 2 * f + rt) * 112 + 2 * (un0 + u) + s) * 1024u);
            }
        }
        drain();
        if (lds_arrive(sy + SY_F, lane) == 3 && lane == 0) __hip_atomic_store((GAS u64*)(ws + flag_at(1, hd, t)), tag, RLX_AGENT);
        STAMP(3);
        poll_tags(ws, hh ? flag_at(1, cp + 8 * (r32 & 3), r32 >> 2) : flag_at(1, r32, t), true, tag);
        STAMP(4);
#pragma unroll
        for (int i = 0; i < 4; ++i) acc[i] = (f32x16){};
        const unsigned hb = WS_H + (unsigned)((8 * t + 2 * rq) * 112 + 28 * f) * 1024u, wb = WS_W + (unsigned)(2 * cp * 144 + 28 * f) * 1024u;
        u32x4 fr[4][4];
#define LDK(slot, n) do { fr[slot][0] = ld1(rs, lane * 16, wb + (n) * 1024); fr[slot][1] = ld1(rs, lane * 16, wb + (144 + (n)) * 1024); \
                          fr[slot][2] = ld1(rs, lane * 16, hb + (n) * 1024); fr[slot][3] = ld1(rs, lane * 16, hb + (112 + (n)) * 1024); } while (0)
#define BC(v) __builtin_bit_cast(bf16x8, v)
#pragma unroll
        for (int n = 0; n < 4; ++n) LDK(n, n);
#pragma unroll
        for (int n = 0; n < 28; ++n) {
            const u32x4 w0 = fr[n & 3][0], w1_ = fr[n & 3][1], h0 = fr[n & 3][2], h1 = fr[n & 3][3];
            acc[0] = MFMA(BC(w0), BC(h0), acc[0]); acc[1] = MFMA(BC(w0), BC(h1), acc[1]); acc[2] = MFMA(BC(w1_), BC(h0), acc[2]); acc[3] = MFMA(BC(w1_), BC(h1), acc[3]);
            SBAR();
            if (n + 4 < 28) LDK(n & 3, n + 4);
            SBAR();
        }
#undef LDK
        STAMP(5);
        { const float og0 = bfround(1.f - gate_of(lds, 64 * rq + r32)), og1 = bfround(1.f - gate_of(lds, 64 * rq + 32 + r32));
#pragma unroll
          for (int ct = 0; ct < 2; ++ct)
#pragma unroll
              for (int g = 0; g < 4; ++g) { u32x2 w = *(const LAS u32x2*)(lds + L_B2 + ct * 64 + g * 16 + hh * 8); if (f != 0) w = (u32x2){0u, 0u};
                  const float bb[4] = {__uint_as_float(w[0] << 16), __uint_as_float(w[0] & 0xffff0000u), __uint_as_float(w[1] << 16), __uint_as_float(w[1] & 0xffff0000u)};
#pragma unroll
                  for (int i = 0; i < 4; ++i) { acc[2 * ct][4 * g + i] = (acc[2 * ct][4 * g + i] + bb[i]) * og0; acc[2 * ct + 1][4 * g + i] = (acc[2 * ct + 1][4 * g + i] + bb[i]) * og1; } } }
        { u32x4 tmp[16];
#pragma unroll
          for (int i = 0; i < 16; ++i) tmp[i] = ld1(rs, lane * 16, WS_W + (unsigned)((2 * cp + (i & 1)) * 144 + 112 + 8 * f + (i >> 1)) * 1024u);
          lds_wait_ge(sy + SY_PASS, 8);
#pragma unroll
          for (int i = 0; i < 16; ++i) *(LAS u32x4*)(lds + f * 16384 + i * 1024 + lane * 16) = tmp[i]; }
        STAMP(6);
        poll_tags(ws, flag_at(2, 8 * f + (lane & 7), t), lane < 8, tag);
        STAMP(7);
        { u32x4 of[16];
#pragma unroll
          for (int i = 0; i < 16; ++i) of[i] = ld1(rs, lane * 16, WS_O + (unsigned)((8 * t + 2 * rq + (i & 1)) * 32 + 8 * f + (i >> 1)) * 1024u);
#pragma unroll
          for (int n = 0; n < 8; ++n) { const bf16x8 w0 = ldsr(lds + f * 16384 + n * 2048 + lane * 16), w1_ = ldsr(lds + f * 16384 + n * 2048 + 1024 + lane * 16);
              acc[0] = MFMA(w0, BC(of[2 * n]), acc[0]); acc[1] = MFMA(w0, BC(of[2 * n + 1]), acc[1]); acc[2] = MFMA(w1_, BC(of[2 * n]), acc[2]); acc[3] = MFMA(w1_, BC(of[2 * n + 1]), acc[3]); } }
#undef BC
        STAMP(8);
#pragma unroll
        for (int qd = 0; qd < 4; ++qd)
#pragma unroll
            for (int g = 0; g < 4; ++g) *(LAS f32x4*)(lds + f * 16384 + qd * 4096 + g * 1024 + lane * 16) = (f32x4){acc[qd][4 * g], acc[qd][4 * g + 1], acc[qd][4 * g + 2], acc[qd][4 * g + 3]};
        asm volatile("s_waitcnt lgkmcnt(0)" ::: "memory");
        lds_arrive(sy + SY_RED, lane);
        lds_wait_ge(sy + SY_RED, 4);
        unsigned pk[8];
#pragma unroll
        for (int g = 0; g < 4; ++g) { f32x4 s = *(const LAS f32x4*)(lds + f * 4096 + g * 1024 + lane * 16);
#pragma unroll
            for (int w = 1; w < 4; ++w) s += *(const LAS f32x4*)(lds + w * 16384 + f * 4096 + g * 1024 + lane * 16);
            pk[2 * g] = pack2(s[0], s[1]); pk[2 * g + 1] = pack2(s[2], s[3]); }
        unsigned short* orow = out + (size_t)(256 * t + 64 * rq + 32 * (f & 1) + r32) * DM + 64 * cp + 32 * (f >> 1) + 8 * hh;
#pragma unroll
        for (int k = 0; k < 2; ++k) {
            auto a = __builtin_amdgcn_permlane32_swap(pk[4 * k], pk[4 * k + 2], false, false), b = __builtin_amdgcn_permlane32_swap(pk[4 * k + 1], pk[4 * k + 3], false, false);
            *(u32x4*)(orow + 16 * k) = (u32x4){a[0], b[0], a[1], b[1]};
        }
        if (bx == 0 && f == 0 && lane == 0) *(GAS u64*)(ws + WS_FLAGS) = tag;
        STAMP(9);
    }
}

extern "C" void kernel_launch(void* const* d_in, const int* in_sizes, int n_in, void* d_out, int out_size, void* d_ws, size_t ws_size, hipStream_t stream) {
    (void)in_sizes; (void)n_in; (void)out_size; (void)ws_size;
    hipLaunchKernelGGL(gated_mix_fused, dim3(256), dim3(768), 0, stream, (const unsigned short*)d_in[0], (const unsigned short*)d_in[1], (const unsigned short*)d_in[2],
                       (const unsigned short*)d_in[4], (const unsigned short*)d_in[5], (const unsigned short*)d_in[6], (const unsigned short*)d_in[7], (const unsigned short*)d_in[8],
                       (unsigned short*)d_out, (char*)d_ws);
}
```

```cpp
#include <hip/hip_runtime.h>
#include <hip/hip_bf16.h>
#include <stdint.h>

typedef __attribute__((ext_vector_type(8))) __bf16 bf16x8;
typedef __attribute__((ext_vector_type(16))) __bf16 bf16x16;
typedef __attribute__((ext_vector_type(2))) __bf16 bf16x2;
typedef __attribute__((ext_vector_type(4))) short s16x4;
typedef __attribute__((ext_vector_type(16))) float f32x16;
typedef __attribute__((ext_vector_type(8))) float f32x8;
typedef __attribute__((ext_vector_type(4))) float f32x4;
typedef __attribute__((ext_vector_type(2))) float f32x2;
typedef __attribute__((ext_vector_type(4))) unsigned u32x4;
typedef __attribute__((ext_vector_type(2))) unsigned u32x2;
typedef unsigned long long u64;
#define LAS __attribute__((address_space(3)))
#define GAS __attribute__((address_space(1)))
#if defined(__HIP_DEVICE_COMPILE__) && !defined(ALLOW_PK)
#define NOPK __attribute__((target("no-packed-fp32-ops")))
#else
#define NOPK
#endif
#define DI __device__ __forceinline__ NOPK
#define U2F(x) __builtin_bit_cast(float, (unsigned)(x))
#define F2U(x) __builtin_bit_cast(unsigned, (float)(x))
#define RLX_AGENT __ATOMIC_RELAXED, __HIP_MEMORY_SCOPE_AGENT
#define RLX_WG __ATOMIC_RELAXED, __HIP_MEMORY_SCOPE_WORKGROUP

constexpr int DM = 512, DFF = 1792;
constexpr unsigned WS_FLAGS = 0;
constexpr unsigned WS_FLAG0 = 4096, FLAG_STRIDE = 128;
constexpr unsigned WS_G = 106496;
constexpr unsigned WS_KV = 131072;
constexpr unsigned WS_H = WS_KV + 32u * 8u * 16384u;
constexpr unsigned WS_O = WS_H + 64u * 112u * 1024u;
constexpr unsigned WS_W = WS_O + 64u * 32u * 1024u;
constexpr unsigned WS_END = WS_W + 16u * 144u * 1024u;
constexpr unsigned WS_DIAG = WS_END;
constexpr int XB = 32768, BUFB = XB + 6144 + 8192;
constexpr int L_V = 65536;
constexpr int L_GATE = 3 * BUFB;
constexpr int L_RT = L_GATE + 4096;
constexpr int L_MISC = 163840 - 2048;
constexpr int L_DUMMY = L_RT + 10240;
constexpr int L_B1 = L_MISC + 272, L_B2 = L_MISC + 512 + 272, L_SYNC = L_MISC + 1024 + 272;
static_assert(L_DUMMY + 1024 <= L_MISC, "lds");
enum { SY_READY = 0  , SY_KV = 8, SY_DONE = 9, SY_F = 10, SY_PASS = 11, SY_RED = 12, SY_TRIG = 13, SY_FSEEN = 14, SY_AW = 15, SY_OL = 16  , SY_FA = 20, SY_PROG = 24  , SY_N = 32 };
constexpr unsigned SENT = 0xFFFFFFFFu;
constexpr unsigned SPIN_CAP = 1u << 18;

#define MFMA(a, b, c) __builtin_amdgcn_mfma_f32_32x32x16_bf16((a), (b), (c), 0, 0, 0)
#define MFMA16(a, b, c) __builtin_amdgcn_mfma_f32_16x16x32_bf16((a), (b), (c), 0, 0, 0)
#define SBAR() __builtin_amdgcn_sched_barrier(0)
#define CFENCE() asm volatile("" ::: "memory")
#define PIN(x) asm volatile("" : "+v"(x))

#define F2_PRIO 0
#define DSPLIT 4
#define FPA 0
#define TPF 0
#define TSLP 8
#define TLA 4
#define RTC 3
#define LEAP 1
#define PA_F 1
#define NP1 20
#define TRIGJ 5
#define STAMP(i) do {} while (0)
#define STAMP_FLUSH() do {} while (0)

DI unsigned pack2(float lo, float hi) { f32x2 v = {lo, hi}; return __builtin_bit_cast(unsigned, __builtin_convertvector(v, bf16x2)); }
DI u32x4 pack8(const f32x16& a, int o, float s) {
    u32x4 r; r[0] = pack2(a[o] * s, a[o + 1] * s); r[1] = pack2(a[o + 2] * s, a[o + 3] * s); r[2] = pack2(a[o + 4] * s, a[o + 5] * s); r[3] = pack2(a[o + 6] * s, a[o + 7] * s); return r; }
DI u32x4 pack8(const f32x16& a, int o) {
    u32x4 r; r[0] = pack2(a[o], a[o + 1]); r[1] = pack2(a[o + 2], a[o + 3]); r[2] = pack2(a[o + 4], a[o + 5]); r[3] = pack2(a[o + 6], a[o + 7]); return r; }
DI float bfround(float v) { return (float)(__bf16)v; }
DI float bf2f(unsigned short u) { return U2F((unsigned)u << 16); }
DI bf16x8 ldsr(const LAS char* p) { return *(const LAS bf16x8*)p; }
DI bf16x8 ldtr(const LAS char* p0, const LAS char* p1) {
    s16x4 a = __builtin_amdgcn_ds_read_tr16_b64_v4i16((LAS s16x4*)p0), b = __builtin_amdgcn_ds_read_tr16_b64_v4i16((LAS s16x4*)p1);
    return __builtin_bit_cast(bf16x8, __builtin_shufflevector(a, b, 0, 1, 2, 3, 4, 5, 6, 7)); }
DI void glds16(unsigned voff, const void* sbase, unsigned lds) {
    unsigned keep; asm volatile("s_mov_b32 %0, m0\n\ts_mov_b32 m0, %3\n\ts_nop 0\n\tglobal_load_lds_dwordx4 %1, %2\n\ts_mov_b32 m0, %0" : "=&s"(keep) : "v"(voff), "s"(sbase), "s"(lds) : "memory"); }
DI void glds4(unsigned voff, const void* sbase, unsigned lds) {
    unsigned keep; asm volatile("s_mov_b32 %0, m0\n\ts_mov_b32 m0, %3\n\ts_nop 0\n\tglobal_load_lds_dword %1, %2\n\ts_mov_b32 m0, %0" : "=&s"(keep) : "v"(voff), "s"(sbase), "s"(lds) : "memory"); }
DI u32x4 ld1(__amdgpu_buffer_rsrc_t rs, unsigned voff, unsigned soff) { return __builtin_amdgcn_raw_buffer_load_b128(rs, voff, soff, 16); }
DI void st1(u32x4 v, __amdgpu_buffer_rsrc_t rs, unsigned voff, unsigned soff) {
    __builtin_amdgcn_raw_buffer_store_b128(v, rs, voff, soff, 16); asm volatile("s_nop 1" :: "v"(v) : "memory"); }
DI void drain() { asm volatile("s_waitcnt vmcnt(0)" ::: "memory"); }
DI unsigned lds_arrive(LAS unsigned* w, int lane) {
    unsigned old = 0; if (lane == 0) old = __hip_atomic_fetch_add(w, 1u, RLX_WG); return __builtin_amdgcn_readfirstlane(old); }
template <int SL = 1> DI void lds_wait_ge(LAS unsigned* w, unsigned want) {
#pragma nounroll
    for (unsigned s = 0; s < (SPIN_CAP << 4); ++s) { if ((unsigned)__builtin_amdgcn_readfirstlane(__hip_atomic_load(w, RLX_WG)) >= want) break; __builtin_amdgcn_s_sleep(SL); }
    CFENCE(); }
template <int SL = 1> DI void poll_tags(const char* ws, unsigned flag_off, bool active, u64 tag) {
#pragma nounroll
    for (unsigned s = 0; s < SPIN_CAP; ++s) {
        u64 v = tag; if (active) v = __hip_atomic_load((GAS u64*)(ws + flag_off), RLX_AGENT);
        if (__builtin_amdgcn_ballot_w64(v != tag) == 0) break; __builtin_amdgcn_s_sleep(SL); }
    __builtin_amdgcn_fence(__ATOMIC_ACQUIRE, "wavefront"); CFENCE(); }
DI unsigned flag_at(int kind, int hd, int t) { return WS_FLAG0 + (unsigned)(kind * 256 + hd * 8 + t) * FLAG_STRIDE; }
DI int g_at(int i) { return L_MISC + (i >> 6) * 512 + 16 + (i & 63) * 4; }
DI u32x4 join16(const f32x4& a, const f32x4& b, float sa, float sb) {
    auto r0 = __builtin_amdgcn_permlane16_swap(pack2(a[0] * sa, a[1] * sa), pack2(b[0] * sb, b[1] * sb), false, false);
    auto r1 = __builtin_amdgcn_permlane16_swap(pack2(a[2] * sa, a[3] * sa), pack2(b[2] * sb, b[3] * sb), false, false);
    return (u32x4){r0[0], r1[0], r0[1], r1[1]}; }
DI f32x16 exp16(f32x16 s) {
#pragma unroll
    for (int i = 0; i < 16; ++i) s[i] = __builtin_amdgcn_exp2f(s[i]);
    return s; }
DI f32x16 pv(bf16x8 v0, bf16x8 v1, const f32x16& p, f32x16 o, int idx) {
    const f32x8 lo = __builtin_shufflevector(p, p, 0, 1, 2, 3, 4, 5, 6, 7), hi = __builtin_shufflevector(p, p, 8, 9, 10, 11, 12, 13, 14, 15);
    o = __builtin_amdgcn_smfmac_f32_32x32x32_bf16(v0, __builtin_bit_cast(bf16x16, lo), o, idx, 0, 0);
    o = __builtin_amdgcn_smfmac_f32_32x32x32_bf16(v1, __builtin_bit_cast(bf16x16, hi), o, idx, 0, 0);
    return o; }
DI float rowmax16(const f32x16& s) {
    float m = __builtin_fmaxf(__builtin_fmaxf(s[0], s[1]), s[2]);
#pragma unroll
    for (int i = 3; i < 15; i += 2) m = __builtin_fmaxf(__builtin_fmaxf(m, s[i]), s[i + 1]);
    return __builtin_fmaxf(m, s[15]); }
DI float xhalf_max(float m) { auto r = __builtin_amdgcn_permlane32_swap(F2U(m), F2U(m), false, false); return __builtin_fmaxf(U2F(r[0]), U2F(r[1])); }

__global__ __launch_bounds__(768, 3) NOPK void gated_mix_fused(const unsigned short* __restrict__ x, const unsigned short* __restrict__ wqkv, const unsigned short* __restrict__ wo,
                                                         const unsigned short* __restrict__ w1, const unsigned short* __restrict__ b1, const unsigned short* __restrict__ w2,
                                                         const unsigned short* __restrict__ b2, const unsigned short* __restrict__ gate_w, unsigned short* __restrict__ out, char* ws) {
    __shared__ __attribute__((aligned(1024))) char smem[163840];
    LAS char* lds = (LAS char*)smem;
    const unsigned lds0 = (unsigned)(uintptr_t)lds;
    const int lane = __builtin_amdgcn_workitem_id_x() & 63, wid = __builtin_amdgcn_readfirstlane(__builtin_amdgcn_workitem_id_x() >> 6), r32 = lane & 31, hh = lane >> 5;
    const int bx = __builtin_amdgcn_workgroup_id_x(), hd = 8 * (bx & 3) + ((bx >> 3) & 7), t = 4 * ((bx >> 2) & 1) + (bx >> 6);
    const __amdgpu_buffer_rsrc_t rs = __builtin_amdgcn_make_buffer_rsrc(ws, 0, WS_END, 0x00020000);
    LAS unsigned* sy = (LAS unsigned*)(lds + L_SYNC);
    asm volatile("" :: "s"(x), "s"(wqkv), "s"(wo), "s"(w1), "s"(b1), "s"(w2), "s"(b2), "s"(gate_w), "s"(out), "s"(ws));
    u64 tag; u32x2 ep;
#define EPOCH_LOAD() asm volatile("global_load_dwordx2 %0, %1, %2" : "=v"(ep) : "v"(0), "s"(ws) : "memory")
#define EPOCH_TAKE() do { PIN(ep); tag = (((u64)(unsigned)__builtin_amdgcn_readfirstlane(ep[1]) << 32) | (unsigned)__builtin_amdgcn_readfirstlane(ep[0])) + 1ull; } while (0)
    const int cp = hd & 7, rq = hd >> 3;
    const int g4 = lane >> 4, r16 = lane & 15, q4 = r16 >> 2, p4 = lane & 3;
    const int xsw = g4 ^ (r16 >> 1);
    STAMP(0);
#define BUFOF(c) ((((c) + 1) % 3) * BUFB)
#define CHUNK_SYNC(c, n) do { if ((c) < 7) asm volatile("s_waitcnt vmcnt(" #n ") lgkmcnt(0)\n\ts_barrier" ::: "memory"); else asm volatile("s_waitcnt vmcnt(0) lgkmcnt(0)\n\ts_barrier" ::: "memory"); } while (0)

    if (wid < 8) {
        f32x4 aq[2] = {}, ak[2] = {}, av[2] = {};
        { const int c0 = (lane & 7) ^ (lane >> 4), rho = lane >> 1, r8 = lane >> 3;
          const unsigned dvo0 = r8 * 1024 + c0 * 16, dvo1 = r8 * 1024 + (c0 ^ 4) * 16;
          const unsigned dvq = (8 * ((rho >> 2) & 3) + 4 * (rho >> 4) + (rho & 3)) * 3072 + (lane & 1) * 16;
          const unsigned dvf = (8 * (r8 >> 2) + (r8 & 3)) * 3584 + (((lane & 7) ^ (r8 & 6)) * 16);
          const char* xbase = (const char*)x + (size_t)(256 * t + 32 * wid) * 1024;
          const bool hq = wid < 6, hf = hd < 28;
          const char* qbase = (const char*)wqkv + hd * 32 + (hq ? (wid >> 1) * 1024 + (wid & 1) * 32 * 3072 : 0);
          const char* fbase = (const char*)w1 + (hf ? hd * 128 : 0) + (32 * (wid >> 2) + 16 * (wid & 1) + 4 * ((wid >> 1) & 1)) * 3584;
          const unsigned vq_ = hq ? dvq : 0u, vf_ = hf ? dvf : 0u;
#define DMA_X(c) do { _Pragma("unroll") for (int i = 0; i < 4; ++i) glds16((i & 1) ? dvo1 : dvo0, xbase + (c) * 128 + i * 8192, lds0 + BUFOF(c) + wid * 4096 + i * 1024); } while (0)
#define DMA_ALL(c) do { glds16(vq_, qbase + (c) * 64 * 3072, hq ? lds0 + BUFOF(c) + XB + wid * 1024 : lds0 + L_DUMMY); \
              glds16(vf_, fbase + (c) * 64 * 3584, hf ? lds0 + BUFOF(c) + XB + 6144 + wid * 1024 : lds0 + L_DUMMY); DMA_X(c); } while (0)
          DMA_ALL(0);
          EPOCH_LOAD();
          if (wid == 0) {
              if (lane < SY_N) sy[lane] = 0;
              if (lane < 8) { const unsigned one = lane < 4 ? 0x3F803F80u : 0u; *(LAS u32x4*)(lds + L_MISC + (lane & 3) * 512 + (lane >> 2) * 496) = (u32x4){one, one, one, one}; }
          }
          __builtin_amdgcn_s_sleep(4);
          DMA_ALL(1);
          STAMP(20);
          const int xo = (32 * wid + r16) * 128, wo_ = XB + (4 * g4 + q4) * 32 + p4 * 8;
          bf16x8 xa[2], xb[2], wq[2], wk[2], wv[2];
#define RX(d, c, ks, rt) d[ks] = ldsr(lds + BUFOF(c) + xo + (rt) * 2048 + ((xsw ^ (4 * (ks))) * 16))
#define RW(d, c, ks, im) d[ks] = ldtr(lds + BUFOF(c) + wo_ + (im) * 2048 + (ks) * 1024, lds + BUFOF(c) + wo_ + (im) * 2048 + (ks) * 1024 + 512)
#define M2(A, W, ks) do { A[0] = MFMA16(W[ks], xa[ks], A[0]); A[1] = MFMA16(W[ks], xb[ks], A[1]); PIN(A[0]); PIN(A[1]); SBAR(); } while (0)
#define MV(ks) do { av[0] = MFMA16(xa[ks], wv[ks], av[0]); av[1] = MFMA16(xb[ks], wv[ks], av[1]); PIN(av[0]); PIN(av[1]); SBAR(); } while (0)
#pragma unroll
          for (int c = 0; c < 8; ++c) {
              CHUNK_SYNC(c, 6);
              STAMP(12 + c);
              if (c == 7) { EPOCH_TAKE(); __builtin_amdgcn_s_setprio(3); }
              if (c + 2 < 8 && wid >= DSPLIT) DMA_ALL(c + 2);
              if (c > 0) M2(aq, wq, 1);
              RX(xa, c, 0, 0); RX(xb, c, 0, 1); RW(wq, c, 0, 0); SBAR();
              if (c > 0) M2(ak, wk, 1);
              RW(wk, c, 0, 1); RW(wv, c, 0, 2); SBAR();
              if (c > 0) MV(1);
              RX(xa, c, 1, 0); RX(xb, c, 1, 1); RW(wq, c, 1, 0); SBAR();
              M2(aq, wq, 0);
              RW(wk, c, 1, 1); RW(wv, c, 1, 2); SBAR();
              M2(ak, wk, 0);
              MV(0);
              if (c + 2 < 8 && wid < DSPLIT) DMA_ALL(c + 2);
          }
          M2(aq, wq, 1); M2(ak, wk, 1); MV(1);
#undef RX
#undef RW
#undef M2
#undef MV
#undef DMA_ALL
#undef DMA_X
        }
        STAMP(1);
        constexpr float QS = 0.25f * 1.4426950408889634f;
        const bf16x8 qf = __builtin_bit_cast(bf16x8, join16(aq[0], aq[1], QS, QS));
        const unsigned kvg = WS_KV + (unsigned)(hd * 8 + t) * 16384u, og = WS_O + (unsigned)((8 * t + wid) * 32 + hd) * 1024u;
        { const u32x4 kk = join16(ak[0], ak[1], 1.f, 1.f), vv = join16(av[0], av[1], 1.f, 1.f);
          const int ok = wid * 1024 + lane * 16, ov = wid * 1024 + (g4 & 1) * 512 + hh * 256 + r16 * 16;
          *(LAS u32x4*)(lds + ok) = kk; *(LAS u32x4*)(lds + L_V + ov) = vv;
          asm volatile("s_waitcnt lgkmcnt(0)" ::: "memory");
          if (lane == 0) __hip_atomic_fetch_add(sy + SY_AW, 1u, RLX_WG);
          st1(kk, rs, ok, kvg); st1(vv, rs, ov + 8192, kvg); }
        lds_wait_ge<1>(sy + SY_AW, 8);
        STAMP(2);
        drain();
        if (lds_arrive(sy + SY_KV, lane) == 7 && lane == 0) __hip_atomic_store((GAS u64*)(ws + flag_at(0, hd, t)), tag, RLX_AGENT);
        const int idx = 0xDDDD;
        const LAS char* kp = lds + lane * 16;
        const bool vl = r32 < 16;
        const LAS char* vp = lds + (vl ? L_V + 512 + hh * 256 + r32 * 16 : (r32 & 11) == 0 ? L_MISC : L_MISC + 496);
        const int vstep = vl ? 2048 : 0;
        f32x16 o = (f32x16){}, negm, sA, sB = (f32x16){};
        bf16x8 kf = ldsr(kp), v0 = ldsr(vl ? vp - 512 : vp), v1 = (bf16x8){};
        { const f32x16 s0 = MFMA(kf, qf, (f32x16){}); const float m0 = -xhalf_max(rowmax16(s0));
#pragma unroll
          for (int i = 0; i < 16; ++i) negm[i] = m0; }
        sA = MFMA(kf, qf, negm);
        kf = ldsr(kp + 1024);
        u32x4 stg[8];
        const unsigned rkv = WS_KV + (unsigned)(hd * 8 + ((t + wid) & 7)) * 16384u;
        if (wid > 0) {
            poll_tags(ws, flag_at(0, hd, (t + wid) & 7), lane == 0, tag);
#pragma unroll
            for (int i = 0; i < 8; ++i) stg[i] = ld1(rs, lane * 16 + i * 1024, rkv);
        }
        STAMP(10);
        __builtin_amdgcn_s_setprio(0);
#define EX(S, a, b) do { _Pragma("unroll") for (int i_ = (a); i_ < (b); ++i_) S[i_] = __builtin_amdgcn_exp2f(S[i_]); PIN(S); SBAR(); } while (0)
#define LO8(S) __builtin_bit_cast(bf16x16, __builtin_shufflevector(S, S, 0, 1, 2, 3, 4, 5, 6, 7))
#define HI8(S) __builtin_bit_cast(bf16x16, __builtin_shufflevector(S, S, 8, 9, 10, 11, 12, 13, 14, 15))
#define SM(a, b) do { o = __builtin_amdgcn_smfmac_f32_32x32x32_bf16(a, b, o, idx, 0, 0); PIN(o); SBAR(); } while (0)
#define TILE(CUR, OTH, KO, VO) do { SBAR(); EX(CUR, 0, 2); SM(v1, HI8(OTH)); v1 = ldsr(vp + (VO)); SBAR(); EX(CUR, 2, 6); \
            OTH = MFMA(kf, qf, negm); PIN(OTH); SBAR(); kf = ldsr(kp + (KO)); SBAR(); EX(CUR, 6, 10); SM(v0, LO8(CUR)); v0 = ldsr(vp + (VO) + 512); SBAR(); EX(CUR, 10, 16); } while (0)
#define PAIR(KO) do { TILE(sA, sB, (KO) + 2048, 0); TILE(sB, sA, (KO) + 3072, 1024); vp += vstep; } while (0)
        PAIR(0);
        if (wid > 0) {
            lds_wait_ge(sy + SY_FA, 4);
#pragma unroll
            for (int i = 0; i < 8; ++i) { *(LAS u32x4*)(lds + wid * 8192 + i * 1024 + lane * 16) = stg[i]; stg[i] = ld1(rs, lane * 16 + i * 1024, rkv + 8192); }
        }
        PAIR(2048);
        if (wid > 0) {
#pragma unroll
            for (int i = 0; i < 8; ++i) *(LAS u32x4*)(lds + L_V + wid * 8192 + i * 1024 + lane * 16) = stg[i];
            asm volatile("s_waitcnt lgkmcnt(0)" ::: "memory");
            if (lane == 0) __hip_atomic_store(sy + SY_READY + wid, 1u, RLX_WG);
        }
        PAIR(4096);
        lds_wait_ge(sy + SY_READY + 1, 1);
        PAIR(6144);
        STAMP(3);
        for (int j = 1; j < 8; ++j) {
            kp += 8192;
            if (j > 1 && j < 6) STAMP(4 + j);
            if (LEAP) {
                if (lane == 0) __hip_atomic_store(sy + SY_PROG + wid, (unsigned)j, RLX_WG);
                if ((unsigned)__builtin_amdgcn_readfirstlane(__hip_atomic_load(sy + SY_PROG + (wid ^ 4), RLX_WG)) >= (unsigned)j) __builtin_amdgcn_s_setprio(1); else __builtin_amdgcn_s_setprio(0);
            }
            PAIR(0); PAIR(2048); PAIR(4096);
            if (j < 7) lds_wait_ge(sy + SY_READY + j + 1, 1);
            PAIR(6144);
            if (j == 4 && lane == 0) __hip_atomic_fetch_add(sy + SY_PASS, 1u, RLX_WG);
            if (j == TRIGJ && lane == 0) __hip_atomic_fetch_add(sy + SY_TRIG, 1u, RLX_WG);
        }
        SM(v1, HI8(sB));
#undef PAIR
#undef TILE
#undef SM
#undef EX
        asm volatile("s_waitcnt lgkmcnt(0)" ::: "memory");
        if (lane == 0) __hip_atomic_fetch_add(sy + SY_DONE, 1u, RLX_WG);
        STAMP(4);
        { float chk = o[8] * 0.f;
#pragma unroll
          for (int i = 0; i < 8; ++i) chk = __builtin_fmaf(o[i], 0.f, chk);
          if (__builtin_expect(0 != __builtin_amdgcn_ballot_w64(!(chk == 0.f) || o[8] > 1.8e19f), 0)) {
              float m = -__builtin_inff();
#pragma nounroll
              for (int tl = 0; tl < 64; ++tl) { const bf16x8 kg = __builtin_bit_cast(bf16x8, ld1(rs, lane * 16, WS_KV + (unsigned)(hd * 8 + (tl >> 3)) * 16384u + (tl & 7) * 1024));
                  m = __builtin_fmaxf(m, rowmax16(MFMA(kg, qf, (f32x16){}))); }
              m = -xhalf_max(m);
#pragma unroll
              for (int i = 0; i < 16; ++i) negm[i] = m;
              o = (f32x16){};
              const u32x4 ones = {0x3F803F80u, 0x3F803F80u, 0x3F803F80u, 0x3F803F80u};
#pragma nounroll
              for (int tl = 0; tl < 64; ++tl) { const unsigned so = WS_KV + (unsigned)(hd * 8 + (tl >> 3)) * 16384u + (tl & 7) * 1024;
                  const bf16x8 kg = __builtin_bit_cast(bf16x8, ld1(rs, lane * 16, so));
                  const u32x4 a0 = ld1(rs, 8192 + hh * 256 + r16 * 16, so), a1 = ld1(rs, 8192 + 512 + hh * 256 + r16 * 16, so);
                  o = pv(__builtin_bit_cast(bf16x8, vl ? a0 : ones), __builtin_bit_cast(bf16x8, vl ? a1 : ones), exp16(MFMA(kg, qf, negm)), o, idx); }
          } }
        lds_wait_ge(sy + SY_FSEEN, 4);
        { u32x4 ov = pack8(o, 0, *(const LAS float*)(lds + g_at(32 * wid + r32)) * __builtin_amdgcn_rcpf(o[8]));
#pragma unroll
          for (int i = 0; i < 4; ++i) ov[i] = ov[i] == SENT ? 0x7FFF7FFFu : ov[i];
          st1(ov, rs, lane * 16, og); }
        STAMP(5);
        { u32x4 of[8];
          const unsigned ob = WS_O + (unsigned)((8 * t + 2 * rq) * 32 + 4 * wid) * 1024u;
#pragma nounroll
          for (unsigned s = 0; s < SPIN_CAP; ++s) {
#pragma unroll
              for (int i = 0; i < 8; ++i) of[i] = ld1(rs, lane * 16, ob + ((i & 1) * 32 + (i >> 1)) * 1024);
              unsigned mx = 0;
#pragma unroll
              for (int i = 0; i < 8; ++i) mx = __builtin_elementwise_max(__builtin_elementwise_max(mx, __builtin_elementwise_max(of[i][0], of[i][1])), __builtin_elementwise_max(of[i][2], of[i][3]));
              if (__builtin_amdgcn_ballot_w64(mx == SENT) == 0) break;
              __builtin_amdgcn_s_sleep(2); CFENCE();
          }
          STAMP(11);
          lds_wait_ge(sy + SY_DONE, 8);
#pragma unroll
          for (int i = 0; i < 8; ++i) *(LAS u32x4*)(lds + (wid >> 2) * L_V + 32768 + (wid & 3) * 8192 + i * 1024 + lane * 16) = of[i];
          asm volatile("s_waitcnt lgkmcnt(0)" ::: "memory");
          if (lane == 0) __hip_atomic_fetch_add(sy + SY_OL + (wid >> 1), 1u, RLX_WG); }
        STAMP(21); STAMP_FLUSH();
    } else {
        const int f = wid - 8, i32 = rq + 4 * t;
        const bool hasF = hd < (PA_F ? 28 : 0);
        f32x4 af[4][4] = {};
        { if (FPA) __builtin_amdgcn_s_setprio(1);
          unsigned d0 = 0;
          EPOCH_LOAD();
          const int l6 = ((lane - 48) * 43) >> 8, m6 = lane - 48 - 6 * l6;
          const char* tp = lane < 32 ? (const char*)x + (size_t)(256 * t + 32 * cp + lane) * 1024 : lane < 48 ? (const char*)w1 + (hasF ? hd : 0) * 128 + (16 * (t & 3) + lane - 32) * 3584
                                     : (const char*)wqkv + (hd & 24) * 32 + (2 * (cp + 8 * (t & 3)) + (l6 & 1)) * 3072 + (m6 >> 1) * 1024 + (m6 & 1) * 128;
          const unsigned tstep = lane < 32 ? 128 : lane < 48 ? 64 * 3584 : 64 * 3072;
#define TOUCH(c) do { if (TPF && lane < 60) asm volatile("global_load_dword %0, %1, off" : "+v"(d0) : "v"(tp + (c) * tstep) : "memory"); } while (0)
          if (hd == 28) { glds16(lane * 16, gate_w, lds0 + L_GATE + f * 1024); asm volatile("s_waitcnt vmcnt(0)" ::: "memory"); }
          __builtin_amdgcn_s_sleep(TSLP);
          if (f < TLA - 2) TOUCH(2 + f);
          const int xo = (64 * f + r16) * 128, fo = XB + 6144 + (4 * g4 + q4) * 128 + p4 * 8, fsw = (2 * g4 + (q4 >> 1)) & 3;
          bf16x8 xf[2][4], wf[2][4];
#define RX(c, ks, rt) xf[ks][rt] = ldsr(lds + BUFOF(c) + xo + (rt) * 2048 + ((xsw ^ (4 * (ks))) * 16))
#define RW(c, ks, cb) wf[ks][cb] = ldtr(lds + BUFOF(c) + fo + (ks) * 4096 + (((cb) ^ fsw) * 32), lds + BUFOF(c) + fo + (ks) * 4096 + 2048 + (((cb) ^ fsw) * 32))
#define M2(ks, cb, r0) do { af[cb][r0] = MFMA16(wf[ks][cb], xf[ks][r0], af[cb][r0]); af[cb][r0 + 1] = MFMA16(wf[ks][cb], xf[ks][r0 + 1], af[cb][r0 + 1]); PIN(af[cb][r0]); PIN(af[cb][r0 + 1]); SBAR(); } while (0)
#define F_SYNC(c) do { if ((c) < 7) asm volatile("s_waitcnt lgkmcnt(0)\n\ts_barrier" ::: "memory"); else CHUNK_SYNC(c, 0); \
              STAMP(12 + (c)); \
              if ((c) == 4) {     \
                  _Pragma("unroll") for (int i = 0; i < 2; ++i) st1((u32x4){SENT, SENT, SENT, SENT}, rs, lane * 16, WS_O + (unsigned)((8 * t + 2 * f + i) * 32 + hd) * 1024u); } \
              if ((c) + TLA < 8 && f == ((c) & 3)) TOUCH((c) + TLA); \
              if ((c) == RTC) {   \
                  const int u = i32 + 32 * f; const char* src = u < 112 ? (const char*)w2 + (size_t)u * 16384 : (const char*)wo + (size_t)(u - 112) * 16384; \
                  const unsigned vo = (lane >> 3) * 1024 + cp * 128 + (lane & 7) * 16; \
                  glds16(vo, src, lds0 + L_RT + f * 2048); glds16(vo, src + 8192, lds0 + L_RT + f * 2048 + 1024); \
                  if (f == 0 && i32 < 16) { const char* s2 = (const char*)wo + (size_t)(i32 + 16) * 16384; glds16(vo, s2, lds0 + L_RT + 8192); glds16(vo, s2 + 8192, lds0 + L_RT + 8192 + 1024); } \
                  if (f == 1 && lane < 32) { if (hasF) glds4(lane * 4, (const char*)b1 + hd * 128, lds0 + L_B1); glds4(lane * 4, (const char*)b2 + cp * 128, lds0 + L_B2); } } \
              if ((c) == 7) { EPOCH_TAKE(); PIN(d0); } } while (0)
          if (hasF) {
#pragma unroll
              for (int c = 0; c < 8; ++c) {
                  F_SYNC(c);
                  if (c > 0) M2(1, 0, 0);
                  RX(c, 0, 0); SBAR();
                  if (c > 0) M2(1, 0, 2);
                  RW(c, 0, 0); SBAR();
                  if (c > 0) M2(1, 1, 0);
                  RX(c, 0, 1); SBAR();
                  if (c > 0) M2(1, 1, 2);
                  RX(c, 0, 2); SBAR();
                  if (c > 0) M2(1, 2, 0);
                  RX(c, 0, 3); SBAR();
                  if (c > 0) M2(1, 2, 2);
                  RW(c, 0, 1); SBAR();
                  if (c > 0) M2(1, 3, 0);
                  RW(c, 0, 2); SBAR();
                  if (c > 0) M2(1, 3, 2);
                  RW(c, 0, 3); SBAR();
                  M2(0, 0, 0); RX(c, 1, 0); RW(c, 1, 0); SBAR();
                  M2(0, 0, 2); RX(c, 1, 1); RX(c, 1, 2); SBAR();
                  M2(0, 1, 0); RX(c, 1, 3); RW(c, 1, 1); SBAR();
                  M2(0, 1, 2); RW(c, 1, 2); RW(c, 1, 3); SBAR();
                  M2(0, 2, 0); M2(0, 2, 2); M2(0, 3, 0); M2(0, 3, 2);
              }
              M2(1, 0, 0); M2(1, 0, 2); M2(1, 1, 0); M2(1, 1, 2); M2(1, 2, 0); M2(1, 2, 2); M2(1, 3, 0); M2(1, 3, 2);
          } else {
#pragma unroll
              for (int c = 0; c < 8; ++c) {
                  F_SYNC(c);
                  if (hd == 28) {
#pragma unroll
                      for (int ks = 0; ks < 2; ++ks) { const bf16x8 gf = ldsr(lds + L_GATE + f * 1024 + c * 128 + ks * 64 + g4 * 16);
#pragma unroll
                          for (int rt = 0; rt < 4; ++rt) { RX(c, ks, rt); af[0][rt] = MFMA16(gf, xf[ks][rt], af[0][rt]); } }
                  }
              }
          }
#undef F_SYNC
#undef RX
#undef RW
#undef M2
#undef TOUCH
        }
        STAMP(1);
        asm volatile("s_waitcnt lgkmcnt(0)" ::: "memory");
        if (lane == 0) __hip_atomic_fetch_add(sy + SY_FA, 1u, RLX_WG);
        STAMP(2);
        __builtin_amdgcn_s_setprio(2);
        const int freg = (f >> 1) * L_V + (f & 1) * 16384;
        for (int n = f; n < ((f == 0 && i32 < 16) ? 5 : 4); n += 4) {
            const int u = i32 + 32 * n, r0 = (u < 112 ? 8 : 4) * hh + q4, r1 = u < 112 ? 512 : 1024;
#pragma unroll
            for (int ct = 0; ct < 2; ++ct) { const LAS char* s = lds + L_RT + n * 2048 + r0 * 128 + (g4 & 1) * 32 + p4 * 8 + ct * 64;
                st1(__builtin_bit_cast(u32x4, ldtr(s, s + r1)), rs, lane * 16, WS_W + (unsigned)((2 * cp + ct) * 144 + u) * 1024u); }
        }
        if (hd == 28) {
            const float z = bfround(g4 == 0 ? af[0][0][0] : g4 == 1 ? af[0][1][0] : g4 == 2 ? af[0][2][0] : af[0][3][0]);
            __hip_atomic_store((GAS float*)(ws + WS_G + (256 * t + 64 * f + lane) * 4), bfround(__builtin_amdgcn_rcpf(1.f + __builtin_amdgcn_exp2f(-1.4426950408889634f * z))), RLX_AGENT);
        }
        if (hasF) {
#pragma unroll
            for (int cb = 0; cb < 4; ++cb) {
                const u32x2 w = *(const LAS u32x2*)(lds + L_B1 + cb * 32 + g4 * 8);
                const float bb[4] = {U2F(w[0] << 16), U2F(w[0] & 0xffff0000u), U2F(w[1] << 16), U2F(w[1] & 0xffff0000u)};
#pragma unroll
                for (int rt = 0; rt < 4; ++rt)
#pragma unroll
                    for (int i = 0; i < 4; ++i) { const float v = af[cb][rt][i] + bb[i];
                        const float a = v * __builtin_fmaf(v * v, -2.f * 0.7978845608028654f * 1.4426950408889634f * 0.044715f, -2.f * 0.7978845608028654f * 1.4426950408889634f);
                        af[cb][rt][i] = v * __builtin_amdgcn_rcpf(1.f + __builtin_amdgcn_exp2f(a)); }
#pragma unroll
                for (int rp = 0; rp < 2; ++rp) st1(join16(af[cb][2 * rp], af[cb][2 * rp + 1], 1.f, 1.f), rs, lane * 16, WS_H + (unsigned)((8 * t + 2 * f + rp) * 112 + 4 * hd + cb) * 1024u);
            }
        }
        drain();
        if (lds_arrive(sy + SY_F, lane) == 3 && lane == 0) __hip_atomic_store((GAS u64*)(ws + flag_at(1, hd, t)), tag, RLX_AGENT);
        STAMP(3);
        f32x16 acc[4] = {};
        poll_tags<4>(ws, hh ? flag_at(1, cp + 8 * (r32 & 3), r32 >> 2) : flag_at(1, r32, t), true, tag);
        *(LAS float*)(lds + g_at(64 * f + lane)) = __hip_atomic_load((GAS float*)(ws + WS_G + (256 * t + 64 * f + lane) * 4), RLX_AGENT);
        asm volatile("s_waitcnt lgkmcnt(0)" ::: "memory");
        if (lane == 0) __hip_atomic_fetch_add(sy + SY_FSEEN, 1u, RLX_WG);
        __builtin_amdgcn_s_setprio(F2_PRIO);
        STAMP(4);
        const unsigned hb = WS_H + (unsigned)((8 * t + 2 * rq) * 112 + 28 * f) * 1024u, wb = WS_W + (unsigned)(2 * cp * 144 + 28 * f) * 1024u;
        u32x4 fr[4][4];
#define LDK(slot, n) do { fr[slot][0] = ld1(rs, lane * 16, wb + (n) * 1024); fr[slot][1] = ld1(rs, lane * 16, wb + (144 + (n)) * 1024); \
                          fr[slot][2] = ld1(rs, lane * 16, hb + (n) * 1024); fr[slot][3] = ld1(rs, lane * 16, hb + (112 + (n)) * 1024); } while (0)
#define BC(v) __builtin_bit_cast(bf16x8, v)
#define MM4(w0, w1_, h0, h1) do { acc[0] = MFMA(w0, h0, acc[0]); acc[1] = MFMA(w0, h1, acc[1]); acc[2] = MFMA(w1_, h0, acc[2]); acc[3] = MFMA(w1_, h1, acc[3]); PIN(acc[0]); PIN(acc[1]); PIN(acc[2]); PIN(acc[3]); SBAR(); } while (0)
#define FFN2(n0, n1) do { _Pragma("unroll") for (int n = (n0); n < (n1); ++n) { MM4(BC(fr[n & 3][0]), BC(fr[n & 3][1]), BC(fr[n & 3][2]), BC(fr[n & 3][3])); if (n + 4 < (n1)) LDK(n & 3, n + 4); SBAR(); } } while (0)
#pragma unroll
        for (int n = 0; n < 4; ++n) LDK(n, n);
        FFN2(0, NP1);
        STAMP(5);
        { u32x4 tmp[16];
#pragma unroll
          for (int i = 0; i < 16; ++i) tmp[i] = ld1(rs, lane * 16, WS_W + (unsigned)((2 * cp + (i & 1)) * 144 + 112 + 8 * f + (i >> 1)) * 1024u);
          __builtin_amdgcn_s_setprio(2);
          lds_wait_ge<16>(sy + SY_PASS, 8);
#pragma unroll
          for (int i = 0; i < 16; ++i) *(LAS u32x4*)(lds + freg + i * 1024 + lane * 16) = tmp[i]; }
        STAMP(6);
        if (NP1 < 28) {
#pragma unroll
            for (int n = NP1; n < NP1 + 4; ++n) LDK(n & 3, n);
            lds_wait_ge<8>(sy + SY_TRIG, 8);
            __builtin_amdgcn_s_setprio(F2_PRIO);
            FFN2(NP1, 28);
        }
#undef LDK
#undef FFN2
        lds_wait_ge(sy + SY_FSEEN, 4);
        { const float og0 = bfround(1.f - *(const LAS float*)(lds + g_at(64 * rq + r32))), og1 = bfround(1.f - *(const LAS float*)(lds + g_at(64 * rq + 32 + r32)));
#pragma unroll
          for (int ct = 0; ct < 2; ++ct)
#pragma unroll
              for (int g = 0; g < 4; ++g) { u32x2 w = *(const LAS u32x2*)(lds + L_B2 + ct * 64 + g * 16 + hh * 8); if (f != 0) w = (u32x2){0u, 0u};
                  const float bb[4] = {U2F(w[0] << 16), U2F(w[0] & 0xffff0000u), U2F(w[1] << 16), U2F(w[1] & 0xffff0000u)};
#pragma unroll
                  for (int i = 0; i < 4; ++i) { acc[2 * ct][4 * g + i] = (acc[2 * ct][4 * g + i] + bb[i]) * og0; acc[2 * ct + 1][4 * g + i] = (acc[2 * ct + 1][4 * g + i] + bb[i]) * og1; } } }
        STAMP(7);
        __builtin_amdgcn_s_setprio(3);
        lds_wait_ge<2>(sy + SY_OL + f, 2);
        STAMP(10);
#pragma unroll
        for (int n = 0; n < 8; ++n) { const LAS char* p = lds + freg + n * 2048 + lane * 16;
            const bf16x8 w0 = ldsr(p), w1_ = ldsr(p + 1024), h0 = ldsr(p + 32768), h1 = ldsr(p + 32768 + 1024); MM4(w0, w1_, h0, h1); }
#undef MM4
#undef BC
        STAMP(8);
#pragma unroll
        for (int qd = 0; qd < 4; ++qd)
#pragma unroll
            for (int g = 0; g < 4; ++g) *(LAS f32x4*)(lds + freg + qd * 4096 + g * 1024 + lane * 16) = (f32x4){acc[qd][4 * g], acc[qd][4 * g + 1], acc[qd][4 * g + 2], acc[qd][4 * g + 3]};
        asm volatile("s_waitcnt lgkmcnt(0)" ::: "memory");
        if (lane == 0) __hip_atomic_fetch_add(sy + SY_RED, 1u, RLX_WG);
        lds_wait_ge<0>(sy + SY_RED, 4);
        unsigned pk[8];
#pragma unroll
        for (int g = 0; g < 4; ++g) { f32x4 s = *(const LAS f32x4*)(lds + f * 4096 + g * 1024 + lane * 16);
#pragma unroll
            for (int w = 1; w < 4; ++w) s += *(const LAS f32x4*)(lds + (w >> 1) * L_V + (w & 1) * 16384 + f * 4096 + g * 1024 + lane * 16);
            pk[2 * g] = pack2(s[0], s[1]); pk[2 * g + 1] = pack2(s[2], s[3]); }
        unsigned short* orow = out + (size_t)(256 * t + 64 * rq + 32 * (f & 1) + r32) * DM + 64 * cp + 32 * (f >> 1) + 8 * hh;
#pragma unroll
        for (int k = 0; k < 2; ++k) {
            auto a = __builtin_amdgcn_permlane32_swap(pk[4 * k], pk[4 * k + 2], false, false), b = __builtin_amdgcn_permlane32_swap(pk[4 * k + 1], pk[4 * k + 3], false, false);
            *(u32x4*)(orow + 16 * k) = (u32x4){a[0], b[0], a[1], b[1]};
        }
        if (bx == 0 && f == 0 && lane == 0) __hip_atomic_store((GAS u64*)(ws + WS_FLAGS), tag, RLX_AGENT);
        STAMP(9); STAMP_FLUSH();
    }
}

extern "C" void kernel_launch(void* const* d_in, const int* in_sizes, int n_in, void* d_out, int out_size, void* d_ws, size_t ws_size, hipStream_t stream) {
    (void)in_sizes; (void)n_in; (void)out_size; (void)ws_size;
    hipLaunchKernelGGL(gated_mix_fused, dim3(256), dim3(768), 0, stream, (const unsigned short*)d_in[0], (const unsigned short*)d_in[1], (const unsigned short*)d_in[2],
                       (const unsigned short*)d_in[4], (const unsigned short*)d_in[5], (const unsigned short*)d_in[6], (const unsigned short*)d_in[7], (const unsigned short*)d_in[8],
                       (unsigned short*)d_out, (char*)d_ws);
}
```

```cpp
#include <hip/hip_runtime.h>
#include <hip/hip_bf16.h>
#include <stdint.h>

typedef __attribute__((ext_vector_type(8))) __bf16 bf16x8;
typedef __attribute__((ext_vector_type(16))) __bf16 bf16x16;
typedef __attribute__((ext_vector_type(2))) __bf16 bf16x2;
typedef __attribute__((ext_vector_type(4))) short s16x4;
typedef __attribute__((ext_vector_type(16))) float f32x16;
typedef __attribute__((ext_vector_type(8))) float f32x8;
typedef __attribute__((ext_vector_type(4))) float f32x4;
typedef __attribute__((ext_vector_type(2))) float f32x2;
typedef __attribute__((ext_vector_type(4))) unsigned u32x4;
typedef __attribute__((ext_vector_type(2))) unsigned u32x2;
typedef unsigned long long u64;
#define LAS __attribute__((address_space(3)))
#define GAS __attribute__((address_space(1)))
#if defined(__HIP_DEVICE_COMPILE__) && !defined(ALLOW_PK)
#define NOPK __attribute__((target("no-packed-fp32-ops")))
#else
#define NOPK
#endif
#define DI __device__ __forceinline__ NOPK
#define U2F(x) __builtin_bit_cast(float, (unsigned)(x))
#define F2U(x) __builtin_bit_cast(unsigned, (float)(x))
#define RLX_AGENT __ATOMIC_RELAXED, __HIP_MEMORY_SCOPE_AGENT
#define RLX_WG __ATOMIC_RELAXED, __HIP_MEMORY_SCOPE_WORKGROUP

constexpr int DM = 512, DFF = 1792;
constexpr unsigned WS_FLAGS = 0;
constexpr unsigned WS_FLAG0 = 4096, FLAG_STRIDE = 128;
constexpr unsigned WS_G = 106496;
constexpr unsigned WS_KV = 131072;
constexpr unsigned WS_H = WS_KV + 32u * 8u * 16384u;
constexpr unsigned WS_O = WS_H + 64u * 112u * 1024u;
constexpr unsigned WS_W = WS_O + 64u * 32u * 1024u;
constexpr unsigned WS_END = WS_W + 16u * 144u * 1024u;
constexpr unsigned WS_DIAG = WS_END;
constexpr int XB = 32768, BUFB = XB + 6144 + 8192;
constexpr int L_V = 65536;
constexpr int L_GATE = 3 * BUFB;
constexpr int L_RT = L_GATE + 4096;
constexpr int L_MISC = 163840 - 2048;
constexpr int L_DUMMY = L_RT + 10240;
constexpr int L_B1 = L_MISC + 272, L_B2 = L_MISC + 512 + 272, L_SYNC = L_MISC + 1024 + 272;
static_assert(L_DUMMY + 1024 <= L_MISC, "lds");
enum { SY_READY = 0  , SY_KV = 8, SY_DONE = 9, SY_F = 10, SY_PASS = 11, SY_RED = 12, SY_TRIG = 13, SY_FSEEN = 14, SY_AW = 15, SY_OL = 16  , SY_FA = 20, SY_PROG = 24  , SY_N = 32 };
constexpr unsigned SENT = 0xFFFFFFFFu;
constexpr unsigned SPIN_CAP = 1u << 18;

#define MFMA(a, b, c) __builtin_amdgcn_mfma_f32_32x32x16_bf16((a), (b), (c), 0, 0, 0)
#define MFMA16(a, b, c) __builtin_amdgcn_mfma_f32_16x16x32_bf16((a), (b), (c), 0, 0, 0)
#define SBAR() __builtin_amdgcn_sched_barrier(0)
#define CFENCE() asm volatile("" ::: "memory")
#define PIN(x) asm volatile("" : "+v"(x))

#define F2_PRIO 0
#define DSPLIT 4
#define FPA 0
#define TPF 2
#define TSLP 16
#define TLA 4
#define RTC 3
#define CPF 1
#define LEAP 1
#define PA_F 1
#define NP1 20
#define TRIGJ 5
#define STAMP(i) do {} while (0)
#define STAMP_FLUSH() do {} while (0)

DI unsigned pack2(float lo, float hi) { f32x2 v = {lo, hi}; return __builtin_bit_cast(unsigned, __builtin_convertvector(v, bf16x2)); }
DI u32x4 pack8(const f32x16& a, int o, float s) {
    u32x4 r; r[0] = pack2(a[o] * s, a[o + 1] * s); r[1] = pack2(a[o + 2] * s, a[o + 3] * s); r[2] = pack2(a[o + 4] * s, a[o + 5] * s); r[3] = pack2(a[o + 6] * s, a[o + 7] * s); return r; }
DI u32x4 pack8(const f32x16& a, int o) {
    u32x4 r; r[0] = pack2(a[o], a[o + 1]); r[1] = pack2(a[o + 2], a[o + 3]); r[2] = pack2(a[o + 4], a[o + 5]); r[3] = pack2(a[o + 6], a[o + 7]); return r; }
DI float bfround(float v) { return (float)(__bf16)v; }
DI float bf2f(unsigned short u) { return U2F((unsigned)u << 16); }
DI bf16x8 ldsr(const LAS char* p) { return *(const LAS bf16x8*)p; }
DI bf16x8 ldtr(const LAS char* p0, const LAS char* p1) {
    s16x4 a = __builtin_amdgcn_ds_read_tr16_b64_v4i16((LAS s16x4*)p0), b = __builtin_amdgcn_ds_read_tr16_b64_v4i16((LAS s16x4*)p1);
    return __builtin_bit_cast(bf16x8, __builtin_shufflevector(a, b, 0, 1, 2, 3, 4, 5, 6, 7)); }
DI void glds16(unsigned voff, const void* sbase, unsigned lds) {
    unsigned keep; asm volatile("s_mov_b32 %0, m0\n\ts_mov_b32 m0, %3\n\ts_nop 0\n\tglobal_load_lds_dwordx4 %1, %2\n\ts_mov_b32 m0, %0" : "=&s"(keep) : "v"(voff), "s"(sbase), "s"(lds) : "memory"); }
DI void glds4(unsigned voff, const void* sbase, unsigned lds) {
    unsigned keep; asm volatile("s_mov_b32 %0, m0\n\ts_mov_b32 m0, %3\n\ts_nop 0\n\tglobal_load_lds_dword %1, %2\n\ts_mov_b32 m0, %0" : "=&s"(keep) : "v"(voff), "s"(sbase), "s"(lds) : "memory"); }
DI u32x4 ld1(__amdgpu_buffer_rsrc_t rs, unsigned voff, unsigned soff) { return __builtin_amdgcn_raw_buffer_load_b128(rs, voff, soff, 16); }
DI void st1(u32x4 v, __amdgpu_buffer_rsrc_t rs, unsigned voff, unsigned soff) {
    __builtin_amdgcn_raw_buffer_store_b128(v, rs, voff, soff, 16); asm volatile("s_nop 1" :: "v"(v) : "memory"); }
DI void drain() { asm volatile("s_waitcnt vmcnt(0)" ::: "memory"); }
DI unsigned lds_arrive(LAS unsigned* w, int lane) {
    unsigned old = 0; if (lane == 0) old = __hip_atomic_fetch_add(w, 1u, RLX_WG); return __builtin_amdgcn_readfirstlane(old); }
template <int SL = 1> DI void lds_wait_ge(LAS unsigned* w, unsigned want) {
#pragma nounroll
    for (unsigned s = 0; s < (SPIN_CAP << 4); ++s) { if ((unsigned)__builtin_amdgcn_readfirstlane(__hip_atomic_load(w, RLX_WG)) >= want) break; __builtin_amdgcn_s_sleep(SL); }
    CFENCE(); }
template <int SL = 1> DI void poll_tags(const char* ws, unsigned flag_off, bool active, u64 tag) {
#pragma nounroll
    for (unsigned s = 0; s < SPIN_CAP; ++s) {
        u64 v = tag; if (active) v = __hip_atomic_load((GAS u64*)(ws + flag_off), RLX_AGENT);
        if (__builtin_amdgcn_ballot_w64(v != tag) == 0) break; __builtin_amdgcn_s_sleep(SL); }
    __builtin_amdgcn_fence(__ATOMIC_ACQUIRE, "wavefront"); CFENCE(); }
DI unsigned flag_at(int kind, int hd, int t) { return WS_FLAG0 + (unsigned)(kind * 256 + hd * 8 + t) * FLAG_STRIDE; }
DI int g_at(int i) { return L_MISC + (i >> 6) * 512 + 16 + (i & 63) * 4; }
DI u32x4 join16(const f32x4& a, const f32x4& b, float sa, float sb) {
    auto r0 = __builtin_amdgcn_permlane16_swap(pack2(a[0] * sa, a[1] * sa), pack2(b[0] * sb, b[1] * sb), false, false);
    auto r1 = __builtin_amdgcn_permlane16_swap(pack2(a[2] * sa, a[3] * sa), pack2(b[2] * sb, b[3] * sb), false, false);
    return (u32x4){r0[0], r1[0], r0[1], r1[1]}; }
DI f32x16 exp16(f32x16 s) {
#pragma unroll
    for (int i = 0; i < 16; ++i) s[i] = __builtin_amdgcn_exp2f(s[i]);
    return s; }
DI f32x16 pv(bf16x8 v0, bf16x8 v1, const f32x16& p, f32x16 o, int idx) {
    const f32x8 lo = __builtin_shufflevector(p, p, 0, 1, 2, 3, 4, 5, 6, 7), hi = __builtin_shufflevector(p, p, 8, 9, 10, 11, 12, 13, 14, 15);
    o = __builtin_amdgcn_smfmac_f32_32x32x32_bf16(v0, __builtin_bit_cast(bf16x16, lo), o, idx, 0, 0);
    o = __builtin_amdgcn_smfmac_f32_32x32x32_bf16(v1, __builtin_bit_cast(bf16x16, hi), o, idx, 0, 0);
    return o; }
DI float rowmax16(const f32x16& s) {
    float m = __builtin_fmaxf(__builtin_fmaxf(s[0], s[1]), s[2]);
#pragma unroll
    for (int i = 3; i < 15; i += 2) m = __builtin_fmaxf(__builtin_fmaxf(m, s[i]), s[i + 1]);
    return __builtin_fmaxf(m, s[15]); }
DI float xhalf_max(float m) { auto r = __builtin_amdgcn_permlane32_swap(F2U(m), F2U(m), false, false); return __builtin_fmaxf(U2F(r[0]), U2F(r[1])); }

__global__ __launch_bounds__(768, 3) NOPK void gated_mix_fused(const unsigned short* __restrict__ x, const unsigned short* __restrict__ wqkv, const unsigned short* __restrict__ wo,
                                                         const unsigned short* __restrict__ w1, const unsigned short* __restrict__ b1, const unsigned short* __restrict__ w2,
                                                         const unsigned short* __restrict__ b2, const unsigned short* __restrict__ gate_w, unsigned short* __restrict__ out, char* ws) {
    __shared__ __attribute__((aligned(1024))) char smem[163840];
    LAS char* lds = (LAS char*)smem;
    const unsigned lds0 = (unsigned)(uintptr_t)lds;
    const int lane = __builtin_amdgcn_workitem_id_x() & 63, wid = __builtin_amdgcn_readfirstlane(__builtin_amdgcn_workitem_id_x() >> 6), r32 = lane & 31, hh = lane >> 5;
    const int bx = __builtin_amdgcn_workgroup_id_x(), hd = 8 * (bx & 3) + ((bx >> 3) & 7), t = 4 * ((bx >> 2) & 1) + (bx >> 6);
    const __amdgpu_buffer_rsrc_t rs = __builtin_amdgcn_make_buffer_rsrc(ws, 0, WS_END, 0x00020000);
    LAS unsigned* sy = (LAS unsigned*)(lds + L_SYNC);
    asm volatile("" :: "s"(x), "s"(wqkv), "s"(wo), "s"(w1), "s"(b1), "s"(w2), "s"(b2), "s"(gate_w), "s"(out), "s"(ws));
    u64 tag; u32x2 ep;
#define EPOCH_LOAD() asm volatile("global_load_dwordx2 %0, %1, %2" : "=v"(ep) : "v"(0), "s"(ws) : "memory")
#define EPOCH_TAKE() do { PIN(ep); tag = (((u64)(unsigned)__builtin_amdgcn_readfirstlane(ep[1]) << 32) | (unsigned)__builtin_amdgcn_readfirstlane(ep[0])) + 1ull; } while (0)
    const int cp = hd & 7, rq = hd >> 3;
    const int g4 = lane >> 4, r16 = lane & 15, q4 = r16 >> 2, p4 = lane & 3;
    const int xsw = g4 ^ (r16 >> 1);
    STAMP(0);
#define BUFOF(c) ((((c) + 1) % 3) * BUFB)
#define CHUNK_SYNC(c, n) do { if ((c) < 7) asm volatile("s_waitcnt vmcnt(" #n ") lgkmcnt(0)\n\ts_barrier" ::: "memory"); else asm volatile("s_waitcnt vmcnt(0) lgkmcnt(0)\n\ts_barrier" ::: "memory"); } while (0)

    if (wid < 8) {
        f32x4 aq[2] = {}, ak[2] = {}, av[2] = {};
        { const int c0 = (lane & 7) ^ (lane >> 4), rho = lane >> 1, r8 = lane >> 3;
          const unsigned dvo0 = r8 * 1024 + c0 * 16, dvo1 = r8 * 1024 + (c0 ^ 4) * 16;
          const unsigned dvq = (8 * ((rho >> 2) & 3) + 4 * (rho >> 4) + (rho & 3)) * 3072 + (lane & 1) * 16;
          const unsigned dvf = (8 * (r8 >> 2) + (r8 & 3)) * 3584 + (((lane & 7) ^ (r8 & 6)) * 16);
          const char* xbase = (const char*)x + (size_t)(256 * t + 32 * wid) * 1024;
          const bool hq = wid < 6, hf = hd < 28;
          const char* qbase = (const char*)wqkv + hd * 32 + (hq ? (wid >> 1) * 1024 + (wid & 1) * 32 * 3072 : 0);
          const char* fbase = (const char*)w1 + (hf ? hd * 128 : 0) + (32 * (wid >> 2) + 16 * (wid & 1) + 4 * ((wid >> 1) & 1)) * 3584;
          const unsigned vq_ = hq ? dvq : 0u, vf_ = hf ? dvf : 0u;
#define DMA_X(c) do { _Pragma("unroll") for (int i = 0; i < 4; ++i) glds16((i & 1) ? dvo1 : dvo0, xbase + (c) * 128 + i * 8192, lds0 + BUFOF(c) + wid * 4096 + i * 1024); } while (0)
#define DMA_ALL(c) do { glds16(vq_, qbase + (c) * 64 * 3072, hq ? lds0 + BUFOF(c) + XB + wid * 1024 : lds0 + L_DUMMY); \
              glds16(vf_, fbase + (c) * 64 * 3584, hf ? lds0 + BUFOF(c) + XB + 6144 + wid * 1024 : lds0 + L_DUMMY); DMA_X(c); } while (0)
          DMA_ALL(0);
          EPOCH_LOAD();
          if (wid == 0) {
              if (lane < SY_N) sy[lane] = 0;
              if (lane < 8) { const unsigned one = lane < 4 ? 0x3F803F80u : 0u; *(LAS u32x4*)(lds + L_MISC + (lane & 3) * 512 + (lane >> 2) * 496) = (u32x4){one, one, one, one}; }
          }
          __builtin_amdgcn_s_sleep(4);
          DMA_ALL(1);
          STAMP(20);
          const int xo = (32 * wid + r16) * 128, wo_ = XB + (4 * g4 + q4) * 32 + p4 * 8;
          bf16x8 xa[2], xb[2], wq[2], wk[2], wv[2];
#define RX(d, c, ks, rt) d[ks] = ldsr(lds + BUFOF(c) + xo + (rt) * 2048 + ((xsw ^ (4 * (ks))) * 16))
#define RW(d, c, ks, im) d[ks] = ldtr(lds + BUFOF(c) + wo_ + (im) * 2048 + (ks) * 1024, lds + BUFOF(c) + wo_ + (im) * 2048 + (ks) * 1024 + 512)
#define M2(A, W, ks) do { A[0] = MFMA16(W[ks], xa[ks], A[0]); A[1] = MFMA16(W[ks], xb[ks], A[1]); PIN(A[0]); PIN(A[1]); SBAR(); } while (0)
#define MV(ks) do { av[0] = MFMA16(xa[ks], wv[ks], av[0]); av[1] = MFMA16(xb[ks], wv[ks], av[1]); PIN(av[0]); PIN(av[1]); SBAR(); } while (0)
#pragma unroll
          for (int c = 0; c < 8; ++c) {
              CHUNK_SYNC(c, 6);
              STAMP(12 + c);
              if (c == 7) { EPOCH_TAKE(); __builtin_amdgcn_s_setprio(3); }
              if (c + 2 < 8 && wid >= DSPLIT) DMA_ALL(c + 2);
              if (c > 0) M2(aq, wq, 1);
              RX(xa, c, 0, 0); RX(xb, c, 0, 1); RW(wq, c, 0, 0); SBAR();
              if (c > 0) M2(ak, wk, 1);
              RW(wk, c, 0, 1); RW(wv, c, 0, 2); SBAR();
              if (c > 0) MV(1);
              RX(xa, c, 1, 0); RX(xb, c, 1, 1); RW(wq, c, 1, 0); SBAR();
              M2(aq, wq, 0);
              RW(wk, c, 1, 1); RW(wv, c, 1, 2); SBAR();
              M2(ak, wk, 0);
              MV(0);
              if (c + 2 < 8 && wid < DSPLIT) DMA_ALL(c + 2);
          }
          M2(aq, wq, 1); M2(ak, wk, 1); MV(1);
#undef RX
#undef RW
#undef M2
#undef MV
#undef DMA_ALL
#undef DMA_X
        }
        STAMP(1);
        constexpr float QS = 0.25f * 1.4426950408889634f;
        const bf16x8 qf = __builtin_bit_cast(bf16x8, join16(aq[0], aq[1], QS, QS));
        const unsigned kvg = WS_KV + (unsigned)(hd * 8 + t) * 16384u, og = WS_O + (unsigned)((8 * t + wid) * 32 + hd) * 1024u;
        { const u32x4 kk = join16(ak[0], ak[1], 1.f, 1.f), vv = join16(av[0], av[1], 1.f, 1.f);
          const int ok = wid * 1024 + lane * 16, ov = wid * 1024 + (g4 & 1) * 512 + hh * 256 + r16 * 16;
          *(LAS u32x4*)(lds + ok) = kk; *(LAS u32x4*)(lds + L_V + ov) = vv;
          asm volatile("s_waitcnt lgkmcnt(0)" ::: "memory");
          if (lane == 0) __hip_atomic_fetch_add(sy + SY_AW, 1u, RLX_WG);
          st1(kk, rs, ok, kvg); st1(vv, rs, ov + 8192, kvg); }
        lds_wait_ge<1>(sy + SY_AW, 8);
        STAMP(2);
        drain();
        if (lds_arrive(sy + SY_KV, lane) == 7 && lane == 0) __hip_atomic_store((GAS u64*)(ws + flag_at(0, hd, t)), tag, RLX_AGENT);
        const int idx = 0xDDDD;
        const LAS char* kp = lds + lane * 16;
        const bool vl = r32 < 16;
        const LAS char* vp = lds + (vl ? L_V + 512 + hh * 256 + r32 * 16 : (r32 & 11) == 0 ? L_MISC : L_MISC + 496);
        const int vstep = vl ? 2048 : 0;
        f32x16 o = (f32x16){}, negm, sA, sB = (f32x16){};
        bf16x8 kf = ldsr(kp), v0 = ldsr(vl ? vp - 512 : vp), v1 = (bf16x8){};
        { const f32x16 s0 = MFMA(kf, qf, (f32x16){}); const float m0 = -xhalf_max(rowmax16(s0));
#pragma unroll
          for (int i = 0; i < 16; ++i) negm[i] = m0; }
        sA = MFMA(kf, qf, negm);
        kf = ldsr(kp + 1024);
        u32x4 stg[8];
        const unsigned rkv = WS_KV + (unsigned)(hd * 8 + ((t + wid) & 7)) * 16384u;
        if (wid > 0) {
            poll_tags(ws, flag_at(0, hd, (t + wid) & 7), lane == 0, tag);
#pragma unroll
            for (int i = 0; i < 8; ++i) stg[i] = ld1(rs, lane * 16 + i * 1024, rkv);
        }
        STAMP(10);
        __builtin_amdgcn_s_setprio(0);
#define EX(S, a, b) do { _Pragma("unroll") for (int i_ = (a); i_ < (b); ++i_) S[i_] = __builtin_amdgcn_exp2f(S[i_]); PIN(S); SBAR(); } while (0)
#define LO8(S) __builtin_bit_cast(bf16x16, __builtin_shufflevector(S, S, 0, 1, 2, 3, 4, 5, 6, 7))
#define HI8(S) __builtin_bit_cast(bf16x16, __builtin_shufflevector(S, S, 8, 9, 10, 11, 12, 13, 14, 15))
#define SM(a, b) do { o = __builtin_amdgcn_smfmac_f32_32x32x32_bf16(a, b, o, idx, 0, 0); PIN(o); SBAR(); } while (0)
#define TILE(CUR, OTH, KO, VO) do { SBAR(); EX(CUR, 0, 2); SM(v1, HI8(OTH)); v1 = ldsr(vp + (VO)); SBAR(); EX(CUR, 2, 6); \
            OTH = MFMA(kf, qf, negm); PIN(OTH); SBAR(); kf = ldsr(kp + (KO)); SBAR(); EX(CUR, 6, 10); SM(v0, LO8(CUR)); v0 = ldsr(vp + (VO) + 512); SBAR(); EX(CUR, 10, 16); } while (0)
#define PAIR(KO) do { TILE(sA, sB, (KO) + 2048, 0); TILE(sB, sA, (KO) + 3072, 1024); vp += vstep; } while (0)
        PAIR(0);
        if (wid > 0) {
            lds_wait_ge(sy + SY_FA, 4);
#pragma unroll
            for (int i = 0; i < 8; ++i) { *(LAS u32x4*)(lds + wid * 8192 + i * 1024 + lane * 16) = stg[i]; stg[i] = ld1(rs, lane * 16 + i * 1024, rkv + 8192); }
        }
        PAIR(2048);
        if (wid > 0) {
#pragma unroll
            for (int i = 0; i < 8; ++i) *(LAS u32x4*)(lds + L_V + wid * 8192 + i * 1024 + lane * 16) = stg[i];
            asm volatile("s_waitcnt lgkmcnt(0)" ::: "memory");
            if (lane == 0) __hip_atomic_store(sy + SY_READY + wid, 1u, RLX_WG);
        }
        PAIR(4096);
        lds_wait_ge(sy + SY_READY + 1, 1);
        PAIR(6144);
        STAMP(3);
        for (int j = 1; j < 8; ++j) {
            kp += 8192;
            if (j > 1 && j < 6) STAMP(4 + j);
            if (LEAP) {
                if (lane == 0) __hip_atomic_store(sy + SY_PROG + wid, (unsigned)j, RLX_WG);
                if ((unsigned)__builtin_amdgcn_readfirstlane(__hip_atomic_load(sy + SY_PROG + (wid ^ 4), RLX_WG)) >= (unsigned)j) __builtin_amdgcn_s_setprio(1); else __builtin_amdgcn_s_setprio(0);
            }
            PAIR(0); PAIR(2048); PAIR(4096);
            if (j < 7) lds_wait_ge(sy + SY_READY + j + 1, 1);
            PAIR(6144);
            if (j == 4 && lane == 0) __hip_atomic_fetch_add(sy + SY_PASS, 1u, RLX_WG);
            if (j == TRIGJ && lane == 0) __hip_atomic_fetch_add(sy + SY_TRIG, 1u, RLX_WG);
        }
        SM(v1, HI8(sB));
#undef PAIR
#undef TILE
#undef SM
#undef EX
        asm volatile("s_waitcnt lgkmcnt(0)" ::: "memory");
        if (lane == 0) __hip_atomic_fetch_add(sy + SY_DONE, 1u, RLX_WG);
        STAMP(4);
        { float chk = o[8] * 0.f;
#pragma unroll
          for (int i = 0; i < 8; ++i) chk = __builtin_fmaf(o[i], 0.f, chk);
          if (__builtin_expect(0 != __builtin_amdgcn_ballot_w64(!(chk == 0.f) || o[8] > 1.8e19f), 0)) {
              float m = -__builtin_inff();
#pragma nounroll
              for (int tl = 0; tl < 64; ++tl) { const bf16x8 kg = __builtin_bit_cast(bf16x8, ld1(rs, lane * 16, WS_KV + (unsigned)(hd * 8 + (tl >> 3)) * 16384u + (tl & 7) * 1024));
                  m = __builtin_fmaxf(m, rowmax16(MFMA(kg, qf, (f32x16){}))); }
              m = -xhalf_max(m);
#pragma unroll
              for (int i = 0; i < 16; ++i) negm[i] = m;
              o = (f32x16){};
              const u32x4 ones = {0x3F803F80u, 0x3F803F80u, 0x3F803F80u, 0x3F803F80u};
#pragma nounroll
              for (int tl = 0; tl < 64; ++tl) { const unsigned so = WS_KV + (unsigned)(hd * 8 + (tl >> 3)) * 16384u + (tl & 7) * 1024;
                  const bf16x8 kg = __builtin_bit_cast(bf16x8, ld1(rs, lane * 16, so));
                  const u32x4 a0 = ld1(rs, 8192 + hh * 256 + r16 * 16, so), a1 = ld1(rs, 8192 + 512 + hh * 256 + r16 * 16, so);
                  o = pv(__builtin_bit_cast(bf16x8, vl ? a0 : ones), __builtin_bit_cast(bf16x8, vl ? a1 : ones), exp16(MFMA(kg, qf, negm)), o, idx); }
          } }
        lds_wait_ge(sy + SY_FSEEN, 4);
        { u32x4 ov = pack8(o, 0, *(const LAS float*)(lds + g_at(32 * wid + r32)) * __builtin_amdgcn_rcpf(o[8]));
#pragma unroll
          for (int i = 0; i < 4; ++i) ov[i] = ov[i] == SENT ? 0x7FFF7FFFu : ov[i];
          st1(ov, rs, lane * 16, og); }
        STAMP(5);
        { u32x4 of[8];
          const unsigned ob = WS_O + (unsigned)((8 * t + 2 * rq) * 32 + 4 * wid) * 1024u;
#pragma nounroll
          for (unsigned s = 0; s < SPIN_CAP; ++s) {
#pragma unroll
              for (int i = 0; i < 8; ++i) of[i] = ld1(rs, lane * 16, ob + ((i & 1) * 32 + (i >> 1)) * 1024);
              unsigned mx = 0;
#pragma unroll
              for (int i = 0; i < 8; ++i) mx = __builtin_elementwise_max(__builtin_elementwise_max(mx, __builtin_elementwise_max(of[i][0], of[i][1])), __builtin_elementwise_max(of[i][2], of[i][3]));
              if (__builtin_amdgcn_ballot_w64(mx == SENT) == 0) break;
              __builtin_amdgcn_s_sleep(2); CFENCE();
          }
          STAMP(11);
          lds_wait_ge(sy + SY_DONE, 8);
#pragma unroll
          for (int i = 0; i < 8; ++i) *(LAS u32x4*)(lds + (wid >> 2) * L_V + 32768 + (wid & 3) * 8192 + i * 1024 + lane * 16) = of[i];
          asm volatile("s_waitcnt lgkmcnt(0)" ::: "memory");
          if (lane == 0) __hip_atomic_fetch_add(sy + SY_OL + (wid >> 1), 1u, RLX_WG); }
        asm volatile(".Lgm_end:");
        STAMP(21); STAMP_FLUSH();
    } else {
        const int f = wid - 8, i32 = rq + 4 * t;
        const bool hasF = hd < (PA_F ? 28 : 0);
        f32x4 af[4][4] = {};
        { if (FPA) __builtin_amdgcn_s_setprio(1);
          unsigned d0 = 0;
          EPOCH_LOAD();
          const int l6 = ((lane - 48) * 43) >> 8, m6 = lane - 48 - 6 * l6;
          const char* tp = lane < 32 ? (const char*)x + (size_t)(256 * t + 32 * cp + lane) * 1024 : lane < 48 ? (const char*)w1 + (hasF ? hd : 0) * 128 + (16 * (t & 3) + lane - 32) * 3584
                                     : (const char*)wqkv + (hd & 24) * 32 + (2 * (cp + 8 * (t & 3)) + (l6 & 1)) * 3072 + (m6 >> 1) * 1024 + (m6 & 1) * 128;
          const unsigned tstep = lane < 32 ? 128 : lane < 48 ? 64 * 3584 : 64 * 3072;
#define TOUCH(c) do { if (TPF && lane < 60) asm volatile("global_load_dword %0, %1, off" : "+v"(d0) : "v"(tp + (c) * tstep) : "memory"); } while (0)
          unsigned d1 = 0;
          if (CPF && f == 3) {
              const char* pc; unsigned len;
              asm volatile("s_getpc_b64 %0\n.Lgm_ref:\n\ts_mov_b32 %1, .Lgm_end-.Lgm_ref" : "=s"(pc), "=s"(len));
              const unsigned po = (unsigned)((bx >> 3) + 32 * lane) * 128u;
              if ((int)po < (int)len) asm volatile("global_load_dword %0, %1, %2" : "+v"(d1) : "v"(po), "s"(pc) : "memory");
          }
          if (hd == 28) { glds16(lane * 16, gate_w, lds0 + L_GATE + f * 1024); asm volatile("s_waitcnt vmcnt(0)" ::: "memory"); }
          __builtin_amdgcn_s_sleep(TSLP);
          if (TPF == 2) { TOUCH(2 + f); if (f < 2) TOUCH(6 + f); } else if (f < TLA - 2) TOUCH(2 + f);
          const int xo = (64 * f + r16) * 128, fo = XB + 6144 + (4 * g4 + q4) * 128 + p4 * 8, fsw = (2 * g4 + (q4 >> 1)) & 3;
          bf16x8 xf[2][4], wf[2][4];
#define RX(c, ks, rt) xf[ks][rt] = ldsr(lds + BUFOF(c) + xo + (rt) * 2048 + ((xsw ^ (4 * (ks))) * 16))
#define RW(c, ks, cb) wf[ks][cb] = ldtr(lds + BUFOF(c) + fo + (ks) * 4096 + (((cb) ^ fsw) * 32), lds + BUFOF(c) + fo + (ks) * 4096 + 2048 + (((cb) ^ fsw) * 32))
#define M2(ks, cb, r0) do { af[cb][r0] = MFMA16(wf[ks][cb], xf[ks][r0], af[cb][r0]); af[cb][r0 + 1] = MFMA16(wf[ks][cb], xf[ks][r0 + 1], af[cb][r0 + 1]); PIN(af[cb][r0]); PIN(af[cb][r0 + 1]); SBAR(); } while (0)
#define F_SYNC(c) do { if ((c) < 7) asm volatile("s_waitcnt lgkmcnt(0)\n\ts_barrier" ::: "memory"); else CHUNK_SYNC(c, 0); \
              STAMP(12 + (c)); \
              if ((c) == 4) {     \
                  _Pragma("unroll") for (int i = 0; i < 2; ++i) st1((u32x4){SENT, SENT, SENT, SENT}, rs, lane * 16, WS_O + (unsigned)((8 * t + 2 * f + i) * 32 + hd) * 1024u); } \
              if (TPF == 1 && (c) + TLA < 8 && f == ((c) & 3)) TOUCH((c) + TLA); \
              if ((c) == RTC) {   \
                  const int u = i32 + 32 * f; const char* src = u < 112 ? (const char*)w2 + (size_t)u * 16384 : (const char*)wo + (size_t)(u - 112) * 16384; \
                  const unsigned vo = (lane >> 3) * 1024 + cp * 128 + (lane & 7) * 16; \
                  glds16(vo, src, lds0 + L_RT + f * 2048); glds16(vo, src + 8192, lds0 + L_RT + f * 2048 + 1024); \
                  if (f == 0 && i32 < 16) { const char* s2 = (const char*)wo + (size_t)(i32 + 16) * 16384; glds16(vo, s2, lds0 + L_RT + 8192); glds16(vo, s2 + 8192, lds0 + L_RT + 8192 + 1024); } \
                  if (f == 1 && lane < 32) { if (hasF) glds4(lane * 4, (const char*)b1 + hd * 128, lds0 + L_B1); glds4(lane * 4, (const char*)b2 + cp * 128, lds0 + L_B2); } } \
              if ((c) == 7) { EPOCH_TAKE(); PIN(d0); PIN(d1); } } while (0)
          if (hasF) {
#pragma unroll
              for (int c = 0; c < 8; ++c) {
                  F_SYNC(c);
                  if (c > 0) M2(1, 0, 0);
                  RX(c, 0, 0); SBAR();
                  if (c > 0) M2(1, 0, 2);
                  RW(c, 0, 0); SBAR();
                  if (c > 0) M2(1, 1, 0);
                  RX(c, 0, 1); SBAR();
                  if (c > 0) M2(1, 1, 2);
                  RX(c, 0, 2); SBAR();
                  if (c > 0) M2(1, 2, 0);
                  RX(c, 0, 3); SBAR();
                  if (c > 0) M2(1, 2, 2);
                  RW(c, 0, 1); SBAR();
                  if (c > 0) M2(1, 3, 0);
                  RW(c, 0, 2); SBAR();
                  if (c > 0) M2(1, 3, 2);
                  RW(c, 0, 3); SBAR();
                  M2(0, 0, 0); RX(c, 1, 0); RW(c, 1, 0); SBAR();
                  M2(0, 0, 2); RX(c, 1, 1); RX(c, 1, 2); SBAR();
                  M2(0, 1, 0); RX(c, 1, 3); RW(c, 1, 1); SBAR();
                  M2(0, 1, 2); RW(c, 1, 2); RW(c, 1, 3); SBAR();
                  M2(0, 2, 0); M2(0, 2, 2); M2(0, 3, 0); M2(0, 3, 2);
              }
              M2(1, 0, 0); M2(1, 0, 2); M2(1, 1, 0); M2(1, 1, 2); M2(1, 2, 0); M2(1, 2, 2); M2(1, 3, 0); M2(1, 3, 2);
          } else {
#pragma unroll
              for (int c = 0; c < 8; ++c) {
                  F_SYNC(c);
                  if (hd == 28) {
#pragma unroll
                      for (int ks = 0; ks < 2; ++ks) { const bf16x8 gf = ldsr(lds + L_GATE + f * 1024 + c * 128 + ks * 64 + g4 * 16);
#pragma unroll
                          for (int rt = 0; rt < 4; ++rt) { RX(c, ks, rt); af[0][rt] = MFMA16(gf, xf[ks][rt], af[0][rt]); } }
                  }
              }
          }
#undef F_SYNC
#undef RX
#undef RW
#undef M2
#undef TOUCH
        }
        STAMP(1);
        asm volatile("s_waitcnt lgkmcnt(0)" ::: "memory");
        if (lane == 0) __hip_atomic_fetch_add(sy + SY_FA, 1u, RLX_WG);
        STAMP(2);
        __builtin_amdgcn_s_setprio(2);
        const int freg = (f >> 1) * L_V + (f & 1) * 16384;
        for (int n = f; n < ((f == 0 && i32 < 16) ? 5 : 4); n += 4) {
            const int u = i32 + 32 * n, r0 = (u < 112 ? 8 : 4) * hh + q4, r1 = u < 112 ? 512 : 1024;
#pragma unroll
            for (int ct = 0; ct < 2; ++ct) { const LAS char* s = lds + L_RT + n * 2048 + r0 * 128 + (g4 & 1) * 32 + p4 * 8 + ct * 64;
                st1(__builtin_bit_cast(u32x4, ldtr(s, s + r1)), rs, lane * 16, WS_W + (unsigned)((2 * cp + ct) * 144 + u) * 1024u); }
        }
        if (hd == 28) {
            const float z = bfround(g4 == 0 ? af[0][0][0] : g4 == 1 ? af[0][1][0] : g4 == 2 ? af[0][2][0] : af[0][3][0]);
            __hip_atomic_store((GAS float*)(ws + WS_G + (256 * t + 64 * f + lane) * 4), bfround(__builtin_amdgcn_rcpf(1.f + __builtin_amdgcn_exp2f(-1.4426950408889634f * z))), RLX_AGENT);
        }
        if (hasF) {
#pragma unroll
            for (int cb = 0; cb < 4; ++cb) {
                const u32x2 w = *(const LAS u32x2*)(lds + L_B1 + cb * 32 + g4 * 8);
                const float bb[4] = {U2F(w[0] << 16), U2F(w[0] & 0xffff0000u), U2F(w[1] << 16), U2F(w[1] & 0xffff0000u)};
#pragma unroll
                for (int rt = 0; rt < 4; ++rt)
#pragma unroll
                    for (int i = 0; i < 4; ++i) { const float v = af[cb][rt][i] + bb[i];
                        const float a = v * __builtin_fmaf(v * v, -2.f * 0.7978845608028654f * 1.4426950408889634f * 0.044715f, -2.f * 0.7978845608028654f * 1.4426950408889634f);
                        af[cb][rt][i] = v * __builtin_amdgcn_rcpf(1.f + __builtin_amdgcn_exp2f(a)); }
#pragma unroll
                for (int rp = 0; rp < 2; ++rp) st1(join16(af[cb][2 * rp], af[cb][2 * rp + 1], 1.f, 1.f), rs, lane * 16, WS_H + (unsigned)((8 * t + 2 * f + rp) * 112 + 4 * hd + cb) * 1024u);
            }
        }
        drain();
        if (lds_arrive(sy + SY_F, lane) == 3 && lane == 0) __hip_atomic_store((GAS u64*)(ws + flag_at(1, hd, t)), tag, RLX_AGENT);
        STAMP(3);
        f32x16 acc[4] = {};
        poll_tags<4>(ws, hh ? flag_at(1, cp + 8 * (r32 & 3), r32 >> 2) : flag_at(1, r32, t), true, tag);
        *(LAS float*)(lds + g_at(64 * f + lane)) = __hip_atomic_load((GAS float*)(ws + WS_G + (256 * t + 64 * f + lane) * 4), RLX_AGENT);
        asm volatile("s_waitcnt lgkmcnt(0)" ::: "memory");
        if (lane == 0) __hip_atomic_fetch_add(sy + SY_FSEEN, 1u, RLX_WG);
        __builtin_amdgcn_s_setprio(F2_PRIO);
        STAMP(4);
        const unsigned hb = WS_H + (unsigned)((8 * t + 2 * rq) * 112 + 28 * f) * 1024u, wb = WS_W + (unsigned)(2 * cp * 144 + 28 * f) * 1024u;
        u32x4 fr[4][4];
#define LDK(slot, n) do { fr[slot][0] = ld1(rs, lane * 16, wb + (n) * 1024); fr[slot][1] = ld1(rs, lane * 16, wb + (144 + (n)) * 1024); \
                          fr[slot][2] = ld1(rs, lane * 16, hb + (n) * 1024); fr[slot][3] = ld1(rs, lane * 16, hb + (112 + (n)) * 1024); } while (0)
#define BC(v) __builtin_bit_cast(bf16x8, v)
#define MM4(w0, w1_, h0, h1) do { acc[0] = MFMA(w0, h0, acc[0]); acc[1] = MFMA(w0, h1, acc[1]); acc[2] = MFMA(w1_, h0, acc[2]); acc[3] = MFMA(w1_, h1, acc[3]); PIN(acc[0]); PIN(acc[1]); PIN(acc[2]); PIN(acc[3]); SBAR(); } while (0)
#define FFN2(n0, n1) do { _Pragma("unroll") for (int n = (n0); n < (n1); ++n) { MM4(BC(fr[n & 3][0]), BC(fr[n & 3][1]), BC(fr[n & 3][2]), BC(fr[n & 3][3])); if (n + 4 < (n1)) LDK(n & 3, n + 4); SBAR(); } } while (0)
#pragma unroll
        for (int n = 0; n < 4; ++n) LDK(n, n);
        FFN2(0, NP1);
        STAMP(5);
        { u32x4 tmp[16];
#pragma unroll
          for (int i = 0; i < 16; ++i) tmp[i] = ld1(rs, lane * 16, WS_W + (unsigned)((2 * cp + (i & 1)) * 144 + 112 + 8 * f + (i >> 1)) * 1024u);
          __builtin_amdgcn_s_setprio(2);
          lds_wait_ge<16>(sy + SY_PASS, 8);
#pragma unroll
          for (int i = 0; i < 16; ++i) *(LAS u32x4*)(lds + freg + i * 1024 + lane * 16) = tmp[i]; }
        STAMP(6);
        if (NP1 < 28) {
#pragma unroll
            for (int n = NP1; n < NP1 + 4; ++n) LDK(n & 3, n);
            lds_wait_ge<8>(sy + SY_TRIG, 8);
            __builtin_amdgcn_s_setprio(F2_PRIO);
            FFN2(NP1, 28);
        }
#undef LDK
#undef FFN2
        lds_wait_ge(sy + SY_FSEEN, 4);
        { const float og0 = bfround(1.f - *(const LAS float*)(lds + g_at(64 * rq + r32))), og1 = bfround(1.f - *(const LAS float*)(lds + g_at(64 * rq + 32 + r32)));
#pragma unroll
          for (int ct = 0; ct < 2; ++ct)
#pragma unroll
              for (int g = 0; g < 4; ++g) { u32x2 w = *(const LAS u32x2*)(lds + L_B2 + ct * 64 + g * 16 + hh * 8); if (f != 0) w = (u32x2){0u, 0u};
                  const float bb[4] = {U2F(w[0] << 16), U2F(w[0] & 0xffff0000u), U2F(w[1] << 16), U2F(w[1] & 0xffff0000u)};
#pragma unroll
                  for (int i = 0; i < 4; ++i) { acc[2 * ct][4 * g + i] = (acc[2 * ct][4 * g + i] + bb[i]) * og0; acc[2 * ct + 1][4 * g + i] = (acc[2 * ct + 1][4 * g + i] + bb[i]) * og1; } } }
        STAMP(7);
        __builtin_amdgcn_s_setprio(3);
        lds_wait_ge<2>(sy + SY_OL + f, 2);
        STAMP(10);
#pragma unroll
        for (int n = 0; n < 8; ++n) { const LAS char* p = lds + freg + n * 2048 + lane * 16;
            const bf16x8 w0 = ldsr(p), w1_ = ldsr(p + 1024), h0 = ldsr(p + 32768), h1 = ldsr(p + 32768 + 1024); MM4(w0, w1_, h0, h1); }
#undef MM4
#undef BC
        STAMP(8);
#pragma unroll
        for (int qd = 0; qd < 4; ++qd)
#pragma unroll
            for (int g = 0; g < 4; ++g) *(LAS f32x4*)(lds + freg + qd * 4096 + g * 1024 + lane * 16) = (f32x4){acc[qd][4 * g], acc[qd][4 * g + 1], acc[qd][4 * g + 2], acc[qd][4 * g + 3]};
        asm volatile("s_waitcnt lgkmcnt(0)" ::: "memory");
        if (lane == 0) __hip_atomic_fetch_add(sy + SY_RED, 1u, RLX_WG);
        lds_wait_ge<0>(sy + SY_RED, 4);
        unsigned pk[8];
#pragma unroll
        for (int g = 0; g < 4; ++g) { f32x4 s = *(const LAS f32x4*)(lds + f * 4096 + g * 1024 + lane * 16);
#pragma unroll
            for (int w = 1; w < 4; ++w) s += *(const LAS f32x4*)(lds + (w >> 1) * L_V + (w & 1) * 16384 + f * 4096 + g * 1024 + lane * 16);
            pk[2 * g] = pack2(s[0], s[1]); pk[2 * g + 1] = pack2(s[2], s[3]); }
        unsigned short* orow = out + (size_t)(256 * t + 64 * rq + 32 * (f & 1) + r32) * DM + 64 * cp + 32 * (f >> 1) + 8 * hh;
#pragma unroll
        for (int k = 0; k < 2; ++k) {
            auto a = __builtin_amdgcn_permlane32_swap(pk[4 * k], pk[4 * k + 2], false, false), b = __builtin_amdgcn_permlane32_swap(pk[4 * k + 1], pk[4 * k + 3], false, false);
            *(u32x4*)(orow + 16 * k) = (u32x4){a[0], b[0], a[1], b[1]};
        }
        if (bx == 0 && f == 0 && lane == 0) __hip_atomic_store((GAS u64*)(ws + WS_FLAGS), tag, RLX_AGENT);
        STAMP(9); STAMP_FLUSH();
    }
}

extern "C" void kernel_launch(void* const* d_in, const int* in_sizes, int n_in, void* d_out, int out_size, void* d_ws, size_t ws_size, hipStream_t stream) {
    (void)in_sizes; (void)n_in; (void)out_size; (void)ws_size;
    hipLaunchKernelGGL(gated_mix_fused, dim3(256), dim3(768), 0, stream, (const unsigned short*)d_in[0], (const unsigned short*)d_in[1], (const unsigned short*)d_in[2],
                       (const unsigned short*)d_in[4], (const unsigned short*)d_in[5], (const unsigned short*)d_in[6], (const unsigned short*)d_in[7], (const unsigned short*)d_in[8],
                       (unsigned short*)d_out, (char*)d_ws);
}
```

```cpp
#include <hip/hip_runtime.h>
#include <hip/hip_bf16.h>
#include <stdint.h>

typedef __attribute__((ext_vector_type(8))) __bf16 bf16x8;
typedef __attribute__((ext_vector_type(16))) __bf16 bf16x16;
typedef __attribute__((ext_vector_type(2))) __bf16 bf16x2;
typedef __attribute__((ext_vector_type(4))) short s16x4;
typedef __attribute__((ext_vector_type(16))) float f32x16;
typedef __attribute__((ext_vector_type(8))) float f32x8;
typedef __attribute__((ext_vector_type(4))) float f32x4;
typedef __attribute__((ext_vector_type(2))) float f32x2;
typedef __attribute__((ext_vector_type(4))) unsigned u32x4;
typedef __attribute__((ext_vector_type(2))) unsigned u32x2;
typedef unsigned long long u64;
#define LAS __attribute__((address_space(3)))
#define GAS __attribute__((address_space(1)))
#if defined(__HIP_DEVICE_COMPILE__) && !defined(ALLOW_PK)
#define NOPK __attribute__((target("no-packed-fp32-ops")))
#else
#define NOPK
#endif
#define DI __device__ __forceinline__ NOPK
#define U2F(x) __builtin_bit_cast(float, (unsigned)(x))
#define F2U(x) __builtin_bit_cast(unsigned, (float)(x))
#define RLX_AGENT __ATOMIC_RELAXED, __HIP_MEMORY_SCOPE_AGENT
#define RLX_WG __ATOMIC_RELAXED, __HIP_MEMORY_SCOPE_WORKGROUP

constexpr int DM = 512, DFF = 1792;
constexpr unsigned WS_FLAGS = 0;
constexpr unsigned WS_FLAG0 = 4096, FLAG_STRIDE = 128;
constexpr unsigned WS_G = 106496;
constexpr unsigned WS_KV = 131072;
constexpr unsigned WS_H = WS_KV + 32u * 8u * 16384u;
constexpr unsigned WS_O = WS_H + 64u * 112u * 1024u;
constexpr unsigned WS_W = WS_O + 64u * 32u * 1024u;
constexpr unsigned WS_END = WS_W + 16u * 144u * 1024u;
constexpr unsigned WS_DIAG = WS_END;
constexpr int XB = 32768, BUFB = XB + 6144 + 8192;
constexpr int L_V = 65536;
constexpr int L_GATE = 3 * BUFB;
constexpr int L_RT = L_GATE + 4096;
constexpr int L_MISC = 163840 - 2048;
constexpr int L_DUMMY = L_RT + 10240;
constexpr int L_B1 = L_MISC + 272, L_B2 = L_MISC + 512 + 272, L_SYNC = L_MISC + 1024 + 272;
static_assert(L_DUMMY + 1024 <= L_MISC, "lds");
enum { SY_READY = 0  , SY_KV = 8, SY_DONE = 9, SY_F = 10, SY_PASS = 11, SY_RED = 12, SY_TRIG = 13, SY_FSEEN = 14, SY_AW = 15, SY_OL = 16  , SY_FA = 20, SY_PROG = 24  , SY_N = 32 };
constexpr unsigned SENT = 0xFFFFFFFFu;
constexpr unsigned SPIN_CAP = 1u << 18;

#define MFMA(a, b, c) __builtin_amdgcn_mfma_f32_32x32x16_bf16((a), (b), (c), 0, 0, 0)
#define MFMA16(a, b, c) __builtin_amdgcn_mfma_f32_16x16x32_bf16((a), (b), (c), 0, 0, 0)
#define SBAR() __builtin_amdgcn_sched_barrier(0)
#define CFENCE() asm volatile("" ::: "memory")
#define PIN(x) asm volatile("" : "+v"(x))

#define F2_PRIO 0
#define DSPLIT 4
#define FPA 0
#define TPF 0
#define TSLP 8
#define TLA 4
#define RTC 3
#define CPF 1
#define LEAP 1
#define PA_F 1
#define NP1 20
#define TRIGJ 5
#define STAMP(i) do {} while (0)
#define STAMP_FLUSH() do {} while (0)

DI unsigned pack2(float lo, float hi) { f32x2 v = {lo, hi}; return __builtin_bit_cast(unsigned, __builtin_convertvector(v, bf16x2)); }
DI u32x4 pack8(const f32x16& a, int o, float s) {
    u32x4 r; r[0] = pack2(a[o] * s, a[o + 1] * s); r[1] = pack2(a[o + 2] * s, a[o + 3] * s); r[2] = pack2(a[o + 4] * s, a[o + 5] * s); r[3] = pack2(a[o + 6] * s, a[o + 7] * s); return r; }
DI u32x4 pack8(const f32x16& a, int o) {
    u32x4 r; r[0] = pack2(a[o], a[o + 1]); r[1] = pack2(a[o + 2], a[o + 3]); r[2] = pack2(a[o + 4], a[o + 5]); r[3] = pack2(a[o + 6], a[o + 7]); return r; }
DI float bfround(float v) { return (float)(__bf16)v; }
DI float bf2f(unsigned short u) { return U2F((unsigned)u << 16); }
DI bf16x8 ldsr(const LAS char* p) { return *(const LAS bf16x8*)p; }
DI bf16x8 ldtr(const LAS char* p0, const LAS char* p1) {
    s16x4 a = __builtin_amdgcn_ds_read_tr16_b64_v4i16((LAS s16x4*)p0), b = __builtin_amdgcn_ds_read_tr16_b64_v4i16((LAS s16x4*)p1);
    return __builtin_bit_cast(bf16x8, __builtin_shufflevector(a, b, 0, 1, 2, 3, 4, 5, 6, 7)); }
DI void glds16(unsigned voff, const void* sbase, unsigned lds) {
    unsigned keep; asm volatile("s_mov_b32 %0, m0\n\ts_mov_b32 m0, %3\n\ts_nop 0\n\tglobal_load_lds_dwordx4 %1, %2\n\ts_mov_b32 m0, %0" : "=&s"(keep) : "v"(voff), "s"(sbase), "s"(lds) : "memory"); }
DI void glds4(unsigned voff, const void* sbase, unsigned lds) {
    unsigned keep; asm volatile("s_mov_b32 %0, m0\n\ts_mov_b32 m0, %3\n\ts_nop 0\n\tglobal_load_lds_dword %1, %2\n\ts_mov_b32 m0, %0" : "=&s"(keep) : "v"(voff), "s"(sbase), "s"(lds) : "memory"); }
DI u32x4 ld1(__amdgpu_buffer_rsrc_t rs, unsigned voff, unsigned soff) { return __builtin_amdgcn_raw_buffer_load_b128(rs, voff, soff, 16); }
DI void st1(u32x4 v, __amdgpu_buffer_rsrc_t rs, unsigned voff, unsigned soff) {
    __builtin_amdgcn_raw_buffer_store_b128(v, rs, voff, soff, 16); asm volatile("s_nop 1" :: "v"(v) : "memory"); }
DI void drain() { asm volatile("s_waitcnt vmcnt(0)" ::: "memory"); }
DI unsigned lds_arrive(LAS unsigned* w, int lane) {
    unsigned old = 0; if (lane == 0) old = __hip_atomic_fetch_add(w, 1u, RLX_WG); return __builtin_amdgcn_readfirstlane(old); }
template <int SL = 1> DI void lds_wait_ge(LAS unsigned* w, unsigned want) {
#pragma nounroll
    for (unsigned s = 0; s < (SPIN_CAP << 4); ++s) { if ((unsigned)__builtin_amdgcn_readfirstlane(__hip_atomic_load(w, RLX_WG)) >= want) break; __builtin_amdgcn_s_sleep(SL); }
    CFENCE(); }
template <int SL = 1> DI void poll_tags(const char* ws, unsigned flag_off, bool active, u64 tag) {
#pragma nounroll
    for (unsigned s = 0; s < SPIN_CAP; ++s) {
        u64 v = tag; if (active) v = __hip_atomic_load((GAS u64*)(ws + flag_off), RLX_AGENT);
        if (__builtin_amdgcn_ballot_w64(v != tag) == 0) break; __builtin_amdgcn_s_sleep(SL); }
    __builtin_amdgcn_fence(__ATOMIC_ACQUIRE, "wavefront"); CFENCE(); }
DI unsigned flag_at(int kind, int hd, int t) { return WS_FLAG0 + (unsigned)(kind * 256 + hd * 8 + t) * FLAG_STRIDE; }
DI int g_at(int i) { return L_MISC + (i >> 6) * 512 + 16 + (i & 63) * 4; }
DI u32x4 join16(const f32x4& a, const f32x4& b, float sa, float sb) {
    auto r0 = __builtin_amdgcn_permlane16_swap(pack2(a[0] * sa, a[1] * sa), pack2(b[0] * sb, b[1] * sb), false, false);
    auto r1 = __builtin_amdgcn_permlane16_swap(pack2(a[2] * sa, a[3] * sa), pack2(b[2] * sb, b[3] * sb), false, false);
    return (u32x4){r0[0], r1[0], r0[1], r1[1]}; }
DI f32x16 exp16(f32x16 s) {
#pragma unroll
    for (int i = 0; i < 16; ++i) s[i] = __builtin_amdgcn_exp2f(s[i]);
    return s; }
DI f32x16 pv(bf16x8 v0, bf16x8 v1, const f32x16& p, f32x16 o, int idx) {
    const f32x8 lo = __builtin_shufflevector(p, p, 0, 1, 2, 3, 4, 5, 6, 7), hi = __builtin_shufflevector(p, p, 8, 9, 10, 11, 12, 13, 14, 15);
    o = __builtin_amdgcn_smfmac_f32_32x32x32_bf16(v0, __builtin_bit_cast(bf16x16, lo), o, idx, 0, 0);
    o = __builtin_amdgcn_smfmac_f32_32x32x32_bf16(v1, __builtin_bit_cast(bf16x16, hi), o, idx, 0, 0);
    return o; }
DI float rowmax16(const f32x16& s) {
    float m = __builtin_fmaxf(__builtin_fmaxf(s[0], s[1]), s[2]);
#pragma unroll
    for (int i = 3; i < 15; i += 2) m = __builtin_fmaxf(__builtin_fmaxf(m, s[i]), s[i + 1]);
    return __builtin_fmaxf(m, s[15]); }
DI float xhalf_max(float m) { auto r = __builtin_amdgcn_permlane32_swap(F2U(m), F2U(m), false, false); return __builtin_fmaxf(U2F(r[0]), U2F(r[1])); }

__global__ __launch_bounds__(768, 3) NOPK void gated_mix_fused(const unsigned short* __restrict__ x, const unsigned short* __restrict__ wqkv, const unsigned short* __restrict__ wo,
                                                         const unsigned short* __restrict__ w1, const unsigned short* __restrict__ b1, const unsigned short* __restrict__ w2,
                                                         const unsigned short* __restrict__ b2, const unsigned short* __restrict__ gate_w, unsigned short* __restrict__ out, char* ws) {
    __shared__ __attribute__((aligned(1024))) char smem[163840];
    LAS char* lds = (LAS char*)smem;
    const unsigned lds0 = (unsigned)(uintptr_t)lds;
    const int lane = __builtin_amdgcn_workitem_id_x() & 63, wid = __builtin_amdgcn_readfirstlane(__builtin_amdgcn_workitem_id_x() >> 6), r32 = lane & 31, hh = lane >> 5;
    const int bx = __builtin_amdgcn_workgroup_id_x(), hd = 8 * (bx & 3) + ((bx >> 3) & 7), t = 4 * ((bx >> 2) & 1) + (bx >> 6);
    const __amdgpu_buffer_rsrc_t rs = __builtin_amdgcn_make_buffer_rsrc(ws, 0, WS_END, 0x00020000);
    LAS unsigned* sy = (LAS unsigned*)(lds + L_SYNC);
    asm volatile("" :: "s"(x), "s"(wqkv), "s"(wo), "s"(w1), "s"(b1), "s"(w2), "s"(b2), "s"(gate_w), "s"(out), "s"(ws));
    u64 tag; u32x2 ep;
#define EPOCH_LOAD() asm volatile("global_load_dwordx2 %0, %1, %2" : "=v"(ep) : "v"(0), "s"(ws) : "memory")
#define EPOCH_TAKE() do { PIN(ep); tag = (((u64)(unsigned)__builtin_amdgcn_readfirstlane(ep[1]) << 32) | (unsigned)__builtin_amdgcn_readfirstlane(ep[0])) + 1ull; } while (0)
    const int cp = hd & 7, rq = hd >> 3;
    const int g4 = lane >> 4, r16 = lane & 15, q4 = r16 >> 2, p4 = lane & 3;
    const int xsw = g4 ^ (r16 >> 1);
    STAMP(0);
#define BUFOF(c) ((((c) + 1) % 3) * BUFB)
#define CHUNK_SYNC(c, n) do { if ((c) < 7) asm volatile("s_waitcnt vmcnt(" #n ") lgkmcnt(0)\n\ts_barrier" ::: "memory"); else asm volatile("s_waitcnt vmcnt(0) lgkmcnt(0)\n\ts_barrier" ::: "memory"); } while (0)

    if (wid < 8) {
        f32x4 aq[2] = {}, ak[2] = {}, av[2] = {};
        { const int c0 = (lane & 7) ^ (lane >> 4), rho = lane >> 1, r8 = lane >> 3;
          const unsigned dvo0 = r8 * 1024 + c0 * 16, dvo1 = r8 * 1024 + (c0 ^ 4) * 16;
          const unsigned dvq = (8 * ((rho >> 2) & 3) + 4 * (rho >> 4) + (rho & 3)) * 3072 + (lane & 1) * 16;
          const unsigned dvf = (8 * (r8 >> 2) + (r8 & 3)) * 3584 + (((lane & 7) ^ (r8 & 6)) * 16);
          const char* xbase = (const char*)x + (size_t)(256 * t + 32 * wid) * 1024;
          const bool hq = wid < 6, hf = hd < 28;
          const char* qbase = (const char*)wqkv + hd * 32 + (hq ? (wid >> 1) * 1024 + (wid & 1) * 32 * 3072 : 0);
          const char* fbase = (const char*)w1 + (hf ? hd * 128 : 0) + (32 * (wid >> 2) + 16 * (wid & 1) + 4 * ((wid >> 1) & 1)) * 3584;
          const unsigned vq_ = hq ? dvq : 0u, vf_ = hf ? dvf : 0u;
#define DMA_X(c) do { _Pragma("unroll") for (int i = 0; i < 4; ++i) glds16((i & 1) ? dvo1 : dvo0, xbase + (c) * 128 + i * 8192, lds0 + BUFOF(c) + wid * 4096 + i * 1024); } while (0)
#define DMA_ALL(c) do { glds16(vq_, qbase + (c) * 64 * 3072, hq ? lds0 + BUFOF(c) + XB + wid * 1024 : lds0 + L_DUMMY); \
              glds16(vf_, fbase + (c) * 64 * 3584, hf ? lds0 + BUFOF(c) + XB + 6144 + wid * 1024 : lds0 + L_DUMMY); DMA_X(c); } while (0)
          DMA_ALL(0);
          EPOCH_LOAD();
          if (wid == 0) {
              if (lane < SY_N) sy[lane] = 0;
              if (lane < 8) { const unsigned one = lane < 4 ? 0x3F803F80u : 0u; *(LAS u32x4*)(lds + L_MISC + (lane & 3) * 512 + (lane >> 2) * 496) = (u32x4){one, one, one, one}; }
          }
          __builtin_amdgcn_s_sleep(4);
          DMA_ALL(1);
          STAMP(20);
          const int xo = (32 * wid + r16) * 128, wo_ = XB + (4 * g4 + q4) * 32 + p4 * 8;
          bf16x8 xa[2], xb[2], wq[2], wk[2], wv[2];
#define RX(d, c, ks, rt) d[ks] = ldsr(lds + BUFOF(c) + xo + (rt) * 2048 + ((xsw ^ (4 * (ks))) * 16))
#define RW(d, c, ks, im) d[ks] = ldtr(lds + BUFOF(c) + wo_ + (im) * 2048 + (ks) * 1024, lds + BUFOF(c) + wo_ + (im) * 2048 + (ks) * 1024 + 512)
#define M2(A, W, ks) do { A[0] = MFMA16(W[ks], xa[ks], A[0]); A[1] = MFMA16(W[ks], xb[ks], A[1]); PIN(A[0]); PIN(A[1]); SBAR(); } while (0)
#define MV(ks) do { av[0] = MFMA16(xa[ks], wv[ks], av[0]); av[1] = MFMA16(xb[ks], wv[ks], av[1]); PIN(av[0]); PIN(av[1]); SBAR(); } while (0)
#pragma unroll
          for (int c = 0; c < 8; ++c) {
              CHUNK_SYNC(c, 6);
              STAMP(12 + c);
              if (c == 7) { EPOCH_TAKE(); __builtin_amdgcn_s_setprio(3); }
              if (c + 2 < 8 && wid >= DSPLIT) DMA_ALL(c + 2);
              if (c > 0) M2(aq, wq, 1);
              RX(xa, c, 0, 0); RX(xb, c, 0, 1); RW(wq, c, 0, 0); SBAR();
              if (c > 0) M2(ak, wk, 1);
              RW(wk, c, 0, 1); RW(wv, c, 0, 2); SBAR();
              if (c > 0) MV(1);
              RX(xa, c, 1, 0); RX(xb, c, 1, 1); RW(wq, c, 1, 0); SBAR();
              M2(aq, wq, 0);
              RW(wk, c, 1, 1); RW(wv, c, 1, 2); SBAR();
              M2(ak, wk, 0);
              MV(0);
              if (c + 2 < 8 && wid < DSPLIT) DMA_ALL(c + 2);
          }
          M2(aq, wq, 1); M2(ak, wk, 1); MV(1);
#undef RX
#undef RW
#undef M2
#undef MV
#undef DMA_ALL
#undef DMA_X
        }
        STAMP(1);
        constexpr float QS = 0.25f * 1.4426950408889634f;
        const bf16x8 qf = __builtin_bit_cast(bf16x8, join16(aq[0], aq[1], QS, QS));
        const unsigned kvg = WS_KV + (unsigned)(hd * 8 + t) * 16384u, og = WS_O + (unsigned)((8 * t + wid) * 32 + hd) * 1024u;
        { const u32x4 kk = join16(ak[0], ak[1], 1.f, 1.f), vv = join16(av[0], av[1], 1.f, 1.f);
          const int ok = wid * 1024 + lane * 16, ov = wid * 1024 + (g4 & 1) * 512 + hh * 256 + r16 * 16;
          *(LAS u32x4*)(lds + ok) = kk; *(LAS u32x4*)(lds + L_V + ov) = vv;
          asm volatile("s_waitcnt lgkmcnt(0)" ::: "memory");
          if (lane == 0) __hip_atomic_fetch_add(sy + SY_AW, 1u, RLX_WG);
          st1(kk, rs, ok, kvg); st1(vv, rs, ov + 8192, kvg); }
        lds_wait_ge<1>(sy + SY_AW, 8);
        STAMP(2);
        drain();
        if (lds_arrive(sy + SY_KV, lane) == 7 && lane == 0) __hip_atomic_store((GAS u64*)(ws + flag_at(0, hd, t)), tag, RLX_AGENT);
        const int idx = 0xDDDD;
        const LAS char* kp = lds + lane * 16;
        const bool vl = r32 < 16;
        const LAS char* vp = lds + (vl ? L_V + 512 + hh * 256 + r32 * 16 : (r32 & 11) == 0 ? L_MISC : L_MISC + 496);
        const int vstep = vl ? 2048 : 0;
        f32x16 o = (f32x16){}, negm, sA, sB = (f32x16){};
        bf16x8 kf = ldsr(kp), v0 = ldsr(vl ? vp - 512 : vp), v1 = (bf16x8){};
        { const f32x16 s0 = MFMA(kf, qf, (f32x16){}); const float m0 = -xhalf_max(rowmax16(s0));
#pragma unroll
          for (int i = 0; i < 16; ++i) negm[i] = m0; }
        sA = MFMA(kf, qf, negm);
        kf = ldsr(kp + 1024);
        u32x4 stg[8];
        const unsigned rkv = WS_KV + (unsigned)(hd * 8 + ((t + wid) & 7)) * 16384u;
        if (wid > 0) {
            poll_tags(ws, flag_at(0, hd, (t + wid) & 7), lane == 0, tag);
#pragma unroll
            for (int i = 0; i < 8; ++i) stg[i] = ld1(rs, lane * 16 + i * 1024, rkv);
        }
        STAMP(10);
        __builtin_amdgcn_s_setprio(0);
#define EX(S, a, b) do { _Pragma("unroll") for (int i_ = (a); i_ < (b); ++i_) S[i_] = __builtin_amdgcn_exp2f(S[i_]); PIN(S); SBAR(); } while (0)
#define LO8(S) __builtin_bit_cast(bf16x16, __builtin_shufflevector(S, S, 0, 1, 2, 3, 4, 5, 6, 7))
#define HI8(S) __builtin_bit_cast(bf16x16, __builtin_shufflevector(S, S, 8, 9, 10, 11, 12, 13, 14, 15))
#define SM(a, b) do { o = __builtin_amdgcn_smfmac_f32_32x32x32_bf16(a, b, o, idx, 0, 0); PIN(o); SBAR(); } while (0)
#define TILE(CUR, OTH, KO, VO) do { SBAR(); EX(CUR, 0, 2); SM(v1, HI8(OTH)); v1 = ldsr(vp + (VO)); SBAR(); EX(CUR, 2, 6); \
            OTH = MFMA(kf, qf, negm); PIN(OTH); SBAR(); kf = ldsr(kp + (KO)); SBAR(); EX(CUR, 6, 10); SM(v0, LO8(CUR)); v0 = ldsr(vp + (VO) + 512); SBAR(); EX(CUR, 10, 16); } while (0)
#define PAIR(KO) do { TILE(sA, sB, (KO) + 2048, 0); TILE(sB, sA, (KO) + 3072, 1024); vp += vstep; } while (0)
        PAIR(0);
        if (wid > 0) {
            lds_wait_ge(sy + SY_FA, 4);
#pragma unroll
            for (int i = 0; i < 8; ++i) { *(LAS u32x4*)(lds + wid * 8192 + i * 1024 + lane * 16) = stg[i]; stg[i] = ld1(rs, lane * 16 + i * 1024, rkv + 8192); }
        }
        PAIR(2048);
        if (wid > 0) {
#pragma unroll
            for (int i = 0; i < 8; ++i) *(LAS u32x4*)(lds + L_V + wid * 8192 + i * 1024 + lane * 16) = stg[i];
            asm volatile("s_waitcnt lgkmcnt(0)" ::: "memory");
            if (lane == 0) __hip_atomic_store(sy + SY_READY + wid, 1u, RLX_WG);
        }
        PAIR(4096);
        lds_wait_ge(sy + SY_READY + 1, 1);
        PAIR(6144);
        STAMP(3);
        for (int j = 1; j < 8; ++j) {
            kp += 8192;
            if (j > 1 && j < 6) STAMP(4 + j);
            if (LEAP) {
                if (lane == 0) __hip_atomic_store(sy + SY_PROG + wid, (unsigned)j, RLX_WG);
                if ((unsigned)__builtin_amdgcn_readfirstlane(__hip_atomic_load(sy + SY_PROG + (wid ^ 4), RLX_WG)) >= (unsigned)j) __builtin_amdgcn_s_setprio(1); else __builtin_amdgcn_s_setprio(0);
            }
            PAIR(0); PAIR(2048); PAIR(4096);
            if (j < 7) lds_wait_ge(sy + SY_READY + j + 1, 1);
            PAIR(6144);
            if (j == 4 && lane == 0) __hip_atomic_fetch_add(sy + SY_PASS, 1u, RLX_WG);
            if (j == TRIGJ && lane == 0) __hip_atomic_fetch_add(sy + SY_TRIG, 1u, RLX_WG);
        }
        SM(v1, HI8(sB));
#undef PAIR
#undef TILE
#undef SM
#undef EX
        asm volatile("s_waitcnt lgkmcnt(0)" ::: "memory");
        if (lane == 0) __hip_atomic_fetch_add(sy + SY_DONE, 1u, RLX_WG);
        STAMP(4);
        { float chk = o[8] * 0.f;
#pragma unroll
          for (int i = 0; i < 8; ++i) chk = __builtin_fmaf(o[i], 0.f, chk);
          if (__builtin_expect(0 != __builtin_amdgcn_ballot_w64(!(chk == 0.f) || o[8] > 1.8e19f), 0)) {
              float m = -__builtin_inff();
#pragma nounroll
              for (int tl = 0; tl < 64; ++tl) { const bf16x8 kg = __builtin_bit_cast(bf16x8, ld1(rs, lane * 16, WS_KV + (unsigned)(hd * 8 + (tl >> 3)) * 16384u + (tl & 7) * 1024));
                  m = __builtin_fmaxf(m, rowmax16(MFMA(kg, qf, (f32x16){}))); }
              m = -xhalf_max(m);
#pragma unroll
              for (int i = 0; i < 16; ++i) negm[i] = m;
              o = (f32x16){};
              const u32x4 ones = {0x3F803F80u, 0x3F803F80u, 0x3F803F80u, 0x3F803F80u};
#pragma nounroll
              for (int tl = 0; tl < 64; ++tl) { const unsigned so = WS_KV + (unsigned)(hd * 8 + (tl >> 3)) * 16384u + (tl & 7) * 1024;
                  const bf16x8 kg = __builtin_bit_cast(bf16x8, ld1(rs, lane * 16, so));
                  const u32x4 a0 = ld1(rs, 8192 + hh * 256 + r16 * 16, so), a1 = ld1(rs, 8192 + 512 + hh * 256 + r16 * 16, so);
                  o = pv(__builtin_bit_cast(bf16x8, vl ? a0 : ones), __builtin_bit_cast(bf16x8, vl ? a1 : ones), exp16(MFMA(kg, qf, negm)), o, idx); }
          } }
        lds_wait_ge(sy + SY_FSEEN, 4);
        { u32x4 ov = pack8(o, 0, *(const LAS float*)(lds + g_at(32 * wid + r32)) * __builtin_amdgcn_rcpf(o[8]));
#pragma unroll
          for (int i = 0; i < 4; ++i) ov[i] = ov[i] == SENT ? 0x7FFF7FFFu : ov[i];
          st1(ov, rs, lane * 16, og); }
        STAMP(5);
        { u32x4 of[8];
          const unsigned ob = WS_O + (unsigned)((8 * t + 2 * rq) * 32 + 4 * wid) * 1024u;
#pragma nounroll
          for (unsigned s = 0; s < SPIN_CAP; ++s) {
#pragma unroll
              for (int i = 0; i < 8; ++i) of[i] = ld1(rs, lane * 16, ob + ((i & 1) * 32 + (i >> 1)) * 1024);
              unsigned mx = 0;
#pragma unroll
              for (int i = 0; i < 8; ++i) mx = __builtin_elementwise_max(__builtin_elementwise_max(mx, __builtin_elementwise_max(of[i][0], of[i][1])), __builtin_elementwise_max(of[i][2], of[i][3]));
              if (__builtin_amdgcn_ballot_w64(mx == SENT) == 0) break;
              __builtin_amdgcn_s_sleep(2); CFENCE();
          }
          STAMP(11);
          lds_wait_ge(sy + SY_DONE, 8);
#pragma unroll
          for (int i = 0; i < 8; ++i) *(LAS u32x4*)(lds + (wid >> 2) * L_V + 32768 + (wid & 3) * 8192 + i * 1024 + lane * 16) = of[i];
          asm volatile("s_waitcnt lgkmcnt(0)" ::: "memory");
          if (lane == 0) __hip_atomic_fetch_add(sy + SY_OL + (wid >> 1), 1u, RLX_WG); }
        asm volatile(".Lgm_end:");
        STAMP(21); STAMP_FLUSH();
    } else {
        const int f = wid - 8, i32 = rq + 4 * t;
        const bool hasF = hd < (PA_F ? 28 : 0);
        f32x4 af[4][4] = {};
        { if (FPA) __builtin_amdgcn_s_setprio(1);
          unsigned d0 = 0;
          EPOCH_LOAD();
          const int l6 = ((lane - 48) * 43) >> 8, m6 = lane - 48 - 6 * l6;
          const char* tp = lane < 32 ? (const char*)x + (size_t)(256 * t + 32 * cp + lane) * 1024 : lane < 48 ? (const char*)w1 + (hasF ? hd : 0) * 128 + (16 * (t & 3) + lane - 32) * 3584
                                     : (const char*)wqkv + (hd & 24) * 32 + (2 * (cp + 8 * (t & 3)) + (l6 & 1)) * 3072 + (m6 >> 1) * 1024 + (m6 & 1) * 128;
          const unsigned tstep = lane < 32 ? 128 : lane < 48 ? 64 * 3584 : 64 * 3072;
#define TOUCH(c) do { if (TPF && lane < 60) asm volatile("global_load_dword %0, %1, off" : "+v"(d0) : "v"(tp + (c) * tstep) : "memory"); } while (0)
          unsigned d1 = 0;
          if (CPF && f == 3) {
              const char* pc; unsigned len;
              asm volatile("s_getpc_b64 %0\n.Lgm_ref:\n\ts_mov_b32 %1, .Lgm_end-.Lgm_ref" : "=s"(pc), "=s"(len));
              const unsigned po = (unsigned)((bx >> 3) + 32 * lane) * 128u;
              if ((int)po < (int)len) asm volatile("global_load_dword %0, %1, %2" : "+v"(d1) : "v"(po), "s"(pc) : "memory");
          }
          if (hd == 28) { glds16(lane * 16, gate_w, lds0 + L_GATE + f * 1024); asm volatile("s_waitcnt vmcnt(0)" ::: "memory"); }
          __builtin_amdgcn_s_sleep(TSLP);
          if (TPF == 2) { TOUCH(2 + f); if (f < 2) TOUCH(6 + f); } else if (f < TLA - 2) TOUCH(2 + f);
          const int xo = (64 * f + r16) * 128, fo = XB + 6144 + (4 * g4 + q4) * 128 + p4 * 8, fsw = (2 * g4 + (q4 >> 1)) & 3;
          bf16x8 xf[2][4], wf[2][4];
#define RX(c, ks, rt) xf[ks][rt] = ldsr(lds + BUFOF(c) + xo + (rt) * 2048 + ((xsw ^ (4 * (ks))) * 16))
#define RW(c, ks, cb) wf[ks][cb] = ldtr(lds + BUFOF(c) + fo + (ks) * 4096 + (((cb) ^ fsw) * 32), lds + BUFOF(c) + fo + (ks) * 4096 + 2048 + (((cb) ^ fsw) * 32))
#define M2(ks, cb, r0) do { af[cb][r0] = MFMA16(wf[ks][cb], xf[ks][r0], af[cb][r0]); af[cb][r0 + 1] = MFMA16(wf[ks][cb], xf[ks][r0 + 1], af[cb][r0 + 1]); PIN(af[cb][r0]); PIN(af[cb][r0 + 1]); SBAR(); } while (0)
#define F_SYNC(c) do { if ((c) < 7) asm volatile("s_waitcnt lgkmcnt(0)\n\ts_barrier" ::: "memory"); else CHUNK_SYNC(c, 0); \
              STAMP(12 + (c)); \
              if ((c) == 4) {     \
                  _Pragma("unroll") for (int i = 0; i < 2; ++i) st1((u32x4){SENT, SENT, SENT, SENT}, rs, lane * 16, WS_O + (unsigned)((8 * t + 2 * f + i) * 32 + hd) * 1024u); } \
              if (TPF == 1 && (c) + TLA < 8 && f == ((c) & 3)) TOUCH((c) + TLA); \
              if ((c) == RTC) {   \
                  const int u = i32 + 32 * f; const char* src = u < 112 ? (const char*)w2 + (size_t)u * 16384 : (const char*)wo + (size_t)(u - 112) * 16384; \
                  const unsigned vo = (lane >> 3) * 1024 + cp * 128 + (lane & 7) * 16; \
                  glds16(vo, src, lds0 + L_RT + f * 2048); glds16(vo, src + 8192, lds0 + L_RT + f * 2048 + 1024); \
                  if (f == 0 && i32 < 16) { const char* s2 = (const char*)wo + (size_t)(i32 + 16) * 16384; glds16(vo, s2, lds0 + L_RT + 8192); glds16(vo, s2 + 8192, lds0 + L_RT + 8192 + 1024); } \
                  if (f == 1 && lane < 32) { if (hasF) glds4(lane * 4, (const char*)b1 + hd * 128, lds0 + L_B1); glds4(lane * 4, (const char*)b2 + cp * 128, lds0 + L_B2); } } \
              if ((c) == 7) { EPOCH_TAKE(); PIN(d0); PIN(d1); } } while (0)
          if (hasF) {
#pragma unroll
              for (int c = 0; c < 8; ++c) {
                  F_SYNC(c);
                  if (c > 0) M2(1, 0, 0);
                  RX(c, 0, 0); SBAR();
                  if (c > 0) M2(1, 0, 2);
                  RW(c, 0, 0); SBAR();
                  if (c > 0) M2(1, 1, 0);
                  RX(c, 0, 1); SBAR();
                  if (c > 0) M2(1, 1, 2);
                  RX(c, 0, 2); SBAR();
                  if (c > 0) M2(1, 2, 0);
                  RX(c, 0, 3); SBAR();
                  if (c > 0) M2(1, 2, 2);
                  RW(c, 0, 1); SBAR();
                  if (c > 0) M2(1, 3, 0);
                  RW(c, 0, 2); SBAR();
                  if (c > 0) M2(1, 3, 2);
                  RW(c, 0, 3); SBAR();
                  M2(0, 0, 0); RX(c, 1, 0); RW(c, 1, 0); SBAR();
                  M2(0, 0, 2); RX(c, 1, 1); RX(c, 1, 2); SBAR();
                  M2(0, 1, 0); RX(c, 1, 3); RW(c, 1, 1); SBAR();
                  M2(0, 1, 2); RW(c, 1, 2); RW(c, 1, 3); SBAR();
                  M2(0, 2, 0); M2(0, 2, 2); M2(0, 3, 0); M2(0, 3, 2);
              }
              M2(1, 0, 0); M2(1, 0, 2); M2(1, 1, 0); M2(1, 1, 2); M2(1, 2, 0); M2(1, 2, 2); M2(1, 3, 0); M2(1, 3, 2);
          } else {
#pragma unroll
              for (int c = 0; c < 8; ++c) {
                  F_SYNC(c);
                  if (hd == 28) {
#pragma unroll
                      for (int ks = 0; ks < 2; ++ks) { const bf16x8 gf = ldsr(lds + L_GATE + f * 1024 + c * 128 + ks * 64 + g4 * 16);
#pragma unroll
                          for (int rt = 0; rt < 4; ++rt) { RX(c, ks, rt); af[0][rt] = MFMA16(gf, xf[ks][rt], af[0][rt]); } }
                  }
              }
          }
#undef F_SYNC
#undef RX
#undef RW
#undef M2
#undef TOUCH
        }
        STAMP(1);
        asm volatile("s_waitcnt lgkmcnt(0)" ::: "memory");
        if (lane == 0) __hip_atomic_fetch_add(sy + SY_FA, 1u, RLX_WG);
        STAMP(2);
        __builtin_amdgcn_s_setprio(2);
        const int freg = (f >> 1) * L_V + (f & 1) * 16384;
        for (int n = f; n < ((f == 0 && i32 < 16) ? 5 : 4); n += 4) {
            const int u = i32 + 32 * n, r0 = (u < 112 ? 8 : 4) * hh + q4, r1 = u < 112 ? 512 : 1024;
#pragma unroll
            for (int ct = 0; ct < 2; ++ct) { const LAS char* s = lds + L_RT + n * 2048 + r0 * 128 + (g4 & 1) * 32 + p4 * 8 + ct * 64;
                st1(__builtin_bit_cast(u32x4, ldtr(s, s + r1)), rs, lane * 16, WS_W + (unsigned)((2 * cp + ct) * 144 + u) * 1024u); }
        }
        if (hd == 28) {
            const float z = bfround(g4 == 0 ? af[0][0][0] : g4 == 1 ? af[0][1][0] : g4 == 2 ? af[0][2][0] : af[0][3][0]);
            __hip_atomic_store((GAS float*)(ws + WS_G + (256 * t + 64 * f + lane) * 4), bfround(__builtin_amdgcn_rcpf(1.f + __builtin_amdgcn_exp2f(-1.4426950408889634f * z))), RLX_AGENT);
        }
        if (hasF) {
#pragma unroll
            for (int cb = 0; cb < 4; ++cb) {
                const u32x2 w = *(const LAS u32x2*)(lds + L_B1 + cb * 32 + g4 * 8);
                const float bb[4] = {U2F(w[0] << 16), U2F(w[0] & 0xffff0000u), U2F(w[1] << 16), U2F(w[1] & 0xffff0000u)};
#pragma unroll
                for (int rt = 0; rt < 4; ++rt)
#pragma unroll
                    for (int i = 0; i < 4; ++i) { const float v = af[cb][rt][i] + bb[i];
                        const float a = v * __builtin_fmaf(v * v, -2.f * 0.7978845608028654f * 1.4426950408889634f * 0.044715f, -2.f * 0.7978845608028654f * 1.4426950408889634f);
                        af[cb][rt][i] = v * __builtin_amdgcn_rcpf(1.f + __builtin_amdgcn_exp2f(a)); }
#pragma unroll
                for (int rp = 0; rp < 2; ++rp) st1(join16(af[cb][2 * rp], af[cb][2 * rp + 1], 1.f, 1.f), rs, lane * 16, WS_H + (unsigned)((8 * t + 2 * f + rp) * 112 + 4 * hd + cb) * 1024u);
            }
        }
        drain();
        if (lds_arrive(sy + SY_F, lane) == 3 && lane == 0) __hip_atomic_store((GAS u64*)(ws + flag_at(1, hd, t)), tag, RLX_AGENT);
        STAMP(3);
        f32x16 acc[4] = {};
        poll_tags<4>(ws, hh ? flag_at(1, cp + 8 * (r32 & 3), r32 >> 2) : flag_at(1, r32, t), true, tag);
        *(LAS float*)(lds + g_at(64 * f + lane)) = __hip_atomic_load((GAS float*)(ws + WS_G + (256 * t + 64 * f + lane) * 4), RLX_AGENT);
        asm volatile("s_waitcnt lgkmcnt(0)" ::: "memory");
        if (lane == 0) __hip_atomic_fetch_add(sy + SY_FSEEN, 1u, RLX_WG);
        __builtin_amdgcn_s_setprio(F2_PRIO);
        STAMP(4);
        const unsigned hb = WS_H + (unsigned)((8 * t + 2 * rq) * 112 + 28 * f) * 1024u, wb = WS_W + (unsigned)(2 * cp * 144 + 28 * f) * 1024u;
        u32x4 fr[4][4];
#define LDK(slot, n) do { fr[slot][0] = ld1(rs, lane * 16, wb + (n) * 1024); fr[slot][1] = ld1(rs, lane * 16, wb + (144 + (n)) * 1024); \
                          fr[slot][2] = ld1(rs, lane * 16, hb + (n) * 1024); fr[slot][3] = ld1(rs, lane * 16, hb + (112 + (n)) * 1024); } while (0)
#define BC(v) __builtin_bit_cast(bf16x8, v)
#define MM4(w0, w1_, h0, h1) do { acc[0] = MFMA(w0, h0, acc[0]); acc[1] = MFMA(w0, h1, acc[1]); acc[2] = MFMA(w1_, h0, acc[2]); acc[3] = MFMA(w1_, h1, acc[3]); PIN(acc[0]); PIN(acc[1]); PIN(acc[2]); PIN(acc[3]); SBAR(); } while (0)
#define FFN2(n0, n1) do { _Pragma("unroll") for (int n = (n0); n < (n1); ++n) { MM4(BC(fr[n & 3][0]), BC(fr[n & 3][1]), BC(fr[n & 3][2]), BC(fr[n & 3][3])); if (n + 4 < (n1)) LDK(n & 3, n + 4); SBAR(); } } while (0)
#pragma unroll
        for (int n = 0; n < 4; ++n) LDK(n, n);
        FFN2(0, NP1);
        STAMP(5);
        { u32x4 tmp[16];
#pragma unroll
          for (int i = 0; i < 16; ++i) tmp[i] = ld1(rs, lane * 16, WS_W + (unsigned)((2 * cp + (i & 1)) * 144 + 112 + 8 * f + (i >> 1)) * 1024u);
          __builtin_amdgcn_s_setprio(2);
          lds_wait_ge<16>(sy + SY_PASS, 8);
#pragma unroll
          for (int i = 0; i < 16; ++i) *(LAS u32x4*)(lds + freg + i * 1024 + lane * 16) = tmp[i]; }
        STAMP(6);
        if (NP1 < 28) {
#pragma unroll
            for (int n = NP1; n < NP1 + 4; ++n) LDK(n & 3, n);
            lds_wait_ge<8>(sy + SY_TRIG, 8);
            __builtin_amdgcn_s_setprio(F2_PRIO);
            FFN2(NP1, 28);
        }
#undef LDK
#undef FFN2
        lds_wait_ge(sy + SY_FSEEN, 4);
        { const float og0 = bfround(1.f - *(const LAS float*)(lds + g_at(64 * rq + r32))), og1 = bfround(1.f - *(const LAS float*)(lds + g_at(64 * rq + 32 + r32)));
#pragma unroll
          for (int ct = 0; ct < 2; ++ct)
#pragma unroll
              for (int g = 0; g < 4; ++g) { u32x2 w = *(const LAS u32x2*)(lds + L_B2 + ct * 64 + g * 16 + hh * 8); if (f != 0) w = (u32x2){0u, 0u};
                  const float bb[4] = {U2F(w[0] << 16), U2F(w[0] & 0xffff0000u), U2F(w[1] << 16), U2F(w[1] & 0xffff0000u)};
#pragma unroll
                  for (int i = 0; i < 4; ++i) { acc[2 * ct][4 * g + i] = (acc[2 * ct][4 * g + i] + bb[i]) * og0; acc[2 * ct + 1][4 * g + i] = (acc[2 * ct + 1][4 * g + i] + bb[i]) * og1; } } }
        STAMP(7);
        __builtin_amdgcn_s_setprio(3);
        lds_wait_ge<2>(sy + SY_OL + f, 2);
        STAMP(10);
#pragma unroll
        for (int n = 0; n < 8; ++n) { const LAS char* p = lds + freg + n * 2048 + lane * 16;
            const bf16x8 w0 = ldsr(p), w1_ = ldsr(p + 1024), h0 = ldsr(p + 32768), h1 = ldsr(p + 32768 + 1024); MM4(w0, w1_, h0, h1); }
#undef MM4
#undef BC
        STAMP(8);
#pragma unroll
        for (int qd = 0; qd < 4; ++qd)
#pragma unroll
            for (int g = 0; g < 4; ++g) *(LAS f32x4*)(lds + freg + qd * 4096 + g * 1024 + lane * 16) = (f32x4){acc[qd][4 * g], acc[qd][4 * g + 1], acc[qd][4 * g + 2], acc[qd][4 * g + 3]};
        asm volatile("s_waitcnt lgkmcnt(0)" ::: "memory");
        if (lane == 0) __hip_atomic_fetch_add(sy + SY_RED, 1u, RLX_WG);
        lds_wait_ge<0>(sy + SY_RED, 4);
        unsigned pk[8];
#pragma unroll
        for (int g = 0; g < 4; ++g) { f32x4 s = *(const LAS f32x4*)(lds + f * 4096 + g * 1024 + lane * 16);
#pragma unroll
            for (int w = 1; w < 4; ++w) s += *(const LAS f32x4*)(lds + (w >> 1) * L_V + (w & 1) * 16384 + f * 4096 + g * 1024 + lane * 16);
            pk[2 * g] = pack2(s[0], s[1]); pk[2 * g + 1] = pack2(s[2], s[3]); }
        unsigned short* orow = out + (size_t)(256 * t + 64 * rq + 32 * (f & 1) + r32) * DM + 64 * cp + 32 * (f >> 1) + 8 * hh;
#pragma unroll
        for (int k = 0; k < 2; ++k) {
            auto a = __builtin_amdgcn_permlane32_swap(pk[4 * k], pk[4 * k + 2], false, false), b = __builtin_amdgcn_permlane32_swap(pk[4 * k + 1], pk[4 * k + 3], false, false);
            { const u32x4 v_ = (u32x4){a[0], b[0], a[1], b[1]}; asm volatile("global_store_dwordx4 %0, %1, off sc1\n\ts_nop 1" :: "v"(orow + 16 * k), "v"(v_) : "memory"); }
        }
        if (bx == 0 && f == 0 && lane == 0) __hip_atomic_store((GAS u64*)(ws + WS_FLAGS), tag, RLX_AGENT);
        STAMP(9); STAMP_FLUSH();
    }
}

extern "C" void kernel_launch(void* const* d_in, const int* in_sizes, int n_in, void* d_out, int out_size, void* d_ws, size_t ws_size, hipStream_t stream) {
    (void)in_sizes; (void)n_in; (void)out_size; (void)ws_size;
    hipLaunchKernelGGL(gated_mix_fused, dim3(256), dim3(768), 0, stream, (const unsigned short*)d_in[0], (const unsigned short*)d_in[1], (const unsigned short*)d_in[2],
                       (const unsigned short*)d_in[4], (const unsigned short*)d_in[5], (const unsigned short*)d_in[6], (const unsigned short*)d_in[7], (const unsigned short*)d_in[8],
                       (unsigned short*)d_out, (char*)d_ws);
}
```

```cpp
#include <hip/hip_runtime.h>
#include <hip/hip_bf16.h>
#include <stdint.h>

typedef __attribute__((ext_vector_type(8))) __bf16 bf16x8;
typedef __attribute__((ext_vector_type(16))) __bf16 bf16x16;
typedef __attribute__((ext_vector_type(2))) __bf16 bf16x2;
typedef __attribute__((ext_vector_type(4))) short s16x4;
typedef __attribute__((ext_vector_type(16))) float f32x16;
typedef __attribute__((ext_vector_type(8))) float f32x8;
typedef __attribute__((ext_vector_type(4))) float f32x4;
typedef __attribute__((ext_vector_type(2))) float f32x2;
typedef __attribute__((ext_vector_type(4))) unsigned u32x4;
typedef __attribute__((ext_vector_type(2))) unsigned u32x2;
typedef unsigned long long u64;
#define LAS __attribute__((address_space(3)))
#define GAS __attribute__((address_space(1)))
#if defined(__HIP_DEVICE_COMPILE__) && !defined(ALLOW_PK)
#define NOPK __attribute__((target("no-packed-fp32-ops")))
#else
#define NOPK
#endif
#define DI __device__ __forceinline__ NOPK
#define U2F(x) __builtin_bit_cast(float, (unsigned)(x))
#define F2U(x) __builtin_bit_cast(unsigned, (float)(x))
#define RLX_AGENT __ATOMIC_RELAXED, __HIP_MEMORY_SCOPE_AGENT
#define RLX_WG __ATOMIC_RELAXED, __HIP_MEMORY_SCOPE_WORKGROUP

constexpr int DM = 512, DFF = 1792;
constexpr unsigned WS_FLAGS = 0;
constexpr unsigned WS_FLAG0 = 4096, FLAG_STRIDE = 128;
constexpr unsigned WS_G = 106496;
constexpr unsigned WS_KV = 131072;
constexpr unsigned WS_H = WS_KV + 32u * 8u * 16384u;
constexpr unsigned WS_O = WS_H + 64u * 112u * 1024u;
constexpr unsigned WS_W = WS_O + 64u * 32u * 1024u;
constexpr unsigned WS_END = WS_W + 16u * 144u * 1024u;
constexpr unsigned WS_DIAG = WS_END;
constexpr int XB = 32768, BUFB = XB + 6144 + 8192;
constexpr int L_V = 65536;
constexpr int L_GATE = 3 * BUFB;
constexpr int L_RT = L_GATE + 4096;
constexpr int L_MISC = 163840 - 2048;
constexpr int L_DUMMY = L_RT + 10240;
constexpr int L_B1 = L_MISC + 272, L_B2 = L_MISC + 512 + 272, L_SYNC = L_MISC + 1024 + 272;
static_assert(L_DUMMY + 1024 <= L_MISC, "lds");
enum { SY_READY = 0  , SY_KV = 8, SY_DONE = 9, SY_F = 10, SY_PASS = 11, SY_RED = 12, SY_TRIG = 13, SY_FSEEN = 14, SY_AW = 15, SY_FA = 16, SY_PROG = 24  , SY_OL = 32  , SY_N = 40 };
constexpr unsigned SENT = 0xFFFFFFFFu;
constexpr unsigned SPIN_CAP = 1u << 18;

#define MFMA(a, b, c) __builtin_amdgcn_mfma_f32_32x32x16_bf16((a), (b), (c), 0, 0, 0)
#define MFMA16(a, b, c) __builtin_amdgcn_mfma_f32_16x16x32_bf16((a), (b), (c), 0, 0, 0)
#define SBAR() __builtin_amdgcn_sched_barrier(0)
#define CFENCE() asm volatile("" ::: "memory")
#define PIN(x) asm volatile("" : "+v"(x))

#define F2_PRIO 0
#define DSPLIT 4
#define FPA 0
#define TPF 0
#define TSLP 8
#define TLA 4
#define RTC 3
#define CPF 0
#define ROTX (hd + 2 * t)
#define EARLYW 4
#define LEAP 1
#define NP1 22
#define TRIGJ 7
#define STAMP(i) do {} while (0)
#define STAMP_FLUSH() do {} while (0)

DI unsigned pack2(float lo, float hi) { f32x2 v = {lo, hi}; return __builtin_bit_cast(unsigned, __builtin_convertvector(v, bf16x2)); }
DI u32x4 pack8(const f32x16& a, int o, float s) {
    u32x4 r; r[0] = pack2(a[o] * s, a[o + 1] * s); r[1] = pack2(a[o + 2] * s, a[o + 3] * s); r[2] = pack2(a[o + 4] * s, a[o + 5] * s); r[3] = pack2(a[o + 6] * s, a[o + 7] * s); return r; }
DI u32x4 pack8(const f32x16& a, int o) {
    u32x4 r; r[0] = pack2(a[o], a[o + 1]); r[1] = pack2(a[o + 2], a[o + 3]); r[2] = pack2(a[o + 4], a[o + 5]); r[3] = pack2(a[o + 6], a[o + 7]); return r; }
DI float bfround(float v) { return (float)(__bf16)v; }
DI float bf2f(unsigned short u) { return U2F((unsigned)u << 16); }
DI bf16x8 ldsr(const LAS char* p) { return *(const LAS bf16x8*)p; }
DI bf16x8 ldtr(const LAS char* p0, const LAS char* p1) {
    s16x4 a = __builtin_amdgcn_ds_read_tr16_b64_v4i16((LAS s16x4*)p0), b = __builtin_amdgcn_ds_read_tr16_b64_v4i16((LAS s16x4*)p1);
    return __builtin_bit_cast(bf16x8, __builtin_shufflevector(a, b, 0, 1, 2, 3, 4, 5, 6, 7)); }
DI void glds16(unsigned voff, const void* sbase, unsigned lds) {
    unsigned keep; asm volatile("s_mov_b32 %0, m0\n\ts_mov_b32 m0, %3\n\ts_nop 0\n\tglobal_load_lds_dwordx4 %1, %2\n\ts_mov_b32 m0, %0" : "=&s"(keep) : "v"(voff), "s"(sbase), "s"(lds) : "memory"); }
DI void glds4(unsigned voff, const void* sbase, unsigned lds) {
    unsigned keep; asm volatile("s_mov_b32 %0, m0\n\ts_mov_b32 m0, %3\n\ts_nop 0\n\tglobal_load_lds_dword %1, %2\n\ts_mov_b32 m0, %0" : "=&s"(keep) : "v"(voff), "s"(sbase), "s"(lds) : "memory"); }
DI u32x4 ld1(__amdgpu_buffer_rsrc_t rs, unsigned voff, unsigned soff) { return __builtin_amdgcn_raw_buffer_load_b128(rs, voff, soff, 16); }
DI void st1(u32x4 v, __amdgpu_buffer_rsrc_t rs, unsigned voff, unsigned soff) {
    __builtin_amdgcn_raw_buffer_store_b128(v, rs, voff, soff, 16); asm volatile("s_nop 1" :: "v"(v) : "memory"); }
DI u32x4 unmark(u32x4 v) {
#pragma unroll
    for (int i = 0; i < 4; ++i) v[i] = v[i] == SENT ? 0x7FFF7FFFu : v[i];
    return v; }
template <int N> DI void fetch_marked(u32x4 (&d)[N], __amdgpu_buffer_rsrc_t rs, unsigned voff, unsigned soff, unsigned s1, unsigned s2) {
    { const unsigned l = voff >> 4, po = (l & 1) * s1 + ((l & (N - 1)) >> 1) * s2 + 1020;
#pragma nounroll
      for (unsigned s = 0; s < SPIN_CAP; ++s) { if (__builtin_amdgcn_ballot_w64(__builtin_amdgcn_raw_buffer_load_b32(rs, po, soff, 16) == SENT) == 0) break; __builtin_amdgcn_s_sleep(1); CFENCE(); } }
#pragma nounroll
    for (unsigned s = 0; s < SPIN_CAP; ++s) {
#pragma unroll
        for (int i = 0; i < N; ++i) d[i] = ld1(rs, voff, soff + (i & 1) * s1 + (i >> 1) * s2);
        unsigned mx = 0;
#pragma unroll
        for (int i = 0; i < N; ++i) mx = __builtin_elementwise_max(__builtin_elementwise_max(mx, __builtin_elementwise_max(d[i][0], d[i][1])), __builtin_elementwise_max(d[i][2], d[i][3]));
        if (__builtin_amdgcn_ballot_w64(mx == SENT) == 0) break;
        __builtin_amdgcn_s_sleep(2); CFENCE();
    } }
DI void drain() { asm volatile("s_waitcnt vmcnt(0)" ::: "memory"); }
DI unsigned lds_arrive(LAS unsigned* w, int lane) {
    unsigned old = 0; if (lane == 0) old = __hip_atomic_fetch_add(w, 1u, RLX_WG); return __builtin_amdgcn_readfirstlane(old); }
template <int SL = 1> DI void lds_wait_ge(LAS unsigned* w, unsigned want) {
#pragma nounroll
    for (unsigned s = 0; s < (SPIN_CAP << 4); ++s) { if ((unsigned)__builtin_amdgcn_readfirstlane(__hip_atomic_load(w, RLX_WG)) >= want) break; __builtin_amdgcn_s_sleep(SL); }
    CFENCE(); }
template <int SL = 1> DI void poll_tags(const char* ws, unsigned flag_off, bool active, u64 tag) {
#pragma nounroll
    for (unsigned s = 0; s < SPIN_CAP; ++s) {
        u64 v = tag; if (active) v = __hip_atomic_load((GAS u64*)(ws + flag_off), RLX_AGENT);
        if (__builtin_amdgcn_ballot_w64(v != tag) == 0) break; __builtin_amdgcn_s_sleep(SL); }
    __builtin_amdgcn_fence(__ATOMIC_ACQUIRE, "wavefront"); CFENCE(); }
DI unsigned flag_at(int kind, int hd, int t) { return WS_FLAG0 + (unsigned)(kind * 256 + hd * 8 + t) * FLAG_STRIDE; }
DI int g_at(int i) { return L_MISC + (i >> 6) * 512 + 16 + (i & 63) * 4; }
DI u32x4 join16(const f32x4& a, const f32x4& b, float sa, float sb) {
    auto r0 = __builtin_amdgcn_permlane16_swap(pack2(a[0] * sa, a[1] * sa), pack2(b[0] * sb, b[1] * sb), false, false);
    auto r1 = __builtin_amdgcn_permlane16_swap(pack2(a[2] * sa, a[3] * sa), pack2(b[2] * sb, b[3] * sb), false, false);
    return (u32x4){r0[0], r1[0], r0[1], r1[1]}; }
DI f32x16 exp16(f32x16 s) {
#pragma unroll
    for (int i = 0; i < 16; ++i) s[i] = __builtin_amdgcn_exp2f(s[i]);
    return s; }
DI f32x16 pv(bf16x8 v0, bf16x8 v1, const f32x16& p, f32x16 o, int idx) {
    const f32x8 lo = __builtin_shufflevector(p, p, 0, 1, 2, 3, 4, 5, 6, 7), hi = __builtin_shufflevector(p, p, 8, 9, 10, 11, 12, 13, 14, 15);
    o = __builtin_amdgcn_smfmac_f32_32x32x32_bf16(v0, __builtin_bit_cast(bf16x16, lo), o, idx, 0, 0);
    o = __builtin_amdgcn_smfmac_f32_32x32x32_bf16(v1, __builtin_bit_cast(bf16x16, hi), o, idx, 0, 0);
    return o; }
DI float rowmax16(const f32x16& s) {
    float m = __builtin_fmaxf(__builtin_fmaxf(s[0], s[1]), s[2]);
#pragma unroll
    for (int i = 3; i < 15; i += 2) m = __builtin_fmaxf(__builtin_fmaxf(m, s[i]), s[i + 1]);
    return __builtin_fmaxf(m, s[15]); }
DI float xhalf_max(float m) { auto r = __builtin_amdgcn_permlane32_swap(F2U(m), F2U(m), false, false); return __builtin_fmaxf(U2F(r[0]), U2F(r[1])); }

__global__ __launch_bounds__(768, 3) NOPK void gated_mix_fused(const unsigned short* __restrict__ x, const unsigned short* __restrict__ wqkv, const unsigned short* __restrict__ wo,
                                                         const unsigned short* __restrict__ w1, const unsigned short* __restrict__ b1, const unsigned short* __restrict__ w2,
                                                         const unsigned short* __restrict__ b2, const unsigned short* __restrict__ gate_w, unsigned short* __restrict__ out, char* ws) {
    __shared__ __attribute__((aligned(1024))) char smem[163840];
    LAS char* lds = (LAS char*)smem;
    const unsigned lds0 = (unsigned)(uintptr_t)lds;
    const int wid = __builtin_amdgcn_readfirstlane(__builtin_amdgcn_workitem_id_x() >> 6);
#define LANES() int lane = __builtin_amdgcn_mbcnt_hi(~0u, __builtin_amdgcn_mbcnt_lo(~0u, 0u)); PIN(lane); const int r32 = lane & 31, hh = lane >> 5, g4 = lane >> 4, r16 = lane & 15, q4 = r16 >> 2, p4 = lane & 3; (void)r32; (void)hh; (void)g4; (void)q4; (void)p4
    const int bx = __builtin_amdgcn_workgroup_id_x(), hd = 8 * (bx & 3) + ((bx >> 3) & 7), t = 4 * ((bx >> 2) & 1) + (bx >> 6);
    const __amdgpu_buffer_rsrc_t rs = __builtin_amdgcn_make_buffer_rsrc(ws, 0, WS_END, 0x00020000);
    LAS unsigned* sy = (LAS unsigned*)(lds + L_SYNC);
    asm volatile("" :: "s"(x), "s"(wqkv), "s"(wo), "s"(w1), "s"(b1), "s"(w2), "s"(b2), "s"(gate_w), "s"(out), "s"(ws));
    u64 tag; u32x2 ep;
#define EPOCH_LOAD() asm volatile("global_load_dwordx2 %0, %1, %2" : "=v"(ep) : "v"(0), "s"(ws) : "memory")
#define EPOCH_TAKE() do { PIN(ep); tag = (((u64)(unsigned)__builtin_amdgcn_readfirstlane(ep[1]) << 32) | (unsigned)__builtin_amdgcn_readfirstlane(ep[0])) + 1ull; } while (0)
    const int cp = hd & 7, rq = hd >> 3;
    const bool hasF = cp != 7; const int fb = hd - rq;
    const int rot = ROTX;
    STAMP(0);
#define CS(c) (((c) + rot) & 7)
#define BUFOF(c) ((((c) + 1) % 3) * BUFB)
#define CHUNK_SYNC(c, n) do { if ((c) < 7) asm volatile("s_waitcnt vmcnt(" #n ") lgkmcnt(0)\n\ts_barrier" ::: "memory"); else asm volatile("s_waitcnt vmcnt(0) lgkmcnt(0)\n\ts_barrier" ::: "memory"); } while (0)

    if (wid < 8) {
        f32x4 aq[2] = {}, ak[2] = {}, av[2] = {};
        { LANES(); const int xsw = g4 ^ (r16 >> 1);
          const int c0 = (lane & 7) ^ (lane >> 4), rho = lane >> 1, r8 = lane >> 3;
          const unsigned dvo0 = r8 * 1024 + c0 * 16, dvo1 = r8 * 1024 + (c0 ^ 4) * 16;
          const unsigned dvq = (8 * ((rho >> 2) & 3) + 4 * (rho >> 4) + (rho & 3)) * 3072 + (lane & 1) * 16;
          const unsigned dvf = (8 * (r8 >> 2) + (r8 & 3)) * 3584 + (((lane & 7) ^ (r8 & 6)) * 16);
          const char* xbase = (const char*)x + (size_t)(256 * t + 32 * wid) * 1024;
          const bool hq = wid < 6, hf = hasF;
          const char* qbase = (const char*)wqkv + hd * 32 + (hq ? (wid >> 1) * 1024 + (wid & 1) * 32 * 3072 : 0);
          const char* fbase = (const char*)w1 + (hf ? fb * 128 : 0) + (32 * (wid >> 2) + 16 * (wid & 1) + 4 * ((wid >> 1) & 1)) * 3584;
          const unsigned vq_ = hq ? dvq : 0u, vf_ = hf ? dvf : 0u;
#define DMA_X(c) do { _Pragma("unroll") for (int i = 0; i < 4; ++i) glds16((i & 1) ? dvo1 : dvo0, xbase + CS(c) * 128 + i * 8192, lds0 + BUFOF(c) + wid * 4096 + i * 1024); } while (0)
#define DMA_ALL(c) do { glds16(vq_, qbase + CS(c) * 64 * 3072, hq ? lds0 + BUFOF(c) + XB + wid * 1024 : lds0 + L_DUMMY); \
              glds16(vf_, fbase + CS(c) * 64 * 3584, hf ? lds0 + BUFOF(c) + XB + 6144 + wid * 1024 : lds0 + L_DUMMY); DMA_X(c); } while (0)
          DMA_ALL(0);
          EPOCH_LOAD();
          if (wid == 0) {
              if (lane < SY_N) sy[lane] = 0;
              if (lane < 8) { const unsigned one = lane < 4 ? 0x3F803F80u : 0u; *(LAS u32x4*)(lds + L_MISC + (lane & 3) * 512 + (lane >> 2) * 496) = (u32x4){one, one, one, one}; }
          }
          __builtin_amdgcn_s_sleep(4);
          DMA_ALL(1);
          STAMP(20);
          const int xo = (32 * wid + r16) * 128, wo_ = XB + (4 * g4 + q4) * 32 + p4 * 8;
          bf16x8 xa[2], xb[2], wq[2], wk[2], wv[2];
#define RX(d, c, ks, rt) d[ks] = ldsr(lds + BUFOF(c) + xo + (rt) * 2048 + ((xsw ^ (4 * (ks))) * 16))
#define RW(d, c, ks, im) d[ks] = ldtr(lds + BUFOF(c) + wo_ + (im) * 2048 + (ks) * 1024, lds + BUFOF(c) + wo_ + (im) * 2048 + (ks) * 1024 + 512)
#define M2(A, W, ks) do { A[0] = MFMA16(W[ks], xa[ks], A[0]); A[1] = MFMA16(W[ks], xb[ks], A[1]); PIN(A[0]); PIN(A[1]); SBAR(); } while (0)
#define MV(ks) do { av[0] = MFMA16(xa[ks], wv[ks], av[0]); av[1] = MFMA16(xb[ks], wv[ks], av[1]); PIN(av[0]); PIN(av[1]); SBAR(); } while (0)
#pragma unroll
          for (int c = 0; c < 8; ++c) {
              CHUNK_SYNC(c, 6);
              STAMP(12 + c);
              if (c == 7) { EPOCH_TAKE(); __builtin_amdgcn_s_setprio(3); }
              if (c + 2 < 8 && wid >= DSPLIT) DMA_ALL(c + 2);
              if (c > 0) M2(aq, wq, 1);
              RX(xa, c, 0, 0); RX(xb, c, 0, 1); RW(wq, c, 0, 0); SBAR();
              if (c > 0) M2(ak, wk, 1);
              RW(wk, c, 0, 1); RW(wv, c, 0, 2); SBAR();
              if (c > 0) MV(1);
              RX(xa, c, 1, 0); RX(xb, c, 1, 1); RW(wq, c, 1, 0); SBAR();
              M2(aq, wq, 0);
              RW(wk, c, 1, 1); RW(wv, c, 1, 2); SBAR();
              M2(ak, wk, 0);
              MV(0);
              if (c + 2 < 8 && wid < DSPLIT) DMA_ALL(c + 2);
          }
          M2(aq, wq, 1); M2(ak, wk, 1); MV(1);
#undef RX
#undef RW
#undef M2
#undef MV
#undef DMA_ALL
#undef DMA_X
        }
        STAMP(1);
        LANES();
        constexpr float QS = 0.25f * 1.4426950408889634f;
        const bf16x8 qf = __builtin_bit_cast(bf16x8, join16(aq[0], aq[1], QS, QS));
        const unsigned kvg = WS_KV + (unsigned)(hd * 8 + t) * 16384u, og = WS_O + (unsigned)((8 * t + wid) * 32 + hd) * 1024u;
        { const u32x4 kk = join16(ak[0], ak[1], 1.f, 1.f), vv = join16(av[0], av[1], 1.f, 1.f);
          const int ok = wid * 1024 + lane * 16, ov = wid * 1024 + (g4 & 1) * 512 + hh * 256 + r16 * 16;
          *(LAS u32x4*)(lds + ok) = kk; *(LAS u32x4*)(lds + L_V + ov) = vv;
          asm volatile("s_waitcnt lgkmcnt(0)" ::: "memory");
          if (lane == 0) __hip_atomic_fetch_add(sy + SY_AW, 1u, RLX_WG);
          st1(kk, rs, ok, kvg); st1(vv, rs, ov + 8192, kvg); }
        lds_wait_ge<1>(sy + SY_AW, 8);
        STAMP(2);
        drain();
        if (lds_arrive(sy + SY_KV, lane) == 7 && lane == 0) __hip_atomic_store((GAS u64*)(ws + flag_at(0, hd, t)), tag, RLX_AGENT);
        const unsigned rkv = WS_KV + (unsigned)(hd * 8 + ((t + wid) & 7)) * 16384u;
#define FETCH(NB) do { poll_tags(ws, flag_at(0, hd, (t + wid) & 7), lane == 0, tag); \
            _Pragma("unroll") for (int b_ = 0; b_ < 16 / (NB); ++b_) { u32x4 stg[NB]; \
                _Pragma("unroll") for (int i = 0; i < (NB); ++i) stg[i] = ld1(rs, lane * 16 + (b_ * (NB) + i) * 1024, rkv); \
                if (b_ == 0) lds_wait_ge(sy + SY_FA, 4);                         \
                _Pragma("unroll") for (int i = 0; i < (NB); ++i) *(LAS u32x4*)(lds + ((b_ * (NB) + i) >> 3) * L_V + wid * 8192 + ((b_ * (NB) + i) & 7) * 1024 + lane * 16) = stg[i]; } \
            asm volatile("s_waitcnt lgkmcnt(0)" ::: "memory"); \
            if (lane == 0) __hip_atomic_store(sy + SY_READY + wid, 1u, RLX_WG); } while (0)
        if (wid > 0 && wid < EARLYW) FETCH(16);
        const int idx = 0xDDDD;
        const LAS char* kp = lds + lane * 16;
        const bool vl = r32 < 16;
        const LAS char* vp = lds + (vl ? L_V + 512 + hh * 256 + r32 * 16 : (r32 & 11) == 0 ? L_MISC : L_MISC + 496);
        const int vstep = vl ? 2048 : 0;
        f32x16 o = (f32x16){}, negm, sA, sB = (f32x16){};
        bf16x8 kf = ldsr(kp), v0 = ldsr(vl ? vp - 512 : vp), v1 = (bf16x8){};
        { const f32x16 s0 = MFMA(kf, qf, (f32x16){}); const float m0 = -xhalf_max(rowmax16(s0));
#pragma unroll
          for (int i = 0; i < 16; ++i) negm[i] = m0; }
        sA = MFMA(kf, qf, negm);
        kf = ldsr(kp + 1024);
        STAMP(10);
        __builtin_amdgcn_s_setprio(0);
#define EX(S, a, b) do { _Pragma("unroll") for (int i_ = (a); i_ < (b); ++i_) S[i_] = __builtin_amdgcn_exp2f(S[i_]); PIN(S); SBAR(); } while (0)
#define LO8(S) __builtin_bit_cast(bf16x16, __builtin_shufflevector(S, S, 0, 1, 2, 3, 4, 5, 6, 7))
#define HI8(S) __builtin_bit_cast(bf16x16, __builtin_shufflevector(S, S, 8, 9, 10, 11, 12, 13, 14, 15))
#define SM(a, b) do { o = __builtin_amdgcn_smfmac_f32_32x32x32_bf16(a, b, o, idx, 0, 0); PIN(o); SBAR(); } while (0)
#define TILE(CUR, OTH, KO, VO) do { SBAR(); EX(CUR, 0, 2); SM(v1, HI8(OTH)); v1 = ldsr(vp + (VO)); SBAR(); EX(CUR, 2, 6); \
            OTH = MFMA(kf, qf, negm); PIN(OTH); SBAR(); kf = ldsr(kp + (KO)); SBAR(); EX(CUR, 6, 10); SM(v0, LO8(CUR)); v0 = ldsr(vp + (VO) + 512); SBAR(); EX(CUR, 10, 16); } while (0)
#define PAIR(KO) do { TILE(sA, sB, (KO) + 2048, 0); TILE(sB, sA, (KO) + 3072, 1024); vp += vstep; } while (0)
        PAIR(0); PAIR(2048);
        PAIR(4096);
        lds_wait_ge(sy + SY_READY + 1, 1);
        PAIR(6144);
        if (wid >= EARLYW) { __builtin_amdgcn_s_setprio(3); FETCH(8); }
#undef FETCH
        STAMP(3);
        for (int j = 1; j < 8; ++j) {
            kp += 8192;
            if (j > 1 && j < 6) STAMP(4 + j);
            if (LEAP) {
                if (lane == 0) __hip_atomic_store(sy + SY_PROG + wid, (unsigned)j, RLX_WG);
                if ((unsigned)__builtin_amdgcn_readfirstlane(__hip_atomic_load(sy + SY_PROG + (wid ^ 4), RLX_WG)) >= (unsigned)j) __builtin_amdgcn_s_setprio(1); else __builtin_amdgcn_s_setprio(0);
            }
            PAIR(0); PAIR(2048); PAIR(4096);
            if (j < 7) lds_wait_ge(sy + SY_READY + j + 1, 1);
            PAIR(6144);
            if (j == 4 && lane == 0) __hip_atomic_fetch_add(sy + SY_PASS, 1u, RLX_WG);
            if (j == TRIGJ && lane == 0) __hip_atomic_fetch_add(sy + SY_TRIG, 1u, RLX_WG);
        }
        SM(v1, HI8(sB));
#undef PAIR
#undef TILE
#undef SM
#undef EX
        asm volatile("s_waitcnt lgkmcnt(0)" ::: "memory");
        if (lane == 0) __hip_atomic_fetch_add(sy + SY_DONE, 1u, RLX_WG);
        STAMP(4);
        { float chk = o[8] * 0.f;
#pragma unroll
          for (int i = 0; i < 8; ++i) chk = __builtin_fmaf(o[i], 0.f, chk);
          if (__builtin_expect(0 != __builtin_amdgcn_ballot_w64(!(chk == 0.f) || o[8] > 1.8e19f), 0)) {
              float m = -__builtin_inff();
#pragma nounroll
              for (int tl = 0; tl < 64; ++tl) { const bf16x8 kg = __builtin_bit_cast(bf16x8, ld1(rs, lane * 16, WS_KV + (unsigned)(hd * 8 + (tl >> 3)) * 16384u + (tl & 7) * 1024));
                  m = __builtin_fmaxf(m, rowmax16(MFMA(kg, qf, (f32x16){}))); }
              m = -xhalf_max(m);
#pragma unroll
              for (int i = 0; i < 16; ++i) negm[i] = m;
              o = (f32x16){};
              const u32x4 ones = {0x3F803F80u, 0x3F803F80u, 0x3F803F80u, 0x3F803F80u};
#pragma nounroll
              for (int tl = 0; tl < 64; ++tl) { const unsigned so = WS_KV + (unsigned)(hd * 8 + (tl >> 3)) * 16384u + (tl & 7) * 1024;
                  const bf16x8 kg = __builtin_bit_cast(bf16x8, ld1(rs, lane * 16, so));
                  const u32x4 a0 = ld1(rs, 8192 + hh * 256 + r16 * 16, so), a1 = ld1(rs, 8192 + 512 + hh * 256 + r16 * 16, so);
                  o = pv(__builtin_bit_cast(bf16x8, vl ? a0 : ones), __builtin_bit_cast(bf16x8, vl ? a1 : ones), exp16(MFMA(kg, qf, negm)), o, idx); }
          } }
        lds_wait_ge(sy + SY_FSEEN, 4);
        { u32x4 ov = pack8(o, 0, *(const LAS float*)(lds + g_at(32 * wid + r32)) * __builtin_amdgcn_rcpf(o[8]));
          st1(unmark(ov), rs, lane * 16, og); }
        STAMP(5);
        { u32x4 of[8];
          fetch_marked(of, rs, lane * 16, WS_O + (unsigned)((8 * t + 2 * rq) * 32 + 4 * wid) * 1024u, 32768, 1024);
          STAMP(11);
          lds_wait_ge(sy + SY_DONE, 8);
#pragma unroll
          for (int i = 0; i < 8; ++i) *(LAS u32x4*)(lds + (wid >> 2) * L_V + 32768 + (wid & 3) * 8192 + i * 1024 + lane * 16) = of[i];
          asm volatile("s_waitcnt lgkmcnt(0)" ::: "memory");
          if (lane == 0) __hip_atomic_store(sy + SY_OL + wid, 1u, RLX_WG); }
        asm volatile(".Lgm_end:");
        STAMP(21); STAMP_FLUSH();
    } else {
        const int f = wid - 8, i32 = rq + 4 * t;
        { LANES(); const int xsw = g4 ^ (r16 >> 1);
          if (FPA) __builtin_amdgcn_s_setprio(1);
          unsigned d0 = 0;
          EPOCH_LOAD();
          const int l6 = ((lane - 48) * 43) >> 8, m6 = lane - 48 - 6 * l6;
          const char* tp = lane < 32 ? (const char*)x + (size_t)(256 * t + 32 * cp + lane) * 1024 : lane < 48 ? (const char*)w1 + (hasF ? fb : 0) * 128 + (16 * (t & 3) + lane - 32) * 3584
                                     : (const char*)wqkv + (hd & 24) * 32 + (2 * (cp + 8 * (t & 3)) + (l6 & 1)) * 3072 + (m6 >> 1) * 1024 + (m6 & 1) * 128;
          const unsigned tstep = lane < 32 ? 128 : lane < 48 ? 64 * 3584 : 64 * 3072;
#define TOUCH(c) do { if (TPF && lane < 60) asm volatile("global_load_dword %0, %1, off" : "+v"(d0) : "v"(tp + (c) * tstep) : "memory"); } while (0)
          unsigned d1 = 0;
          if (CPF && f == 3) {
              const char* pc; unsigned len;
              asm volatile("s_getpc_b64 %0\n.Lgm_ref:\n\ts_mov_b32 %1, .Lgm_end-.Lgm_ref" : "=s"(pc), "=s"(len));
              const unsigned po = (unsigned)((bx >> 3) + 32 * lane) * 128u;
              if ((int)po < (int)len) asm volatile("global_load_dword %0, %1, %2" : "+v"(d1) : "v"(po), "s"(pc) : "memory");
          }
          if (hd == 31) { glds16(lane * 16, gate_w, lds0 + L_GATE + f * 1024); asm volatile("s_waitcnt vmcnt(0)" ::: "memory"); }
          __builtin_amdgcn_s_sleep(TSLP);
          if (TPF == 2) { TOUCH(2 + f); if (f < 2) TOUCH(6 + f); } else if (f < TLA - 2) TOUCH(2 + f);
          const int xo = (64 * f + r16) * 128, fo = XB + 6144 + (4 * g4 + q4) * 128 + p4 * 8, fsw = (2 * g4 + (q4 >> 1)) & 3;
          bf16x8 xf[2][4], wf[2][4];
#define RX(c, ks, rt) xf[ks][rt] = ldsr(lds + BUFOF(c) + xo + (rt) * 2048 + ((xsw ^ (4 * (ks))) * 16))
#define RW(c, ks, cb) wf[ks][cb] = ldtr(lds + BUFOF(c) + fo + (ks) * 4096 + (((cb) ^ fsw) * 32), lds + BUFOF(c) + fo + (ks) * 4096 + 2048 + (((cb) ^ fsw) * 32))
#define M2(ks, cb, r0) do { af[cb][r0] = MFMA16(wf[ks][cb], xf[ks][r0], af[cb][r0]); af[cb][r0 + 1] = MFMA16(wf[ks][cb], xf[ks][r0 + 1], af[cb][r0 + 1]); PIN(af[cb][r0]); PIN(af[cb][r0 + 1]); SBAR(); } while (0)
#define F_SYNC(c) do { if ((c) < 7) asm volatile("s_waitcnt lgkmcnt(0)\n\ts_barrier" ::: "memory"); else CHUNK_SYNC(c, 0); \
              STAMP(12 + (c)); \
              if ((c) == 4 || (c) == RTC) { LANES(); \
              if ((c) == 4) {     \
                  _Pragma("unroll") for (int i = 0; i < 2; ++i) st1((u32x4){SENT, SENT, SENT, SENT}, rs, lane * 16, WS_O + (unsigned)((8 * t + 2 * f + i) * 32 + hd) * 1024u); } \
              if (TPF == 1 && (c) + TLA < 8 && f == ((c) & 3)) TOUCH((c) + TLA); \
              if ((c) == RTC) {   \
                  const int u = i32 + 32 * f; const char* src = u < 112 ? (const char*)w2 + (size_t)u * 16384 : (const char*)wo + (size_t)(u - 112) * 16384; \
                  const unsigned vo = (lane >> 3) * 1024 + cp * 128 + (lane & 7) * 16; \
                  glds16(vo, src, lds0 + L_RT + f * 2048); glds16(vo, src + 8192, lds0 + L_RT + f * 2048 + 1024); \
                  if (f == 0 && i32 < 16) { const char* s2 = (const char*)wo + (size_t)(i32 + 16) * 16384; glds16(vo, s2, lds0 + L_RT + 8192); glds16(vo, s2 + 8192, lds0 + L_RT + 8192 + 1024); } \
                  if (f == 1 && lane < 32) { if (hasF) glds4(lane * 4, (const char*)b1 + fb * 128, lds0 + L_B1); glds4(lane * 4, (const char*)b2 + cp * 128, lds0 + L_B2); } } \
              } if ((c) == 7) { EPOCH_TAKE(); PIN(d0); PIN(d1); } } while (0)
#define F_LEAVE() STAMP(1); LANES(); \
              asm volatile("s_waitcnt lgkmcnt(0)" ::: "memory"); \
              if (lane == 0) __hip_atomic_fetch_add(sy + SY_FA, 1u, RLX_WG); \
              __builtin_amdgcn_s_setprio(2); \
              for (int n = f; n < ((f == 0 && i32 < 16) ? 5 : 4); n += 4) {        \
                  const int u = i32 + 32 * n, r0 = (u < 112 ? 8 : 4) * hh + q4, r1 = u < 112 ? 512 : 1024; \
                  _Pragma("unroll") for (int ct = 0; ct < 2; ++ct) { const LAS char* s = lds + L_RT + n * 2048 + r0 * 128 + (g4 & 1) * 32 + p4 * 8 + ct * 64; \
                      st1(__builtin_bit_cast(u32x4, ldtr(s, s + r1)), rs, lane * 16, WS_W + (unsigned)((2 * cp + ct) * 144 + u) * 1024u); } }
          if (hasF) {
              f32x4 af[4][4] = {};
#pragma unroll
              for (int c = 0; c < 8; ++c) {
                  F_SYNC(c);
                  if (c > 0) M2(1, 0, 0);
                  RX(c, 0, 0); SBAR();
                  if (c > 0) M2(1, 0, 2);
                  RW(c, 0, 0); SBAR();
                  if (c > 0) M2(1, 1, 0);
                  RX(c, 0, 1); SBAR();
                  if (c > 0) M2(1, 1, 2);
                  RX(c, 0, 2); SBAR();
                  if (c > 0) M2(1, 2, 0);
                  RX(c, 0, 3); SBAR();
                  if (c > 0) M2(1, 2, 2);
                  RW(c, 0, 1); SBAR();
                  if (c > 0) M2(1, 3, 0);
                  RW(c, 0, 2); SBAR();
                  if (c > 0) M2(1, 3, 2);
                  RW(c, 0, 3); SBAR();
                  M2(0, 0, 0); RX(c, 1, 0); RW(c, 1, 0); SBAR();
                  M2(0, 0, 2); RX(c, 1, 1); RX(c, 1, 2); SBAR();
                  M2(0, 1, 0); RX(c, 1, 3); RW(c, 1, 1); SBAR();
                  M2(0, 1, 2); RW(c, 1, 2); RW(c, 1, 3); SBAR();
                  M2(0, 2, 0); M2(0, 2, 2); M2(0, 3, 0); M2(0, 3, 2);
              }
              M2(1, 0, 0); M2(1, 0, 2); M2(1, 1, 0); M2(1, 1, 2); M2(1, 2, 0); M2(1, 2, 2); M2(1, 3, 0); M2(1, 3, 2);
              { F_LEAVE();
#pragma unroll
                for (int cb = 0; cb < 4; ++cb) {
                    const u32x2 w = *(const LAS u32x2*)(lds + L_B1 + cb * 32 + g4 * 8);
                    const float bb[4] = {U2F(w[0] << 16), U2F(w[0] & 0xffff0000u), U2F(w[1] << 16), U2F(w[1] & 0xffff0000u)};
#pragma unroll
                    for (int rp = 0; rp < 2; ++rp) {
                        float v[8], e[8];
#pragma unroll
                        for (int i = 0; i < 8; ++i) { v[i] = af[cb][2 * rp + (i >> 2)][i & 3] + bb[i & 3];
                            e[i] = v[i] * __builtin_fmaf(v[i] * v[i], -2.f * 0.7978845608028654f * 1.4426950408889634f * 0.044715f, -2.f * 0.7978845608028654f * 1.4426950408889634f); }
                        SBAR();
#pragma unroll
                        for (int i = 0; i < 8; ++i) e[i] = __builtin_amdgcn_exp2f(e[i]);
                        SBAR();
#pragma unroll
                        for (int i = 0; i < 8; ++i) e[i] = 1.f + e[i];
                        SBAR();
#pragma unroll
                        for (int i = 0; i < 8; ++i) e[i] = __builtin_amdgcn_rcpf(e[i]);
                        SBAR();
                        st1(join16((f32x4){v[0] * e[0], v[1] * e[1], v[2] * e[2], v[3] * e[3]}, (f32x4){v[4] * e[4], v[5] * e[5], v[6] * e[6], v[7] * e[7]}, 1.f, 1.f), rs, lane * 16,
                            WS_H + (unsigned)((8 * t + 2 * f + rp) * 112 + 4 * fb + cb) * 1024u);
                    }
                } }
          } else {
              f32x4 az[4] = {};
#pragma unroll
              for (int c = 0; c < 8; ++c) {
                  F_SYNC(c);
                  if (hd == 31) {
#pragma unroll
                      for (int ks = 0; ks < 2; ++ks) { const bf16x8 gf = ldsr(lds + L_GATE + f * 1024 + CS(c) * 128 + ks * 64 + g4 * 16);
#pragma unroll
                          for (int rt = 0; rt < 4; ++rt) { RX(c, ks, rt); az[rt] = MFMA16(gf, xf[ks][rt], az[rt]); } }
                  }
              }
              { F_LEAVE();
                if (hd == 31) {
                    const float z = bfround(g4 == 0 ? az[0][0] : g4 == 1 ? az[1][0] : g4 == 2 ? az[2][0] : az[3][0]);
                    __hip_atomic_store((GAS float*)(ws + WS_G + (256 * t + 64 * f + lane) * 4), bfround(__builtin_amdgcn_rcpf(1.f + __builtin_amdgcn_exp2f(-1.4426950408889634f * z))), RLX_AGENT);
                } }
          }
#undef F_SYNC
#undef F_LEAVE
#undef RX
#undef RW
#undef M2
#undef TOUCH
        }
        LANES();
        const int freg = (f >> 1) * L_V + (f & 1) * 16384;
        drain();
        if (lds_arrive(sy + SY_F, lane) == 3 && lane == 0) __hip_atomic_store((GAS u64*)(ws + flag_at(1, hd, t)), tag, RLX_AGENT);
        STAMP(3);
        f32x16 acc[4] = {};
        poll_tags<4>(ws, hh ? flag_at(1, cp + 8 * (r32 & 3), r32 >> 2) : flag_at(1, r32, t), true, tag);
        *(LAS float*)(lds + g_at(64 * f + lane)) = __hip_atomic_load((GAS float*)(ws + WS_G + (256 * t + 64 * f + lane) * 4), RLX_AGENT);
        asm volatile("s_waitcnt lgkmcnt(0)" ::: "memory");
        if (lane == 0) __hip_atomic_fetch_add(sy + SY_FSEEN, 1u, RLX_WG);
        __builtin_amdgcn_s_setprio(F2_PRIO);
        STAMP(4);
        const unsigned hb = WS_H + (unsigned)((8 * t + 2 * rq) * 112 + 28 * f) * 1024u, wb = WS_W + (unsigned)(2 * cp * 144 + 28 * f) * 1024u;
        u32x4 fr[4][4];
#define LDK(slot, n) do { fr[slot][0] = ld1(rs, lane * 16, wb + (n) * 1024); fr[slot][1] = ld1(rs, lane * 16, wb + (144 + (n)) * 1024); \
                          fr[slot][2] = ld1(rs, lane * 16, hb + (n) * 1024); fr[slot][3] = ld1(rs, lane * 16, hb + (112 + (n)) * 1024); } while (0)
#define BC(v) __builtin_bit_cast(bf16x8, v)
#define MM4(w0, w1_, h0, h1) do { acc[0] = MFMA(w0, h0, acc[0]); acc[1] = MFMA(w0, h1, acc[1]); acc[2] = MFMA(w1_, h0, acc[2]); acc[3] = MFMA(w1_, h1, acc[3]); PIN(acc[0]); PIN(acc[1]); PIN(acc[2]); PIN(acc[3]); SBAR(); } while (0)
#define FFN2(n0, n1) do { _Pragma("unroll") for (int n = (n0); n < (n1); ++n) { MM4(BC(fr[n & 3][0]), BC(fr[n & 3][1]), BC(fr[n & 3][2]), BC(fr[n & 3][3])); if (n + 4 < (n1)) LDK(n & 3, n + 4); SBAR(); } } while (0)
#pragma unroll
        for (int n = 0; n < 4; ++n) LDK(n, n);
        FFN2(0, NP1);
        STAMP(5);
        { u32x4 tmp[16];
#pragma unroll
          for (int i = 0; i < 16; ++i) tmp[i] = ld1(rs, lane * 16, WS_W + (unsigned)((2 * cp + (i & 1)) * 144 + 112 + 8 * f + (i >> 1)) * 1024u);
          __builtin_amdgcn_s_setprio(2);
          lds_wait_ge<16>(sy + SY_PASS, 8);
#pragma unroll
          for (int i = 0; i < 16; ++i) *(LAS u32x4*)(lds + freg + i * 1024 + lane * 16) = tmp[i]; }
        STAMP(6);
        if (NP1 < 28) {
#pragma unroll
            for (int n = NP1; n < NP1 + 4; ++n) LDK(n & 3, n);
            lds_wait_ge<8>(sy + SY_TRIG, 8);
            __builtin_amdgcn_s_setprio(F2_PRIO);
            FFN2(NP1, 28);
        }
#undef LDK
#undef FFN2
        lds_wait_ge(sy + SY_FSEEN, 4);
        { const float og0 = bfround(1.f - *(const LAS float*)(lds + g_at(64 * rq + r32))), og1 = bfround(1.f - *(const LAS float*)(lds + g_at(64 * rq + 32 + r32)));
#pragma unroll
          for (int ct = 0; ct < 2; ++ct)
#pragma unroll
              for (int g = 0; g < 4; ++g) { u32x2 w = *(const LAS u32x2*)(lds + L_B2 + ct * 64 + g * 16 + hh * 8); if (f != 0) w = (u32x2){0u, 0u};
                  const float bb[4] = {U2F(w[0] << 16), U2F(w[0] & 0xffff0000u), U2F(w[1] << 16), U2F(w[1] & 0xffff0000u)};
#pragma unroll
                  for (int i = 0; i < 4; ++i) { acc[2 * ct][4 * g + i] = (acc[2 * ct][4 * g + i] + bb[i]) * og0; acc[2 * ct + 1][4 * g + i] = (acc[2 * ct + 1][4 * g + i] + bb[i]) * og1; } } }
        STAMP(7);
        __builtin_amdgcn_s_setprio(3);
#pragma unroll
        for (int n = 0; n < 8; ++n) { if ((n & 3) == 0) lds_wait_ge<1>(sy + SY_OL + 2 * f + (n >> 2), 1);
            if (n == 4) STAMP(10);
            const LAS char* p = lds + freg + n * 2048 + lane * 16;
            const bf16x8 w0 = ldsr(p), w1_ = ldsr(p + 1024), h0 = ldsr(p + 32768), h1 = ldsr(p + 32768 + 1024); MM4(w0, w1_, h0, h1); }
#undef MM4
#undef BC
        STAMP(8);
        f32x16 own = f == 0 ? acc[0] : f == 1 ? acc[1] : f == 2 ? acc[2] : acc[3];
#pragma unroll
        for (int qd = 0; qd < 4; ++qd) if (qd != f) {
#pragma unroll
            for (int g = 0; g < 4; ++g) *(LAS f32x4*)(lds + freg + qd * 4096 + g * 1024 + lane * 16) = (f32x4){acc[qd][4 * g], acc[qd][4 * g + 1], acc[qd][4 * g + 2], acc[qd][4 * g + 3]}; }
        asm volatile("s_waitcnt lgkmcnt(0)" ::: "memory");
        if (lane == 0) __hip_atomic_fetch_add(sy + SY_RED, 1u, RLX_WG);
        lds_wait_ge<0>(sy + SY_RED, 4);
        unsigned pk[8];
#pragma unroll
        for (int g = 0; g < 4; ++g) { f32x4 s = {own[4 * g], own[4 * g + 1], own[4 * g + 2], own[4 * g + 3]};
#pragma unroll
            for (int k = 1; k < 4; ++k) { const int w = (f + k) & 3; s += *(const LAS f32x4*)(lds + (w >> 1) * L_V + (w & 1) * 16384 + f * 4096 + g * 1024 + lane * 16); }
            pk[2 * g] = pack2(s[0], s[1]); pk[2 * g + 1] = pack2(s[2], s[3]); }
        unsigned short* orow = out + (size_t)(256 * t + 64 * rq + 32 * (f & 1) + r32) * DM + 64 * cp + 32 * (f >> 1) + 8 * hh;
#pragma unroll
        for (int k = 0; k < 2; ++k) {
            auto a = __builtin_amdgcn_permlane32_swap(pk[4 * k], pk[4 * k + 2], false, false), b = __builtin_amdgcn_permlane32_swap(pk[4 * k + 1], pk[4 * k + 3], false, false);
            *(u32x4*)(orow + 16 * k) = (u32x4){a[0], b[0], a[1], b[1]};
        }
        if (bx == 0 && f == 0 && lane == 0) __hip_atomic_store((GAS u64*)(ws + WS_FLAGS), tag, RLX_AGENT);
        STAMP(9); STAMP_FLUSH();
    }
}

extern "C" void kernel_launch(void* const* d_in, const int* in_sizes, int n_in, void* d_out, int out_size, void* d_ws, size_t ws_size, hipStream_t stream) {
    (void)in_sizes; (void)n_in; (void)out_size; (void)ws_size;
    hipLaunchKernelGGL(gated_mix_fused, dim3(256), dim3(768), 0, stream, (const unsigned short*)d_in[0], (const unsigned short*)d_in[1], (const unsigned short*)d_in[2],
                       (const unsigned short*)d_in[4], (const unsigned short*)d_in[5], (const unsigned short*)d_in[6], (const unsigned short*)d_in[7], (const unsigned short*)d_in[8],
                       (unsigned short*)d_out, (char*)d_ws);
}
```

```cpp
#include <hip/hip_runtime.h>
#include <hip/hip_bf16.h>
#include <stdint.h>

typedef __attribute__((ext_vector_type(8))) __bf16 bf16x8;
typedef __attribute__((ext_vector_type(16))) __bf16 bf16x16;
typedef __attribute__((ext_vector_type(2))) __bf16 bf16x2;
typedef __attribute__((ext_vector_type(4))) short s16x4;
typedef __attribute__((ext_vector_type(16))) float f32x16;
typedef __attribute__((ext_vector_type(8))) float f32x8;
typedef __attribute__((ext_vector_type(4))) float f32x4;
typedef __attribute__((ext_vector_type(2))) float f32x2;
typedef __attribute__((ext_vector_type(4))) unsigned u32x4;
typedef __attribute__((ext_vector_type(2))) unsigned u32x2;
typedef unsigned long long u64;
#define LAS __attribute__((address_space(3)))
#define GAS __attribute__((address_space(1)))
#if defined(__HIP_DEVICE_COMPILE__) && !defined(ALLOW_PK)
#define NOPK __attribute__((target("no-packed-fp32-ops")))
#else
#define NOPK
#endif
#define DI __device__ __forceinline__ NOPK
#define U2F(x) __builtin_bit_cast(float, (unsigned)(x))
#define F2U(x) __builtin_bit_cast(unsigned, (float)(x))
#define RLX_AGENT __ATOMIC_RELAXED, __HIP_MEMORY_SCOPE_AGENT
#define RLX_WG __ATOMIC_RELAXED, __HIP_MEMORY_SCOPE_WORKGROUP

constexpr int DM = 512, DFF = 1792;
constexpr unsigned WS_FLAGS = 0;
constexpr unsigned WS_FLAG0 = 4096, FLAG_STRIDE = 128;
constexpr unsigned WS_G = 106496;
constexpr unsigned WS_KV = 131072;
constexpr unsigned WS_H = WS_KV + 32u * 8u * 16384u;
constexpr unsigned WS_O = WS_H + 64u * 112u * 1024u;
constexpr unsigned WS_W = WS_O + 64u * 32u * 1024u;
constexpr unsigned WS_END = WS_W + 16u * 144u * 1024u;
constexpr unsigned WS_DIAG = WS_END;
constexpr int XB = 32768, BUFB = XB + 6144 + 8192;
constexpr int L_V = 65536;
constexpr int L_GATE = 3 * BUFB;
constexpr int L_RT = L_GATE + 4096;
constexpr int L_MISC = 163840 - 2048;
constexpr int L_DUMMY = L_RT + 10240;
constexpr int L_B1 = L_MISC + 272, L_B2 = L_MISC + 512 + 272, L_SYNC = L_MISC + 1024 + 272;
static_assert(L_DUMMY + 1024 <= L_MISC, "lds");
enum { SY_READY = 0  , SY_KV = 8, SY_DONE = 9, SY_F = 10, SY_PASS = 11, SY_RED = 12, SY_TRIG = 13, SY_FSEEN = 14, SY_AW = 15, SY_FA = 16, SY_PROG = 24  , SY_OL = 32  , SY_N = 40 };
constexpr unsigned SENT = 0xFFFFFFFFu;
constexpr unsigned SPIN_CAP = 1u << 18;

#define MFMA(a, b, c) __builtin_amdgcn_mfma_f32_32x32x16_bf16((a), (b), (c), 0, 0, 0)
#define MFMA16(a, b, c) __builtin_amdgcn_mfma_f32_16x16x32_bf16((a), (b), (c), 0, 0, 0)
#define SBAR() __builtin_amdgcn_sched_barrier(0)
#define CFENCE() asm volatile("" ::: "memory")
#define PIN(x) asm volatile("" : "+v"(x))

#define F2_PRIO 0
#define DSPLIT 4
#define FPA 0
#define TPF 0
#define TSLP 8
#define TLA 4
#define RTC 3
#define CPF 0
#define ROTX (hd + 2 * t)
#define EARLYW 4
#define F2_PACE 0
#define FDEFER 1
#define OSC 0
#define LEAP 2
#define NP1 22
#define TRIGJ 7
#define STAMP(i) do {} while (0)
#define STAMP_FLUSH() do {} while (0)

DI unsigned pack2(float lo, float hi) { f32x2 v = {lo, hi}; return __builtin_bit_cast(unsigned, __builtin_convertvector(v, bf16x2)); }
DI u32x4 pack8(const f32x16& a, int o, float s) {
    u32x4 r; r[0] = pack2(a[o] * s, a[o + 1] * s); r[1] = pack2(a[o + 2] * s, a[o + 3] * s); r[2] = pack2(a[o + 4] * s, a[o + 5] * s); r[3] = pack2(a[o + 6] * s, a[o + 7] * s); return r; }
DI u32x4 pack8(const f32x16& a, int o) {
    u32x4 r; r[0] = pack2(a[o], a[o + 1]); r[1] = pack2(a[o + 2], a[o + 3]); r[2] = pack2(a[o + 4], a[o + 5]); r[3] = pack2(a[o + 6], a[o + 7]); return r; }
DI float bfround(float v) { return (float)(__bf16)v; }
DI float bf2f(unsigned short u) { return U2F((unsigned)u << 16); }
DI bf16x8 ldsr(const LAS char* p) { return *(const LAS bf16x8*)p; }
DI bf16x8 ldtr(const LAS char* p0, const LAS char* p1) {
    s16x4 a = __builtin_amdgcn_ds_read_tr16_b64_v4i16((LAS s16x4*)p0), b = __builtin_amdgcn_ds_read_tr16_b64_v4i16((LAS s16x4*)p1);
    return __builtin_bit_cast(bf16x8, __builtin_shufflevector(a, b, 0, 1, 2, 3, 4, 5, 6, 7)); }
DI void glds16(unsigned voff, const void* sbase, unsigned lds) {
    unsigned keep; asm volatile("s_mov_b32 %0, m0\n\ts_mov_b32 m0, %3\n\ts_nop 0\n\tglobal_load_lds_dwordx4 %1, %2\n\ts_mov_b32 m0, %0" : "=&s"(keep) : "v"(voff), "s"(sbase), "s"(lds) : "memory"); }
DI void glds4(unsigned voff, const void* sbase, unsigned lds) {
    unsigned keep; asm volatile("s_mov_b32 %0, m0\n\ts_mov_b32 m0, %3\n\ts_nop 0\n\tglobal_load_lds_dword %1, %2\n\ts_mov_b32 m0, %0" : "=&s"(keep) : "v"(voff), "s"(sbase), "s"(lds) : "memory"); }
DI u32x4 ld1(__amdgpu_buffer_rsrc_t rs, unsigned voff, unsigned soff) { return __builtin_amdgcn_raw_buffer_load_b128(rs, voff, soff, 16); }
DI void st1(u32x4 v, __amdgpu_buffer_rsrc_t rs, unsigned voff, unsigned soff) {
    __builtin_amdgcn_raw_buffer_store_b128(v, rs, voff, soff, 16); asm volatile("s_nop 1" :: "v"(v) : "memory"); }
DI u32x4 unmark(u32x4 v) {
#pragma unroll
    for (int i = 0; i < 4; ++i) v[i] = v[i] == SENT ? 0x7FFF7FFFu : v[i];
    return v; }
template <int N> DI void fetch_marked(u32x4 (&d)[N], __amdgpu_buffer_rsrc_t rs, unsigned voff, unsigned soff, unsigned s1, unsigned s2) {
    { const unsigned l = voff >> 4, po = (l & 1) * s1 + ((l & (N - 1)) >> 1) * s2 + 1020;
#pragma nounroll
      for (unsigned s = 0; s < SPIN_CAP; ++s) { if (__builtin_amdgcn_ballot_w64(__builtin_amdgcn_raw_buffer_load_b32(rs, po, soff, 16) == SENT) == 0) break; __builtin_amdgcn_s_sleep(1); CFENCE(); } }
#pragma nounroll
    for (unsigned s = 0; s < SPIN_CAP; ++s) {
#pragma unroll
        for (int i = 0; i < N; ++i) d[i] = ld1(rs, voff, soff + (i & 1) * s1 + (i >> 1) * s2);
        unsigned mx = 0;
#pragma unroll
        for (int i = 0; i < N; ++i) mx = __builtin_elementwise_max(__builtin_elementwise_max(mx, __builtin_elementwise_max(d[i][0], d[i][1])), __builtin_elementwise_max(d[i][2], d[i][3]));
        if (__builtin_amdgcn_ballot_w64(mx == SENT) == 0) break;
        __builtin_amdgcn_s_sleep(2); CFENCE();
    } }
DI void drain() { asm volatile("s_waitcnt vmcnt(0)" ::: "memory"); }
DI unsigned lds_arrive(LAS unsigned* w, int lane) {
    unsigned old = 0; if (lane == 0) old = __hip_atomic_fetch_add(w, 1u, RLX_WG); return __builtin_amdgcn_readfirstlane(old); }
template <int SL = 1> DI void lds_wait_ge(LAS unsigned* w, unsigned want) {
    unsigned tv, ts, n;
    asm volatile("s_mov_b32 %2, 0x400000\n"
                 "1:\n\tds_read_b32 %0, %3\n\ts_waitcnt lgkmcnt(0)\n\tv_readfirstlane_b32 %1, %0\n\ts_nop 1\n\ts_cmp_ge_u32 %1, %4\n\ts_cbranch_scc1 2f\n\t"
                 "s_sleep %5\n\ts_sub_u32 %2, %2, 1\n\ts_cmp_lg_u32 %2, 0\n\ts_cbranch_scc1 1b\n"
                 "2:" : "=&v"(tv), "=&s"(ts), "=&s"(n) : "v"((unsigned)(uintptr_t)w), "s"(want), "i"(SL) : "memory", "scc"); }
template <int SL = 1> DI void poll_tags(const char* ws, unsigned flag_off, bool active, u64 tag) {
#pragma nounroll
    for (unsigned s = 0; s < SPIN_CAP; ++s) {
        u64 v = tag; if (active) v = __hip_atomic_load((GAS u64*)(ws + flag_off), RLX_AGENT);
        if (__builtin_amdgcn_ballot_w64(v != tag) == 0) break; __builtin_amdgcn_s_sleep(SL); }
    __builtin_amdgcn_fence(__ATOMIC_ACQUIRE, "wavefront"); CFENCE(); }
DI unsigned flag_at(int kind, int hd, int t) { return WS_FLAG0 + (unsigned)(kind * 256 + hd * 8 + t) * FLAG_STRIDE; }
DI int g_at(int i) { return L_MISC + (i >> 6) * 512 + 16 + (i & 63) * 4; }
DI u32x4 join16(const f32x4& a, const f32x4& b, float sa, float sb) {
    auto r0 = __builtin_amdgcn_permlane16_swap(pack2(a[0] * sa, a[1] * sa), pack2(b[0] * sb, b[1] * sb), false, false);
    auto r1 = __builtin_amdgcn_permlane16_swap(pack2(a[2] * sa, a[3] * sa), pack2(b[2] * sb, b[3] * sb), false, false);
    return (u32x4){r0[0], r1[0], r0[1], r1[1]}; }
DI f32x16 exp16(f32x16 s) {
#pragma unroll
    for (int i = 0; i < 16; ++i) s[i] = __builtin_amdgcn_exp2f(s[i]);
    return s; }
DI f32x16 pv(bf16x8 v0, bf16x8 v1, const f32x16& p, f32x16 o, int idx) {
    const f32x8 lo = __builtin_shufflevector(p, p, 0, 1, 2, 3, 4, 5, 6, 7), hi = __builtin_shufflevector(p, p, 8, 9, 10, 11, 12, 13, 14, 15);
    o = __builtin_amdgcn_smfmac_f32_32x32x32_bf16(v0, __builtin_bit_cast(bf16x16, lo), o, idx, 0, 0);
    o = __builtin_amdgcn_smfmac_f32_32x32x32_bf16(v1, __builtin_bit_cast(bf16x16, hi), o, idx, 0, 0);
    return o; }
DI float rowmax16(const f32x16& s) {
    float m = __builtin_fmaxf(__builtin_fmaxf(s[0], s[1]), s[2]);
#pragma unroll
    for (int i = 3; i < 15; i += 2) m = __builtin_fmaxf(__builtin_fmaxf(m, s[i]), s[i + 1]);
    return __builtin_fmaxf(m, s[15]); }
DI float xhalf_max(float m) { auto r = __builtin_amdgcn_permlane32_swap(F2U(m), F2U(m), false, false); return __builtin_fmaxf(U2F(r[0]), U2F(r[1])); }

__global__ __launch_bounds__(768, 3) NOPK void gated_mix_fused(const unsigned short* __restrict__ x, const unsigned short* __restrict__ wqkv, const unsigned short* __restrict__ wo,
                                                         const unsigned short* __restrict__ w1, const unsigned short* __restrict__ b1, const unsigned short* __restrict__ w2,
                                                         const unsigned short* __restrict__ b2, const unsigned short* __restrict__ gate_w, unsigned short* __restrict__ out, char* ws) {
    __shared__ __attribute__((aligned(1024))) char smem[163840];
    LAS char* lds = (LAS char*)smem;
    const unsigned lds0 = (unsigned)(uintptr_t)lds;
    const int wid = __builtin_amdgcn_readfirstlane(__builtin_amdgcn_workitem_id_x() >> 6);
#define LANES() int lane = __builtin_amdgcn_mbcnt_hi(~0u, __builtin_amdgcn_mbcnt_lo(~0u, 0u)); PIN(lane); const int r32 = lane & 31, hh = lane >> 5, g4 = lane >> 4, r16 = lane & 15, q4 = r16 >> 2, p4 = lane & 3; (void)r32; (void)hh; (void)g4; (void)q4; (void)p4
    const int bx = __builtin_amdgcn_workgroup_id_x(), hd = 8 * (bx & 3) + ((bx >> 3) & 7), t = 4 * ((bx >> 2) & 1) + (bx >> 6);
    const __amdgpu_buffer_rsrc_t rs = __builtin_amdgcn_make_buffer_rsrc(ws, 0, WS_END, 0x00020000);
    LAS unsigned* sy = (LAS unsigned*)(lds + L_SYNC);
    asm volatile("" :: "s"(x), "s"(wqkv), "s"(wo), "s"(w1), "s"(b1), "s"(w2), "s"(b2), "s"(gate_w), "s"(out), "s"(ws));
    u64 tag; u32x2 ep;
#define EPOCH_LOAD() asm volatile("global_load_dwordx2 %0, %1, %2" : "=v"(ep) : "v"(0), "s"(ws) : "memory")
#define EPOCH_TAKE() do { PIN(ep); tag = (((u64)(unsigned)__builtin_amdgcn_readfirstlane(ep[1]) << 32) | (unsigned)__builtin_amdgcn_readfirstlane(ep[0])) + 1ull; } while (0)
    const int cp = hd & 7, rq = hd >> 3;
    const bool hasF = cp != 7; const int fb = hd - rq;
    const int rot = ROTX;
    STAMP(0);
#define CS(c) (((c) + rot) & 7)
#define BUFOF(c) ((((c) + 1) % 3) * BUFB)
#define CHUNK_SYNC(c, n) do { if ((c) < 7) asm volatile("s_waitcnt vmcnt(" #n ") lgkmcnt(0)\n\ts_barrier" ::: "memory"); else asm volatile("s_waitcnt vmcnt(0) lgkmcnt(0)\n\ts_barrier" ::: "memory"); } while (0)

    if (wid < 8) {
        f32x4 aq[2] = {}, ak[2] = {}, av[2] = {};
        { LANES(); const int xsw = g4 ^ (r16 >> 1);
          const int c0 = (lane & 7) ^ (lane >> 4), rho = lane >> 1, r8 = lane >> 3;
          const unsigned dvo0 = r8 * 1024 + c0 * 16, dvo1 = r8 * 1024 + (c0 ^ 4) * 16;
          const unsigned dvq = (8 * ((rho >> 2) & 3) + 4 * (rho >> 4) + (rho & 3)) * 3072 + (lane & 1) * 16;
          const unsigned dvf = (8 * (r8 >> 2) + (r8 & 3)) * 3584 + (((lane & 7) ^ (r8 & 6)) * 16);
          const char* xbase = (const char*)x + (size_t)(256 * t + 32 * wid) * 1024;
          const bool hq = wid < 6, hf = hasF;
          const char* qbase = (const char*)wqkv + hd * 32 + (hq ? (wid >> 1) * 1024 + (wid & 1) * 32 * 3072 : 0);
          const char* fbase = (const char*)w1 + (hf ? fb * 128 : 0) + (32 * (wid >> 2) + 16 * (wid & 1) + 4 * ((wid >> 1) & 1)) * 3584;
          const unsigned vq_ = hq ? dvq : 0u, vf_ = hf ? dvf : 0u;
#define DMA_X(c) do { _Pragma("unroll") for (int i = 0; i < 4; ++i) glds16((i & 1) ? dvo1 : dvo0, xbase + CS(c) * 128 + i * 8192, lds0 + BUFOF(c) + wid * 4096 + i * 1024); } while (0)
#define DMA_ALL(c) do { glds16(vq_, qbase + CS(c) * 64 * 3072, hq ? lds0 + BUFOF(c) + XB + wid * 1024 : lds0 + L_DUMMY); \
              glds16(vf_, fbase + CS(c) * 64 * 3584, hf ? lds0 + BUFOF(c) + XB + 6144 + wid * 1024 : lds0 + L_DUMMY); DMA_X(c); } while (0)
          DMA_ALL(0);
          EPOCH_LOAD();
          if (wid == 0) {
              if (lane < SY_N) sy[lane] = 0;
              if (lane < 8) { const unsigned one = lane < 4 ? 0x3F803F80u : 0u; *(LAS u32x4*)(lds + L_MISC + (lane & 3) * 512 + (lane >> 2) * 496) = (u32x4){one, one, one, one}; }
          }
          __builtin_amdgcn_s_sleep(4);
          DMA_ALL(1);
          STAMP(20);
          const int xo = (32 * wid + r16) * 128, wo_ = XB + (4 * g4 + q4) * 32 + p4 * 8;
          bf16x8 xa[2], xb[2], wq[2], wk[2], wv[2];
#define RX(d, c, ks, rt) d[ks] = ldsr(lds + BUFOF(c) + xo + (rt) * 2048 + ((xsw ^ (4 * (ks))) * 16))
#define RW(d, c, ks, im) d[ks] = ldtr(lds + BUFOF(c) + wo_ + (im) * 2048 + (ks) * 1024, lds + BUFOF(c) + wo_ + (im) * 2048 + (ks) * 1024 + 512)
#define M2(A, W, ks) do { A[0] = MFMA16(W[ks], xa[ks], A[0]); A[1] = MFMA16(W[ks], xb[ks], A[1]); PIN(A[0]); PIN(A[1]); SBAR(); } while (0)
#define MV(ks) do { av[0] = MFMA16(xa[ks], wv[ks], av[0]); av[1] = MFMA16(xb[ks], wv[ks], av[1]); PIN(av[0]); PIN(av[1]); SBAR(); } while (0)
#pragma unroll
          for (int c = 0; c < 8; ++c) {
              CHUNK_SYNC(c, 6);
              STAMP(12 + c);
              if (c == 7) { EPOCH_TAKE(); __builtin_amdgcn_s_setprio(3); }
              if (c + 2 < 8 && wid >= DSPLIT) DMA_ALL(c + 2);
              if (c > 0) M2(aq, wq, 1);
              RX(xa, c, 0, 0); RX(xb, c, 0, 1); RW(wq, c, 0, 0); SBAR();
              if (c > 0) M2(ak, wk, 1);
              RW(wk, c, 0, 1); RW(wv, c, 0, 2); SBAR();
              if (c > 0) MV(1);
              RX(xa, c, 1, 0); RX(xb, c, 1, 1); RW(wq, c, 1, 0); SBAR();
              M2(aq, wq, 0);
              RW(wk, c, 1, 1); RW(wv, c, 1, 2); SBAR();
              M2(ak, wk, 0);
              MV(0);
              if (c + 2 < 8 && wid < DSPLIT) DMA_ALL(c + 2);
          }
          M2(aq, wq, 1); M2(ak, wk, 1); MV(1);
#undef RX
#undef RW
#undef M2
#undef MV
#undef DMA_ALL
#undef DMA_X
        }
        STAMP(1);
        LANES();
        constexpr float QS = 0.25f * 1.4426950408889634f;
        const bf16x8 qf = __builtin_bit_cast(bf16x8, join16(aq[0], aq[1], QS, QS));
        const unsigned kvg = WS_KV + (unsigned)(hd * 8 + t) * 16384u, og = WS_O + (unsigned)((8 * t + wid) * 32 + hd) * 1024u;
        { const u32x4 kk = join16(ak[0], ak[1], 1.f, 1.f), vv = join16(av[0], av[1], 1.f, 1.f);
          const int ok = wid * 1024 + lane * 16, ov = wid * 1024 + (g4 & 1) * 512 + hh * 256 + r16 * 16;
          *(LAS u32x4*)(lds + ok) = kk; *(LAS u32x4*)(lds + L_V + ov) = vv;
          asm volatile("s_waitcnt lgkmcnt(0)" ::: "memory");
          if (lane == 0) __hip_atomic_fetch_add(sy + SY_AW, 1u, RLX_WG);
          st1(kk, rs, ok, kvg); st1(vv, rs, ov + 8192, kvg); }
        lds_wait_ge<1>(sy + SY_AW, 8);
        STAMP(2);
        drain();
        if (lds_arrive(sy + SY_KV, lane) == 7 && lane == 0) __hip_atomic_store((GAS u64*)(ws + flag_at(0, hd, t)), tag, RLX_AGENT);
        const unsigned rkv = WS_KV + (unsigned)(hd * 8 + ((t + wid) & 7)) * 16384u;
#define FETCH(NB) do { poll_tags(ws, flag_at(0, hd, (t + wid) & 7), lane == 0, tag); \
            _Pragma("unroll") for (int b_ = 0; b_ < 16 / (NB); ++b_) { u32x4 stg[NB]; \
                _Pragma("unroll") for (int i = 0; i < (NB); ++i) stg[i] = ld1(rs, lane * 16 + (b_ * (NB) + i) * 1024, rkv); \
                if (b_ == 0) lds_wait_ge(sy + SY_FA, 4);                         \
                _Pragma("unroll") for (int i = 0; i < (NB); ++i) *(LAS u32x4*)(lds + ((b_ * (NB) + i) >> 3) * L_V + wid * 8192 + ((b_ * (NB) + i) & 7) * 1024 + lane * 16) = stg[i]; } \
            asm volatile("s_waitcnt lgkmcnt(0)" ::: "memory"); \
            if (lane == 0) __hip_atomic_store(sy + SY_READY + wid, 1u, RLX_WG); } while (0)
        if (wid > 0 && wid < EARLYW) FETCH(16);
        const int idx = 0xDDDD;
        const LAS char* kp = lds + lane * 16;
        const bool vl = r32 < 16;
        const LAS char* vp = lds + (vl ? L_V + 512 + hh * 256 + r32 * 16 : (r32 & 11) == 0 ? L_MISC : L_MISC + 496);
        const int vstep = vl ? 2048 : 0;
        f32x16 o = (f32x16){}, negm, sA, sB = (f32x16){};
        bf16x8 kf = ldsr(kp), v0 = ldsr(vl ? vp - 512 : vp), v1 = (bf16x8){};
        { const f32x16 s0 = MFMA(kf, qf, (f32x16){}); const float m0 = -xhalf_max(rowmax16(s0));
#pragma unroll
          for (int i = 0; i < 16; ++i) negm[i] = m0; }
        sA = MFMA(kf, qf, negm);
        kf = ldsr(kp + 1024);
        STAMP(10);
        __builtin_amdgcn_s_setprio(0);
#define EX(S, a, b) do { _Pragma("unroll") for (int i_ = (a); i_ < (b); ++i_) S[i_] = __builtin_amdgcn_exp2f(S[i_]); PIN(S); SBAR(); } while (0)
#define LO8(S) __builtin_bit_cast(bf16x16, __builtin_shufflevector(S, S, 0, 1, 2, 3, 4, 5, 6, 7))
#define HI8(S) __builtin_bit_cast(bf16x16, __builtin_shufflevector(S, S, 8, 9, 10, 11, 12, 13, 14, 15))
#define SM(a, b) do { o = __builtin_amdgcn_smfmac_f32_32x32x32_bf16(a, b, o, idx, 0, 0); PIN(o); SBAR(); } while (0)
#define TILE(CUR, OTH, KO, VO) do { SBAR(); EX(CUR, 0, 2); SM(v1, HI8(OTH)); v1 = ldsr(vp + (VO)); SBAR(); EX(CUR, 2, 6); \
            OTH = MFMA(kf, qf, negm); PIN(OTH); SBAR(); kf = ldsr(kp + (KO)); SBAR(); EX(CUR, 6, 10); SM(v0, LO8(CUR)); v0 = ldsr(vp + (VO) + 512); SBAR(); EX(CUR, 10, 16); } while (0)
#define LEAPFROG(pos) do { if (lane == 0) __hip_atomic_store(sy + SY_PROG + wid, (unsigned)(pos), RLX_WG); \
            if ((unsigned)__builtin_amdgcn_readfirstlane(__hip_atomic_load(sy + SY_PROG + (wid ^ 4), RLX_WG)) >= (unsigned)(pos)) __builtin_amdgcn_s_setprio(1); else __builtin_amdgcn_s_setprio(0); } while (0)
#define PAIR(KO) do { TILE(sA, sB, (KO) + 2048, 0); TILE(sB, sA, (KO) + 3072, 1024); vp += vstep; } while (0)
        PAIR(0); PAIR(2048);
        PAIR(4096);
        lds_wait_ge(sy + SY_READY + 1, 1);
        PAIR(6144);
        if (wid >= EARLYW) { __builtin_amdgcn_s_setprio(3); FETCH(8); }
#undef FETCH
        STAMP(3);
        for (int j = 1; j < 8; ++j) {
            kp += 8192;
            if (j > 1 && j < 6) STAMP(4 + j);
            LEAPFROG(2 * j);
            PAIR(0); PAIR(2048);
            if (LEAP == 2) LEAPFROG(2 * j + 1);
            PAIR(4096);
            if (j < 7) lds_wait_ge(sy + SY_READY + j + 1, 1);
            PAIR(6144);
            if (j == 4 && lane == 0) __hip_atomic_fetch_add(sy + SY_PASS, 1u, RLX_WG);
            if (j == TRIGJ && lane == 0) __hip_atomic_fetch_add(sy + SY_TRIG, 1u, RLX_WG);
        }
        SM(v1, HI8(sB));
#undef PAIR
#undef LEAPFROG
#undef TILE
#undef SM
#undef EX
        asm volatile("s_waitcnt lgkmcnt(0)" ::: "memory");
        if (lane == 0) __hip_atomic_fetch_add(sy + SY_DONE, 1u, RLX_WG);
        STAMP(4);
        { float chk = o[8] * 0.f;
#pragma unroll
          for (int i = 0; i < 8; ++i) chk = __builtin_fmaf(o[i], 0.f, chk);
          if (__builtin_expect(0 != __builtin_amdgcn_ballot_w64(!(chk == 0.f) || o[8] > 1.8e19f), 0)) {
              float m = -__builtin_inff();
#pragma nounroll
              for (int tl = 0; tl < 64; ++tl) { const bf16x8 kg = __builtin_bit_cast(bf16x8, ld1(rs, lane * 16, WS_KV + (unsigned)(hd * 8 + (tl >> 3)) * 16384u + (tl & 7) * 1024));
                  m = __builtin_fmaxf(m, rowmax16(MFMA(kg, qf, (f32x16){}))); }
              m = -xhalf_max(m);
#pragma unroll
              for (int i = 0; i < 16; ++i) negm[i] = m;
              o = (f32x16){};
              const u32x4 ones = {0x3F803F80u, 0x3F803F80u, 0x3F803F80u, 0x3F803F80u};
#pragma nounroll
              for (int tl = 0; tl < 64; ++tl) { const unsigned so = WS_KV + (unsigned)(hd * 8 + (tl >> 3)) * 16384u + (tl & 7) * 1024;
                  const bf16x8 kg = __builtin_bit_cast(bf16x8, ld1(rs, lane * 16, so));
                  const u32x4 a0 = ld1(rs, 8192 + hh * 256 + r16 * 16, so), a1 = ld1(rs, 8192 + 512 + hh * 256 + r16 * 16, so);
                  o = pv(__builtin_bit_cast(bf16x8, vl ? a0 : ones), __builtin_bit_cast(bf16x8, vl ? a1 : ones), exp16(MFMA(kg, qf, negm)), o, idx); }
          } }
        lds_wait_ge(sy + SY_FSEEN, 4);
        { u32x4 ov = pack8(o, 0, *(const LAS float*)(lds + g_at(32 * wid + r32)) * __builtin_amdgcn_rcpf(o[8]));
          st1(unmark(ov), rs, lane * 16, og); }
        STAMP(5);
        { u32x4 of[8];
          fetch_marked(of, rs, lane * 16, WS_O + (unsigned)((8 * t + 2 * rq) * 32 + 4 * wid) * 1024u, 32768, 1024);
          STAMP(11);
          lds_wait_ge(sy + SY_DONE, 8);
#pragma unroll
          for (int i = 0; i < 8; ++i) *(LAS u32x4*)(lds + (wid >> 2) * L_V + 32768 + (wid & 3) * 8192 + i * 1024 + lane * 16) = of[i];
          asm volatile("s_waitcnt lgkmcnt(0)" ::: "memory");
          if (lane == 0) __hip_atomic_store(sy + SY_OL + wid, 1u, RLX_WG); }
        asm volatile(".Lgm_end:");
        STAMP(21); STAMP_FLUSH();
    } else {
        const int f = wid - 8, i32 = rq + 4 * t;
        { LANES(); const int xsw = g4 ^ (r16 >> 1);
          if (FPA) __builtin_amdgcn_s_setprio(1);
          unsigned d0 = 0;
          EPOCH_LOAD();
          const int l6 = ((lane - 48) * 43) >> 8, m6 = lane - 48 - 6 * l6;
          const char* tp = lane < 32 ? (const char*)x + (size_t)(256 * t + 32 * cp + lane) * 1024 : lane < 48 ? (const char*)w1 + (hasF ? fb : 0) * 128 + (16 * (t & 3) + lane - 32) * 3584
                                     : (const char*)wqkv + (hd & 24) * 32 + (2 * (cp + 8 * (t & 3)) + (l6 & 1)) * 3072 + (m6 >> 1) * 1024 + (m6 & 1) * 128;
          const unsigned tstep = lane < 32 ? 128 : lane < 48 ? 64 * 3584 : 64 * 3072;
#define TOUCH(c) do { if (TPF && lane < 60) asm volatile("global_load_dword %0, %1, off" : "+v"(d0) : "v"(tp + (c) * tstep) : "memory"); } while (0)
          unsigned d1 = 0;
          if (CPF && f == 3) {
              const char* pc; unsigned len;
              asm volatile("s_getpc_b64 %0\n.Lgm_ref:\n\ts_mov_b32 %1, .Lgm_end-.Lgm_ref" : "=s"(pc), "=s"(len));
              const unsigned po = (unsigned)((bx >> 3) + 32 * lane) * 128u;
              if ((int)po < (int)len) asm volatile("global_load_dword %0, %1, %2" : "+v"(d1) : "v"(po), "s"(pc) : "memory");
          }
          if (hd == 31) { glds16(lane * 16, gate_w, lds0 + L_GATE + f * 1024); asm volatile("s_waitcnt vmcnt(0)" ::: "memory"); }
          __builtin_amdgcn_s_sleep(TSLP);
          if (TPF == 2) { TOUCH(2 + f); if (f < 2) TOUCH(6 + f); } else if (f < TLA - 2) TOUCH(2 + f);
          const int xo = (64 * f + r16) * 128, fo = XB + 6144 + (4 * g4 + q4) * 128 + p4 * 8, fsw = (2 * g4 + (q4 >> 1)) & 3;
          bf16x8 xf[2][4], wf[2][4];
#define RX(c, ks, rt) xf[ks][rt] = ldsr(lds + BUFOF(c) + xo + (rt) * 2048 + ((xsw ^ (4 * (ks))) * 16))
#define RW(c, ks, cb) wf[ks][cb] = ldtr(lds + BUFOF(c) + fo + (ks) * 4096 + (((cb) ^ fsw) * 32), lds + BUFOF(c) + fo + (ks) * 4096 + 2048 + (((cb) ^ fsw) * 32))
#define M2(ks, cb, r0) do { af[cb][r0] = MFMA16(wf[ks][cb], xf[ks][r0], af[cb][r0]); af[cb][r0 + 1] = MFMA16(wf[ks][cb], xf[ks][r0 + 1], af[cb][r0 + 1]); PIN(af[cb][r0]); PIN(af[cb][r0 + 1]); SBAR(); } while (0)
#define F_SYNC(c) do { if ((c) < 7) asm volatile("s_waitcnt lgkmcnt(0)\n\ts_barrier" ::: "memory"); else CHUNK_SYNC(c, 0); \
              STAMP(12 + (c)); \
              if ((c) == 4 || (c) == RTC) { LANES(); \
              if ((c) == 4) {     \
                  _Pragma("unroll") for (int i = 0; i < 2; ++i) st1((u32x4){SENT, SENT, SENT, SENT}, rs, lane * 16, WS_O + (unsigned)((8 * t + 2 * f + i) * 32 + hd) * 1024u); } \
              if (TPF == 1 && (c) + TLA < 8 && f == ((c) & 3)) TOUCH((c) + TLA); \
              if ((c) == RTC) {   \
                  const int u = i32 + 32 * f; const char* src = u < 112 ? (const char*)w2 + (size_t)u * 16384 : (const char*)wo + (size_t)(u - 112) * 16384; \
                  const unsigned vo = (lane >> 3) * 1024 + cp * 128 + (lane & 7) * 16; \
                  glds16(vo, src, lds0 + L_RT + f * 2048); glds16(vo, src + 8192, lds0 + L_RT + f * 2048 + 1024); \
                  if (f == 0 && i32 < 16) { const char* s2 = (const char*)wo + (size_t)(i32 + 16) * 16384; glds16(vo, s2, lds0 + L_RT + 8192); glds16(vo, s2 + 8192, lds0 + L_RT + 8192 + 1024); } \
                  if (f == 1 && lane < 32) { if (hasF) glds4(lane * 4, (const char*)b1 + fb * 128, lds0 + L_B1); glds4(lane * 4, (const char*)b2 + cp * 128, lds0 + L_B2); } } \
              } if ((c) == 7) { EPOCH_TAKE(); PIN(d0); PIN(d1); } } while (0)
#define F_LEAVE() STAMP(1); LANES(); \
              asm volatile("s_waitcnt lgkmcnt(0)" ::: "memory"); \
              if (lane == 0) __hip_atomic_fetch_add(sy + SY_FA, 1u, RLX_WG); \
              __builtin_amdgcn_s_setprio(2); \
              for (int n = f; n < ((f == 0 && i32 < 16) ? 5 : 4); n += 4) {        \
                  const int u = i32 + 32 * n, r0 = (u < 112 ? 8 : 4) * hh + q4, r1 = u < 112 ? 512 : 1024; \
                  _Pragma("unroll") for (int ct = 0; ct < 2; ++ct) { const LAS char* s = lds + L_RT + n * 2048 + r0 * 128 + (g4 & 1) * 32 + p4 * 8 + ct * 64; \
                      st1(__builtin_bit_cast(u32x4, ldtr(s, s + r1)), rs, lane * 16, WS_W + (unsigned)((2 * cp + ct) * 144 + u) * 1024u); } }
          if (hasF) {
              f32x4 af[4][4] = {};
#pragma unroll
              for (int c = 0; c < 8; ++c) {
                  F_SYNC(c);
                  if (FDEFER && c == 7) lds_wait_ge<2>(sy + SY_AW, 8);
                  if (c > 0) M2(1, 0, 0);
                  RX(c, 0, 0); SBAR();
                  if (c > 0) M2(1, 0, 2);
                  RW(c, 0, 0); SBAR();
                  if (c > 0) M2(1, 1, 0);
                  RX(c, 0, 1); SBAR();
                  if (c > 0) M2(1, 1, 2);
                  RX(c, 0, 2); SBAR();
                  if (c > 0) M2(1, 2, 0);
                  RX(c, 0, 3); SBAR();
                  if (c > 0) M2(1, 2, 2);
                  RW(c, 0, 1); SBAR();
                  if (c > 0) M2(1, 3, 0);
                  RW(c, 0, 2); SBAR();
                  if (c > 0) M2(1, 3, 2);
                  RW(c, 0, 3); SBAR();
                  M2(0, 0, 0); RX(c, 1, 0); RW(c, 1, 0); SBAR();
                  M2(0, 0, 2); RX(c, 1, 1); RX(c, 1, 2); SBAR();
                  M2(0, 1, 0); RX(c, 1, 3); RW(c, 1, 1); SBAR();
                  M2(0, 1, 2); RW(c, 1, 2); RW(c, 1, 3); SBAR();
                  M2(0, 2, 0); M2(0, 2, 2); M2(0, 3, 0); M2(0, 3, 2);
              }
              M2(1, 0, 0); M2(1, 0, 2); M2(1, 1, 0); M2(1, 1, 2); M2(1, 2, 0); M2(1, 2, 2); M2(1, 3, 0); M2(1, 3, 2);
              { F_LEAVE();
#pragma unroll
                for (int cb = 0; cb < 4; ++cb) {
                    const u32x2 w = *(const LAS u32x2*)(lds + L_B1 + cb * 32 + g4 * 8);
                    const float bb[4] = {U2F(w[0] << 16), U2F(w[0] & 0xffff0000u), U2F(w[1] << 16), U2F(w[1] & 0xffff0000u)};
#pragma unroll
                    for (int rp = 0; rp < 2; ++rp) {
                        float v[8], e[8];
#pragma unroll
                        for (int i = 0; i < 8; ++i) { v[i] = af[cb][2 * rp + (i >> 2)][i & 3] + bb[i & 3];
                            e[i] = v[i] * __builtin_fmaf(v[i] * v[i], -2.f * 0.7978845608028654f * 1.4426950408889634f * 0.044715f, -2.f * 0.7978845608028654f * 1.4426950408889634f); }
                        SBAR();
#pragma unroll
                        for (int i = 0; i < 8; ++i) e[i] = __builtin_amdgcn_exp2f(e[i]);
                        SBAR();
#pragma unroll
                        for (int i = 0; i < 8; ++i) e[i] = 1.f + e[i];
                        SBAR();
#pragma unroll
                        for (int i = 0; i < 8; ++i) e[i] = __builtin_amdgcn_rcpf(e[i]);
                        SBAR();
                        st1(join16((f32x4){v[0] * e[0], v[1] * e[1], v[2] * e[2], v[3] * e[3]}, (f32x4){v[4] * e[4], v[5] * e[5], v[6] * e[6], v[7] * e[7]}, 1.f, 1.f), rs, lane * 16,
                            WS_H + (unsigned)((8 * t + 2 * f + rp) * 112 + 4 * fb + cb) * 1024u);
                    }
                } }
          } else {
              f32x4 az[4] = {};
#pragma unroll
              for (int c = 0; c < 8; ++c) {
                  F_SYNC(c);
                  if (hd == 31) {
#pragma unroll
                      for (int ks = 0; ks < 2; ++ks) { const bf16x8 gf = ldsr(lds + L_GATE + f * 1024 + CS(c) * 128 + ks * 64 + g4 * 16);
#pragma unroll
                          for (int rt = 0; rt < 4; ++rt) { RX(c, ks, rt); az[rt] = MFMA16(gf, xf[ks][rt], az[rt]); } }
                  }
              }
              { F_LEAVE();
                if (hd == 31) {
                    const float z = bfround(g4 == 0 ? az[0][0] : g4 == 1 ? az[1][0] : g4 == 2 ? az[2][0] : az[3][0]);
                    __hip_atomic_store((GAS float*)(ws + WS_G + (256 * t + 64 * f + lane) * 4), bfround(__builtin_amdgcn_rcpf(1.f + __builtin_amdgcn_exp2f(-1.4426950408889634f * z))), RLX_AGENT);
                } }
          }
#undef F_SYNC
#undef F_LEAVE
#undef RX
#undef RW
#undef M2
#undef TOUCH
        }
        LANES();
        const int freg = (f >> 1) * L_V + (f & 1) * 16384;
        drain();
        if (lds_arrive(sy + SY_F, lane) == 3 && lane == 0) __hip_atomic_store((GAS u64*)(ws + flag_at(1, hd, t)), tag, RLX_AGENT);
        STAMP(3);
        f32x16 acc[4] = {};
        poll_tags<4>(ws, hh ? flag_at(1, cp + 8 * (r32 & 3), r32 >> 2) : flag_at(1, r32, t), true, tag);
        *(LAS float*)(lds + g_at(64 * f + lane)) = __hip_atomic_load((GAS float*)(ws + WS_G + (256 * t + 64 * f + lane) * 4), RLX_AGENT);
        asm volatile("s_waitcnt lgkmcnt(0)" ::: "memory");
        if (lane == 0) __hip_atomic_fetch_add(sy + SY_FSEEN, 1u, RLX_WG);
        __builtin_amdgcn_s_setprio(F2_PRIO);
        STAMP(4);
        const unsigned hb = WS_H + (unsigned)((8 * t + 2 * rq) * 112 + 28 * f) * 1024u, wb = WS_W + (unsigned)(2 * cp * 144 + 28 * f) * 1024u;
        u32x4 fr[4][4];
#define LDK(slot, n) do { fr[slot][0] = ld1(rs, lane * 16, wb + (n) * 1024); fr[slot][1] = ld1(rs, lane * 16, wb + (144 + (n)) * 1024); \
                          fr[slot][2] = ld1(rs, lane * 16, hb + (n) * 1024); fr[slot][3] = ld1(rs, lane * 16, hb + (112 + (n)) * 1024); } while (0)
#define BC(v) __builtin_bit_cast(bf16x8, v)
#define MM4(w0, w1_, h0, h1) do { acc[0] = MFMA(w0, h0, acc[0]); acc[1] = MFMA(w0, h1, acc[1]); acc[2] = MFMA(w1_, h0, acc[2]); acc[3] = MFMA(w1_, h1, acc[3]); PIN(acc[0]); PIN(acc[1]); PIN(acc[2]); PIN(acc[3]); SBAR(); } while (0)
#define FFN2(n0, n1, pace) do { _Pragma("unroll") for (int n = (n0); n < (n1); ++n) { MM4(BC(fr[n & 3][0]), BC(fr[n & 3][1]), BC(fr[n & 3][2]), BC(fr[n & 3][3])); if (n + 4 < (n1)) LDK(n & 3, n + 4); if (pace) __builtin_amdgcn_s_sleep(pace); SBAR(); } } while (0)
#pragma unroll
        for (int n = 0; n < 4; ++n) LDK(n, n);
        FFN2(0, NP1, F2_PACE);
        STAMP(5);
        { u32x4 tmp[16];
#pragma unroll
          for (int i = 0; i < 16; ++i) tmp[i] = ld1(rs, lane * 16, WS_W + (unsigned)((2 * cp + (i & 1)) * 144 + 112 + 8 * f + (i >> 1)) * 1024u);
          __builtin_amdgcn_s_setprio(2);
          lds_wait_ge<16>(sy + SY_PASS, 8);
#pragma unroll
          for (int i = 0; i < 16; ++i) *(LAS u32x4*)(lds + freg + i * 1024 + lane * 16) = tmp[i]; }
        STAMP(6);
        if (NP1 < 28) {
#pragma unroll
            for (int n = NP1; n < NP1 + 4; ++n) LDK(n & 3, n);
            lds_wait_ge<8>(sy + SY_TRIG, 8);
            __builtin_amdgcn_s_setprio(F2_PRIO);
            FFN2(NP1, 28, 0);
        }
#undef LDK
#undef FFN2
        lds_wait_ge(sy + SY_FSEEN, 4);
        { const float og0 = bfround(1.f - *(const LAS float*)(lds + g_at(64 * rq + r32))), og1 = bfround(1.f - *(const LAS float*)(lds + g_at(64 * rq + 32 + r32)));
#pragma unroll
          for (int ct = 0; ct < 2; ++ct)
#pragma unroll
              for (int g = 0; g < 4; ++g) { u32x2 w = *(const LAS u32x2*)(lds + L_B2 + ct * 64 + g * 16 + hh * 8); if (f != 0) w = (u32x2){0u, 0u};
                  const float bb[4] = {U2F(w[0] << 16), U2F(w[0] & 0xffff0000u), U2F(w[1] << 16), U2F(w[1] & 0xffff0000u)};
#pragma unroll
                  for (int i = 0; i < 4; ++i) { acc[2 * ct][4 * g + i] = (acc[2 * ct][4 * g + i] + bb[i]) * og0; acc[2 * ct + 1][4 * g + i] = (acc[2 * ct + 1][4 * g + i] + bb[i]) * og1; } } }
        STAMP(7);
        __builtin_amdgcn_s_setprio(3);
#pragma unroll
        for (int n = 0; n < 8; ++n) { if ((n & 3) == 0) lds_wait_ge<1>(sy + SY_OL + 2 * f + (n >> 2), 1);
            if (n == 4) STAMP(10);
            const LAS char* p = lds + freg + n * 2048 + lane * 16;
            const bf16x8 w0 = ldsr(p), w1_ = ldsr(p + 1024), h0 = ldsr(p + 32768), h1 = ldsr(p + 32768 + 1024); MM4(w0, w1_, h0, h1); }
#undef MM4
#undef BC
        STAMP(8);
        f32x16 own = f == 0 ? acc[0] : f == 1 ? acc[1] : f == 2 ? acc[2] : acc[3];
#pragma unroll
        for (int qd = 0; qd < 4; ++qd) if (qd != f) {
#pragma unroll
            for (int g = 0; g < 4; ++g) *(LAS f32x4*)(lds + freg + qd * 4096 + g * 1024 + lane * 16) = (f32x4){acc[qd][4 * g], acc[qd][4 * g + 1], acc[qd][4 * g + 2], acc[qd][4 * g + 3]}; }
        asm volatile("s_waitcnt lgkmcnt(0)" ::: "memory");
        if (lane == 0) __hip_atomic_fetch_add(sy + SY_RED, 1u, RLX_WG);
        lds_wait_ge<0>(sy + SY_RED, 4);
        unsigned pk[8];
#pragma unroll
        for (int g = 0; g < 4; ++g) { f32x4 s = {own[4 * g], own[4 * g + 1], own[4 * g + 2], own[4 * g + 3]};
#pragma unroll
            for (int k = 1; k < 4; ++k) { const int w = (f + k) & 3; s += *(const LAS f32x4*)(lds + (w >> 1) * L_V + (w & 1) * 16384 + f * 4096 + g * 1024 + lane * 16); }
            pk[2 * g] = pack2(s[0], s[1]); pk[2 * g + 1] = pack2(s[2], s[3]); }
        unsigned short* orow = out + (size_t)(256 * t + 64 * rq + 32 * (f & 1) + r32) * DM + 64 * cp + 32 * (f >> 1) + 8 * hh;
#pragma unroll
        for (int k = 0; k < 2; ++k) {
            auto a = __builtin_amdgcn_permlane32_swap(pk[4 * k], pk[4 * k + 2], false, false), b = __builtin_amdgcn_permlane32_swap(pk[4 * k + 1], pk[4 * k + 3], false, false);
            const u32x4 v_ = {a[0], b[0], a[1], b[1]};
            if (OSC) asm volatile("global_store_dwordx4 %0, %1, off sc0 sc1\n\ts_nop 1" :: "v"(orow + 16 * k), "v"(v_) : "memory"); else *(u32x4*)(orow + 16 * k) = v_;
        }
        if (bx == 0 && f == 0 && lane == 0) __hip_atomic_store((GAS u64*)(ws + WS_FLAGS), tag, RLX_AGENT);
        STAMP(9); STAMP_FLUSH();
    }
}

extern "C" void kernel_launch(void* const* d_in, const int* in_sizes, int n_in, void* d_out, int out_size, void* d_ws, size_t ws_size, hipStream_t stream) {
    (void)in_sizes; (void)n_in; (void)out_size; (void)ws_size;
    hipLaunchKernelGGL(gated_mix_fused, dim3(256), dim3(768), 0, stream, (const unsigned short*)d_in[0], (const unsigned short*)d_in[1], (const unsigned short*)d_in[2],
                       (const unsigned short*)d_in[4], (const unsigned short*)d_in[5], (const unsigned short*)d_in[6], (const unsigned short*)d_in[7], (const unsigned short*)d_in[8],
                       (unsigned short*)d_out, (char*)d_ws);
}
```

```cpp
#include <hip/hip_runtime.h>
#include <hip/hip_bf16.h>
#include <stdint.h>

typedef __attribute__((ext_vector_type(8))) __bf16 bf16x8;
typedef __attribute__((ext_vector_type(16))) __bf16 bf16x16;
typedef __attribute__((ext_vector_type(2))) __bf16 bf16x2;
typedef __attribute__((ext_vector_type(4))) short s16x4;
typedef __attribute__((ext_vector_type(16))) float f32x16;
typedef __attribute__((ext_vector_type(8))) float f32x8;
typedef __attribute__((ext_vector_type(4))) float f32x4;
typedef __attribute__((ext_vector_type(2))) float f32x2;
typedef __attribute__((ext_vector_type(4))) unsigned u32x4;
typedef __attribute__((ext_vector_type(2))) unsigned u32x2;
typedef unsigned long long u64;
#define LAS __attribute__((address_space(3)))
#define GAS __attribute__((address_space(1)))
#if defined(__HIP_DEVICE_COMPILE__) && !defined(ALLOW_PK)
#define NOPK __attribute__((target("no-packed-fp32-ops")))
#else
#define NOPK
#endif
#define DI __device__ __forceinline__ NOPK
#define U2F(x) __builtin_bit_cast(float, (unsigned)(x))
#define F2U(x) __builtin_bit_cast(unsigned, (float)(x))
#define RLX_AGENT __ATOMIC_RELAXED, __HIP_MEMORY_SCOPE_AGENT
#define RLX_WG __ATOMIC_RELAXED, __HIP_MEMORY_SCOPE_WORKGROUP

constexpr int DM = 512, DFF = 1792;
constexpr unsigned WS_FLAGS = 0;
constexpr unsigned WS_FLAG0 = 4096, FLAG_STRIDE = 128;
constexpr unsigned WS_G = 106496;
constexpr unsigned WS_KV = 131072;
constexpr unsigned WS_H = WS_KV + 32u * 8u * 16384u;
constexpr unsigned WS_O = WS_H + 64u * 112u * 1024u;
constexpr unsigned WS_W = WS_O + 64u * 32u * 1024u;
constexpr unsigned WS_END = WS_W + 16u * 144u * 1024u;
constexpr unsigned WS_DIAG = WS_END;
constexpr int XB = 32768, BUFB = XB + 6144 + 8192;
constexpr int L_V = 65536;
constexpr int L_GATE = 3 * BUFB;
constexpr int L_RT = L_GATE + 4096;
constexpr int L_MISC = 163840 - 2048;
constexpr int L_DUMMY = L_RT + 10240;
constexpr int L_B1 = L_MISC + 272, L_B2 = L_MISC + 512 + 272, L_SYNC = L_MISC + 1024 + 272;
static_assert(L_DUMMY + 1024 <= L_MISC && L_V + 3 * 8192 <= 2 * BUFB && 3 * 8192 <= BUFB, "lds");
enum { SY_READY = 0  , SY_KV = 8, SY_DONE = 9, SY_F = 10, SY_PASS = 11, SY_RED = 12, SY_TRIG = 13, SY_FSEEN = 14, SY_AW = 15, SY_FA = 16, SY_PROG = 24  , SY_OL = 32  , SY_N = 40 };
constexpr unsigned SENT = 0xFFFFFFFFu;
constexpr unsigned SPIN_CAP = 1u << 18;

#define MFMA(a, b, c) __builtin_amdgcn_mfma_f32_32x32x16_bf16((a), (b), (c), 0, 0, 0)
#define MFMA16(a, b, c) __builtin_amdgcn_mfma_f32_16x16x32_bf16((a), (b), (c), 0, 0, 0)
#define SBAR() __builtin_amdgcn_sched_barrier(0)
#define CFENCE() asm volatile("" ::: "memory")
#define PIN(x) asm volatile("" : "+v"(x))

#define F2_PRIO 0
#define DSPLIT 4
#define ROTX (2 * ((hd + t) & 3))
#define EARLYW 4
#define F2_PACE 0
#define FDEFER 1
#define OSC 0
#define RTC 7
#define LEAP 1
#define NP1 22
#define TRIGJ 7
#define STAMP(i) do {} while (0)
#define STAMP_FLUSH() do {} while (0)

DI unsigned pack2(float lo, float hi) { f32x2 v = {lo, hi}; return __builtin_bit_cast(unsigned, __builtin_convertvector(v, bf16x2)); }
DI u32x4 pack8(const f32x16& a, int o, float s) {
    u32x4 r; r[0] = pack2(a[o] * s, a[o + 1] * s); r[1] = pack2(a[o + 2] * s, a[o + 3] * s); r[2] = pack2(a[o + 4] * s, a[o + 5] * s); r[3] = pack2(a[o + 6] * s, a[o + 7] * s); return r; }
DI u32x4 pack8(const f32x16& a, int o) {
    u32x4 r; r[0] = pack2(a[o], a[o + 1]); r[1] = pack2(a[o + 2], a[o + 3]); r[2] = pack2(a[o + 4], a[o + 5]); r[3] = pack2(a[o + 6], a[o + 7]); return r; }
DI float bfround(float v) { return (float)(__bf16)v; }
DI float bf2f(unsigned short u) { return U2F((unsigned)u << 16); }
DI bf16x8 ldsr(const LAS char* p) { return *(const LAS bf16x8*)p; }
DI bf16x8 ldtr(const LAS char* p0, const LAS char* p1) {
    s16x4 a = __builtin_amdgcn_ds_read_tr16_b64_v4i16((LAS s16x4*)p0), b = __builtin_amdgcn_ds_read_tr16_b64_v4i16((LAS s16x4*)p1);
    return __builtin_bit_cast(bf16x8, __builtin_shufflevector(a, b, 0, 1, 2, 3, 4, 5, 6, 7)); }
DI void glds16(unsigned voff, const void* sbase, unsigned lds) {
    unsigned keep; asm volatile("s_mov_b32 %0, m0\n\ts_mov_b32 m0, %3\n\ts_nop 0\n\tglobal_load_lds_dwordx4 %1, %2\n\ts_mov_b32 m0, %0" : "=&s"(keep) : "v"(voff), "s"(sbase), "s"(lds) : "memory"); }
DI void glds4(unsigned voff, const void* sbase, unsigned lds) {
    unsigned keep; asm volatile("s_mov_b32 %0, m0\n\ts_mov_b32 m0, %3\n\ts_nop 0\n\tglobal_load_lds_dword %1, %2\n\ts_mov_b32 m0, %0" : "=&s"(keep) : "v"(voff), "s"(sbase), "s"(lds) : "memory"); }
DI u32x4 ld1(__amdgpu_buffer_rsrc_t rs, unsigned voff, unsigned soff) { return __builtin_amdgcn_raw_buffer_load_b128(rs, voff, soff, 16); }
DI void st1(u32x4 v, __amdgpu_buffer_rsrc_t rs, unsigned voff, unsigned soff) {
    __builtin_amdgcn_raw_buffer_store_b128(v, rs, voff, soff, 16); asm volatile("s_nop 1" :: "v"(v) : "memory"); }
DI u32x4 unmark(u32x4 v) {
#pragma unroll
    for (int i = 0; i < 4; ++i) v[i] = v[i] == SENT ? 0x7FFF7FFFu : v[i];
    return v; }
template <int N> DI void fetch_marked(u32x4 (&d)[N], __amdgpu_buffer_rsrc_t rs, unsigned voff, unsigned soff, unsigned s1, unsigned s2) {
    { const unsigned l = voff >> 4, po = (l & 1) * s1 + ((l & (N - 1)) >> 1) * s2 + 1020;
#pragma nounroll
      for (unsigned s = 0; s < SPIN_CAP; ++s) { if (__builtin_amdgcn_ballot_w64(__builtin_amdgcn_raw_buffer_load_b32(rs, po, soff, 16) == SENT) == 0) break; __builtin_amdgcn_s_sleep(1); CFENCE(); } }
#pragma nounroll
    for (unsigned s = 0; s < SPIN_CAP; ++s) {
#pragma unroll
        for (int i = 0; i < N; ++i) d[i] = ld1(rs, voff, soff + (i & 1) * s1 + (i >> 1) * s2);
        unsigned mx = 0;
#pragma unroll
        for (int i = 0; i < N; ++i) mx = __builtin_elementwise_max(__builtin_elementwise_max(mx, __builtin_elementwise_max(d[i][0], d[i][1])), __builtin_elementwise_max(d[i][2], d[i][3]));
        if (__builtin_amdgcn_ballot_w64(mx == SENT) == 0) break;
        __builtin_amdgcn_s_sleep(2); CFENCE();
    } }
DI void drain() { asm volatile("s_waitcnt vmcnt(0)" ::: "memory"); }
DI unsigned lds_arrive(LAS unsigned* w, int lane) {
    unsigned old = 0; if (lane == 0) old = __hip_atomic_fetch_add(w, 1u, RLX_WG); return __builtin_amdgcn_readfirstlane(old); }
template <int SL = 1> DI void lds_wait_ge(LAS unsigned* w, unsigned want) {
    unsigned tv, ts, n;
    asm volatile("s_mov_b32 %2, 0x400000\n"
                 "1:\n\tds_read_b32 %0, %3\n\ts_waitcnt lgkmcnt(0)\n\tv_readfirstlane_b32 %1, %0\n\ts_nop 1\n\ts_cmp_ge_u32 %1, %4\n\ts_cbranch_scc1 2f\n\t"
                 "s_sleep %5\n\ts_sub_u32 %2, %2, 1\n\ts_cmp_lg_u32 %2, 0\n\ts_cbranch_scc1 1b\n"
                 "2:" : "=&v"(tv), "=&s"(ts), "=&s"(n) : "v"((unsigned)(uintptr_t)w), "s"(want), "i"(SL) : "memory", "scc"); }
template <int SL = 1> DI void poll_tags(const char* ws, unsigned flag_off, bool active, u64 tag) {
#pragma nounroll
    for (unsigned s = 0; s < SPIN_CAP; ++s) {
        u64 v = tag; if (active) v = __hip_atomic_load((GAS u64*)(ws + flag_off), RLX_AGENT);
        if (__builtin_amdgcn_ballot_w64(v != tag) == 0) break; __builtin_amdgcn_s_sleep(SL); }
    __builtin_amdgcn_fence(__ATOMIC_ACQUIRE, "wavefront"); CFENCE(); }
DI unsigned flag_at(int kind, int hd, int t) { return WS_FLAG0 + (unsigned)(kind * 256 + hd * 8 + t) * FLAG_STRIDE; }
DI int g_at(int i) { return L_MISC + (i >> 6) * 512 + 16 + (i & 63) * 4; }
DI u32x4 join16(const f32x4& a, const f32x4& b, float sa, float sb) {
    auto r0 = __builtin_amdgcn_permlane16_swap(pack2(a[0] * sa, a[1] * sa), pack2(b[0] * sb, b[1] * sb), false, false);
    auto r1 = __builtin_amdgcn_permlane16_swap(pack2(a[2] * sa, a[3] * sa), pack2(b[2] * sb, b[3] * sb), false, false);
    return (u32x4){r0[0], r1[0], r0[1], r1[1]}; }
DI f32x16 exp16(f32x16 s) {
#pragma unroll
    for (int i = 0; i < 16; ++i) s[i] = __builtin_amdgcn_exp2f(s[i]);
    return s; }
DI f32x16 pv(bf16x8 v0, bf16x8 v1, const f32x16& p, f32x16 o, int idx) {
    const f32x8 lo = __builtin_shufflevector(p, p, 0, 1, 2, 3, 4, 5, 6, 7), hi = __builtin_shufflevector(p, p, 8, 9, 10, 11, 12, 13, 14, 15);
    o = __builtin_amdgcn_smfmac_f32_32x32x32_bf16(v0, __builtin_bit_cast(bf16x16, lo), o, idx, 0, 0);
    o = __builtin_amdgcn_smfmac_f32_32x32x32_bf16(v1, __builtin_bit_cast(bf16x16, hi), o, idx, 0, 0);
    return o; }
DI float rowmax16(const f32x16& s) {
    float m = __builtin_fmaxf(__builtin_fmaxf(s[0], s[1]), s[2]);
#pragma unroll
    for (int i = 3; i < 15; i += 2) m = __builtin_fmaxf(__builtin_fmaxf(m, s[i]), s[i + 1]);
    return __builtin_fmaxf(m, s[15]); }
DI float xhalf_max(float m) { auto r = __builtin_amdgcn_permlane32_swap(F2U(m), F2U(m), false, false); return __builtin_fmaxf(U2F(r[0]), U2F(r[1])); }

__global__ __launch_bounds__(768, 3) NOPK void gated_mix_fused(const unsigned short* __restrict__ x, const unsigned short* __restrict__ wqkv, const unsigned short* __restrict__ wo,
                                                         const unsigned short* __restrict__ w1, const unsigned short* __restrict__ b1, const unsigned short* __restrict__ w2,
                                                         const unsigned short* __restrict__ b2, const unsigned short* __restrict__ gate_w, unsigned short* __restrict__ out, char* ws) {
    __shared__ __attribute__((aligned(1024))) char smem[163840];
    LAS char* lds = (LAS char*)smem;
    const unsigned lds0 = (unsigned)(uintptr_t)lds;
    const int wid = __builtin_amdgcn_readfirstlane(__builtin_amdgcn_workitem_id_x() >> 6);
#define LANES() int lane = __builtin_amdgcn_mbcnt_hi(~0u, __builtin_amdgcn_mbcnt_lo(~0u, 0u)); PIN(lane); const int r32 = lane & 31, hh = lane >> 5, g4 = lane >> 4, r16 = lane & 15, q4 = r16 >> 2, p4 = lane & 3; (void)r32; (void)hh; (void)g4; (void)q4; (void)p4
    const int bx = __builtin_amdgcn_workgroup_id_x(), hd = 8 * (bx & 3) + ((bx >> 3) & 7), t = 4 * ((bx >> 2) & 1) + (bx >> 6);
    const __amdgpu_buffer_rsrc_t rs = __builtin_amdgcn_make_buffer_rsrc(ws, 0, WS_END, 0x00020000);
    LAS unsigned* sy = (LAS unsigned*)(lds + L_SYNC);
    asm volatile("" :: "s"(x), "s"(wqkv), "s"(wo), "s"(w1), "s"(b1), "s"(w2), "s"(b2), "s"(gate_w), "s"(out), "s"(ws));
    u64 tag; u32x2 ep;
#define EPOCH_LOAD() asm volatile("global_load_dwordx2 %0, %1, %2" : "=v"(ep) : "v"(0), "s"(ws) : "memory")
#define EPOCH_TAKE() do { PIN(ep); tag = (((u64)(unsigned)__builtin_amdgcn_readfirstlane(ep[1]) << 32) | (unsigned)__builtin_amdgcn_readfirstlane(ep[0])) + 1ull; } while (0)
    const int cp = hd & 7, rq = hd >> 3;
    const bool hasF = cp != 7; const int fb = hd - rq;
    const int rot = ROTX;
    STAMP(0);
#define CS(c) (((c) + rot) & 7)
#define BUFOF(c) ((((c) + 1) % 3) * BUFB)
#define CHUNK_SYNC(c, n) do { if ((c) < 7) asm volatile("s_waitcnt vmcnt(" #n ") lgkmcnt(0)\n\ts_barrier" ::: "memory"); else asm volatile("s_waitcnt vmcnt(0) lgkmcnt(0)\n\ts_barrier" ::: "memory"); } while (0)

    if (wid < 8) {
        f32x4 aq[2] = {}, ak[2] = {}, av[2] = {};
        { LANES(); const int xsw = g4 ^ (r16 >> 1);
          const int c0 = (lane & 7) ^ (lane >> 4), rho = lane >> 1, r8 = lane >> 3;
          const unsigned dvo0 = r8 * 1024 + c0 * 16, dvo1 = r8 * 1024 + (c0 ^ 4) * 16;
          const unsigned dvq = (8 * ((rho >> 2) & 3) + 4 * (rho >> 4) + (rho & 3)) * 3072 + (lane & 1) * 16;
          const unsigned dvf = (8 * (r8 >> 2) + (r8 & 3)) * 3584 + (((lane & 7) ^ (r8 & 6)) * 16);
          const char* xbase = (const char*)x + (size_t)(256 * t + 32 * wid) * 1024;
          const bool hq = wid < 6, hf = hasF;
          const char* qbase = (const char*)wqkv + hd * 32 + (hq ? (wid >> 1) * 1024 + (wid & 1) * 32 * 3072 : 0);
          const char* fbase = (const char*)w1 + (hf ? fb * 128 : 0) + (32 * (wid >> 2) + 16 * (wid & 1) + 4 * ((wid >> 1) & 1)) * 3584;
          const unsigned vq_ = hq ? dvq : 0u, vf_ = hf ? dvf : 0u;
#define DMA_X(c) do { _Pragma("unroll") for (int i = 0; i < 4; ++i) glds16((i & 1) ? dvo1 : dvo0, xbase + CS(c) * 128 + i * 8192, lds0 + BUFOF(c) + wid * 4096 + i * 1024); } while (0)
#define DMA_ALL(c) do { glds16(vq_, qbase + CS(c) * 64 * 3072, hq ? lds0 + BUFOF(c) + XB + wid * 1024 : lds0 + L_DUMMY); \
              glds16(vf_, fbase + CS(c) * 64 * 3584, hf ? lds0 + BUFOF(c) + XB + 6144 + wid * 1024 : lds0 + L_DUMMY); DMA_X(c); } while (0)
          DMA_ALL(0);
          EPOCH_LOAD();
          if (wid == 0) {
              if (lane < SY_N) sy[lane] = 0;
              if (lane < 8) { const unsigned one = lane < 4 ? 0x3F803F80u : 0u; *(LAS u32x4*)(lds + L_MISC + (lane & 3) * 512 + (lane >> 2) * 496) = (u32x4){one, one, one, one}; }
          }
          __builtin_amdgcn_s_sleep(4);
          DMA_ALL(1);
          STAMP(20);
          const int xo = (32 * wid + r16) * 128, wo_ = XB + (4 * g4 + q4) * 32 + p4 * 8;
          bf16x8 xa[2], xb[2], wq[2], wk[2], wv[2];
#define RX(d, c, ks, rt) d[ks] = ldsr(lds + BUFOF(c) + xo + (rt) * 2048 + ((xsw ^ (4 * (ks))) * 16))
#define RW(d, c, ks, im) d[ks] = ldtr(lds + BUFOF(c) + wo_ + (im) * 2048 + (ks) * 1024, lds + BUFOF(c) + wo_ + (im) * 2048 + (ks) * 1024 + 512)
#define M2(A, W, ks) do { A[0] = MFMA16(W[ks], xa[ks], A[0]); A[1] = MFMA16(W[ks], xb[ks], A[1]); PIN(A[0]); PIN(A[1]); SBAR(); } while (0)
#define MV(ks) do { av[0] = MFMA16(xa[ks], wv[ks], av[0]); av[1] = MFMA16(xb[ks], wv[ks], av[1]); PIN(av[0]); PIN(av[1]); SBAR(); } while (0)
#pragma unroll
          for (int c = 0; c < 8; ++c) {
              CHUNK_SYNC(c, 6);
              STAMP(12 + c);
              if (c == 7) { EPOCH_TAKE(); __builtin_amdgcn_s_setprio(3); }
              if (c + 2 < 8 && wid >= DSPLIT) DMA_ALL(c + 2);
              if (c > 0) M2(aq, wq, 1);
              RX(xa, c, 0, 0); RX(xb, c, 0, 1); RW(wq, c, 0, 0); SBAR();
              if (c > 0) M2(ak, wk, 1);
              RW(wk, c, 0, 1); RW(wv, c, 0, 2); SBAR();
              if (c > 0) MV(1);
              RX(xa, c, 1, 0); RX(xb, c, 1, 1); RW(wq, c, 1, 0); SBAR();
              M2(aq, wq, 0);
              RW(wk, c, 1, 1); RW(wv, c, 1, 2); SBAR();
              M2(ak, wk, 0);
              MV(0);
              if (c + 2 < 8 && wid < DSPLIT) DMA_ALL(c + 2);
          }
          M2(aq, wq, 1); M2(ak, wk, 1); MV(1);
#undef RX
#undef RW
#undef M2
#undef MV
#undef DMA_ALL
#undef DMA_X
        }
        STAMP(1);
        LANES();
        constexpr float QS = 0.25f * 1.4426950408889634f;
        const bf16x8 qf = __builtin_bit_cast(bf16x8, join16(aq[0], aq[1], QS, QS));
        const unsigned kvg = WS_KV + (unsigned)(hd * 8 + t) * 16384u, og = WS_O + (unsigned)((8 * t + wid) * 32 + hd) * 1024u;
        { const u32x4 kk = join16(ak[0], ak[1], 1.f, 1.f), vv = join16(av[0], av[1], 1.f, 1.f);
          const int ok = wid * 1024 + lane * 16, ov = wid * 1024 + (g4 & 1) * 512 + hh * 256 + r16 * 16;
          *(LAS u32x4*)(lds + ok) = kk; *(LAS u32x4*)(lds + L_V + ov) = vv;
          asm volatile("s_waitcnt lgkmcnt(0)" ::: "memory");
          if (lane == 0) __hip_atomic_fetch_add(sy + SY_AW, 1u, RLX_WG);
          st1(kk, rs, ok, kvg); st1(vv, rs, ov + 8192, kvg); }
        lds_wait_ge<1>(sy + SY_AW, 8);
        STAMP(2);
        drain();
        if (lds_arrive(sy + SY_KV, lane) == 7 && lane == 0) __hip_atomic_store((GAS u64*)(ws + flag_at(0, hd, t)), tag, RLX_AGENT);
        const unsigned rkv = WS_KV + (unsigned)(hd * 8 + ((t + wid) & 7)) * 16384u;
#define FETCH(NB) do { poll_tags(ws, flag_at(0, hd, (t + wid) & 7), lane == 0, tag); \
            _Pragma("unroll") for (int b_ = 0; b_ < 16 / (NB); ++b_) { u32x4 stg[NB]; \
                _Pragma("unroll") for (int i = 0; i < (NB); ++i) stg[i] = ld1(rs, lane * 16 + (b_ * (NB) + i) * 1024, rkv); \
                if (b_ == 0 && wid > 2) lds_wait_ge(sy + SY_FA, 4);              \
                _Pragma("unroll") for (int i = 0; i < (NB); ++i) *(LAS u32x4*)(lds + ((b_ * (NB) + i) >> 3) * L_V + wid * 8192 + ((b_ * (NB) + i) & 7) * 1024 + lane * 16) = stg[i]; } \
            asm volatile("s_waitcnt lgkmcnt(0)" ::: "memory"); \
            if (lane == 0) __hip_atomic_store(sy + SY_READY + wid, 1u, RLX_WG); } while (0)
        if (wid > 0 && wid < EARLYW) FETCH(16);
        const int idx = 0xDDDD;
        const LAS char* kp = lds + lane * 16;
        const bool vl = r32 < 16;
        const LAS char* vp = lds + (vl ? L_V + 512 + hh * 256 + r32 * 16 : (r32 & 11) == 0 ? L_MISC : L_MISC + 496);
        const int vstep = vl ? 2048 : 0;
        f32x16 o = (f32x16){}, negm, sA, sB = (f32x16){};
        bf16x8 kf = ldsr(kp), v0 = ldsr(vl ? vp - 512 : vp), v1 = (bf16x8){};
        { const f32x16 s0 = MFMA(kf, qf, (f32x16){}); const float m0 = -xhalf_max(rowmax16(s0));
#pragma unroll
          for (int i = 0; i < 16; ++i) negm[i] = m0; }
        sA = MFMA(kf, qf, negm);
        kf = ldsr(kp + 1024);
        STAMP(10);
        __builtin_amdgcn_s_setprio(0);
#define EX(S, a, b) do { _Pragma("unroll") for (int i_ = (a); i_ < (b); ++i_) S[i_] = __builtin_amdgcn_exp2f(S[i_]); PIN(S); SBAR(); } while (0)
#define LO8(S) __builtin_bit_cast(bf16x16, __builtin_shufflevector(S, S, 0, 1, 2, 3, 4, 5, 6, 7))
#define HI8(S) __builtin_bit_cast(bf16x16, __builtin_shufflevector(S, S, 8, 9, 10, 11, 12, 13, 14, 15))
#define SM(a, b) do { o = __builtin_amdgcn_smfmac_f32_32x32x32_bf16(a, b, o, idx, 0, 0); PIN(o); SBAR(); } while (0)
#define TILE(CUR, OTH, KO, VO) do { SBAR(); EX(CUR, 0, 2); SM(v1, HI8(OTH)); v1 = ldsr(vp + (VO)); SBAR(); EX(CUR, 2, 6); \
            OTH = MFMA(kf, qf, negm); PIN(OTH); SBAR(); kf = ldsr(kp + (KO)); SBAR(); EX(CUR, 6, 10); SM(v0, LO8(CUR)); v0 = ldsr(vp + (VO) + 512); SBAR(); EX(CUR, 10, 16); } while (0)
#define LEAPFROG(pos) do { if (lane == 0) __hip_atomic_store(sy + SY_PROG + wid, (unsigned)(pos), RLX_WG); \
            if ((unsigned)__builtin_amdgcn_readfirstlane(__hip_atomic_load(sy + SY_PROG + (wid ^ 4), RLX_WG)) >= (unsigned)(pos)) __builtin_amdgcn_s_setprio(1); else __builtin_amdgcn_s_setprio(0); } while (0)
#define PAIR(KO) do { TILE(sA, sB, (KO) + 2048, 0); TILE(sB, sA, (KO) + 3072, 1024); vp += vstep; } while (0)
        PAIR(0); PAIR(2048);
        PAIR(4096);
        lds_wait_ge(sy + SY_READY + 1, 1);
        PAIR(6144);
        if (wid >= EARLYW) { __builtin_amdgcn_s_setprio(3); FETCH(8); }
#undef FETCH
        STAMP(3);
        for (int j = 1; j < 8; ++j) {
            kp += 8192;
            if (j > 1 && j < 6) STAMP(4 + j);
            LEAPFROG(2 * j);
            PAIR(0); PAIR(2048);
            if (LEAP == 2) LEAPFROG(2 * j + 1);
            PAIR(4096);
            if (j < 7) lds_wait_ge(sy + SY_READY + j + 1, 1);
            PAIR(6144);
            if (j == 4 && lane == 0) __hip_atomic_fetch_add(sy + SY_PASS, 1u, RLX_WG);
            if (j == TRIGJ && lane == 0) __hip_atomic_fetch_add(sy + SY_TRIG, 1u, RLX_WG);
        }
        SM(v1, HI8(sB));
#undef PAIR
#undef LEAPFROG
#undef TILE
#undef SM
#undef EX
        asm volatile("s_waitcnt lgkmcnt(0)" ::: "memory");
        if (lane == 0) __hip_atomic_fetch_add(sy + SY_DONE, 1u, RLX_WG);
        STAMP(4);
        { float chk = o[8] * 0.f;
#pragma unroll
          for (int i = 0; i < 8; ++i) chk = __builtin_fmaf(o[i], 0.f, chk);
          if (__builtin_expect(0 != __builtin_amdgcn_ballot_w64(!(chk == 0.f) || o[8] > 1.8e19f), 0)) {
              float m = -__builtin_inff();
#pragma nounroll
              for (int tl = 0; tl < 64; ++tl) { const bf16x8 kg = __builtin_bit_cast(bf16x8, ld1(rs, lane * 16, WS_KV + (unsigned)(hd * 8 + (tl >> 3)) * 16384u + (tl & 7) * 1024));
                  m = __builtin_fmaxf(m, rowmax16(MFMA(kg, qf, (f32x16){}))); }
              m = -xhalf_max(m);
#pragma unroll
              for (int i = 0; i < 16; ++i) negm[i] = m;
              o = (f32x16){};
              const u32x4 ones = {0x3F803F80u, 0x3F803F80u, 0x3F803F80u, 0x3F803F80u};
#pragma nounroll
              for (int tl = 0; tl < 64; ++tl) { const unsigned so = WS_KV + (unsigned)(hd * 8 + (tl >> 3)) * 16384u + (tl & 7) * 1024;
                  const bf16x8 kg = __builtin_bit_cast(bf16x8, ld1(rs, lane * 16, so));
                  const u32x4 a0 = ld1(rs, 8192 + hh * 256 + r16 * 16, so), a1 = ld1(rs, 8192 + 512 + hh * 256 + r16 * 16, so);
                  o = pv(__builtin_bit_cast(bf16x8, vl ? a0 : ones), __builtin_bit_cast(bf16x8, vl ? a1 : ones), exp16(MFMA(kg, qf, negm)), o, idx); }
          } }
        lds_wait_ge(sy + SY_FSEEN, 4);
        { u32x4 ov = pack8(o, 0, *(const LAS float*)(lds + g_at(32 * wid + r32)) * __builtin_amdgcn_rcpf(o[8]));
          st1(unmark(ov), rs, lane * 16, og); }
        STAMP(5);
        { u32x4 of[8];
          fetch_marked(of, rs, lane * 16, WS_O + (unsigned)((8 * t + 2 * rq) * 32 + 4 * wid) * 1024u, 32768, 1024);
          STAMP(11);
          lds_wait_ge(sy + SY_DONE, 8);
#pragma unroll
          for (int i = 0; i < 8; ++i) *(LAS u32x4*)(lds + (wid >> 2) * L_V + 32768 + (wid & 3) * 8192 + i * 1024 + lane * 16) = of[i];
          asm volatile("s_waitcnt lgkmcnt(0)" ::: "memory");
          if (lane == 0) __hip_atomic_store(sy + SY_OL + wid, 1u, RLX_WG); }
        STAMP(21); STAMP_FLUSH();
    } else {
        const int f = wid - 8, i32 = rq + 4 * t;
        { LANES(); const int xsw = g4 ^ (r16 >> 1);
          EPOCH_LOAD();
          if (f == 1 && lane < 32) { if (hasF) glds4(lane * 4, (const char*)b1 + fb * 128, lds0 + L_B1); glds4(lane * 4, (const char*)b2 + cp * 128, lds0 + L_B2); }
          if (hd == 31) { glds16(lane * 16, gate_w, lds0 + L_GATE + f * 1024); asm volatile("s_waitcnt vmcnt(0)" ::: "memory"); }
          const int xo = (64 * f + r16) * 128, fo = XB + 6144 + (4 * g4 + q4) * 128 + p4 * 8, fsw = (2 * g4 + (q4 >> 1)) & 3;
          bf16x8 xf[2][4], wf[2][4];
#define RX(c, ks, rt) xf[ks][rt] = ldsr(lds + BUFOF(c) + xo + (rt) * 2048 + ((xsw ^ (4 * (ks))) * 16))
#define RW(c, ks, cb) wf[ks][cb] = ldtr(lds + BUFOF(c) + fo + (ks) * 4096 + (((cb) ^ fsw) * 32), lds + BUFOF(c) + fo + (ks) * 4096 + 2048 + (((cb) ^ fsw) * 32))
#define M2(ks, cb, r0) do { af[cb][r0] = MFMA16(wf[ks][cb], xf[ks][r0], af[cb][r0]); af[cb][r0 + 1] = MFMA16(wf[ks][cb], xf[ks][r0 + 1], af[cb][r0 + 1]); PIN(af[cb][r0]); PIN(af[cb][r0 + 1]); SBAR(); } while (0)
#define F_SYNC(c) do { if ((c) != RTC) asm volatile("s_waitcnt lgkmcnt(0)\n\ts_barrier" ::: "memory"); else asm volatile("s_waitcnt vmcnt(0) lgkmcnt(0)\n\ts_barrier" ::: "memory"); \
              STAMP(12 + (c)); \
              if ((c) == RTC) { EPOCH_TAKE(); LANES();     \
                  const int u = i32 + 32 * f; const char* src = u < 112 ? (const char*)w2 + (size_t)u * 16384 : (const char*)wo + (size_t)(u - 112) * 16384; \
                  const unsigned vo = (lane >> 3) * 1024 + cp * 128 + (lane & 7) * 16; \
                  glds16(vo, src, lds0 + L_RT + f * 2048); glds16(vo, src + 8192, lds0 + L_RT + f * 2048 + 1024); \
                  if (f == 0 && i32 < 16) { const char* s2 = (const char*)wo + (size_t)(i32 + 16) * 16384; glds16(vo, s2, lds0 + L_RT + 8192); glds16(vo, s2 + 8192, lds0 + L_RT + 8192 + 1024); } } } while (0)
#define F_LEAVE() STAMP(1); LANES(); \
              asm volatile("s_waitcnt lgkmcnt(0)" ::: "memory"); \
              if (lane == 0) __hip_atomic_fetch_add(sy + SY_FA, 1u, RLX_WG); \
              __builtin_amdgcn_s_setprio(2);
#define F_RETILE() do { \
              _Pragma("unroll") for (int i = 0; i < 2; ++i)     \
                  st1((u32x4){SENT, SENT, SENT, SENT}, rs, lane * 16, WS_O + (unsigned)((8 * t + 2 * f + i) * 32 + hd) * 1024u); \
              for (int n = f; n < ((f == 0 && i32 < 16) ? 5 : 4); n += 4) {        \
                  const int u = i32 + 32 * n, r0 = (u < 112 ? 8 : 4) * hh + q4, r1 = u < 112 ? 512 : 1024; \
                  _Pragma("unroll") for (int ct = 0; ct < 2; ++ct) { const LAS char* s = lds + L_RT + n * 2048 + r0 * 128 + (g4 & 1) * 32 + p4 * 8 + ct * 64; \
                      st1(__builtin_bit_cast(u32x4, ldtr(s, s + r1)), rs, lane * 16, WS_W + (unsigned)((2 * cp + ct) * 144 + u) * 1024u); } } } while (0)
          if (hasF) {
              f32x4 af[4][4] = {};
#pragma unroll
              for (int c = 0; c < 8; ++c) {
                  F_SYNC(c);
                  if (FDEFER && c == 7) lds_wait_ge<2>(sy + SY_AW, 8);
                  if (c > 0) M2(1, 0, 0);
                  RX(c, 0, 0); SBAR();
                  if (c > 0) M2(1, 0, 2);
                  RW(c, 0, 0); SBAR();
                  if (c > 0) M2(1, 1, 0);
                  RX(c, 0, 1); SBAR();
                  if (c > 0) M2(1, 1, 2);
                  RX(c, 0, 2); SBAR();
                  if (c > 0) M2(1, 2, 0);
                  RX(c, 0, 3); SBAR();
                  if (c > 0) M2(1, 2, 2);
                  RW(c, 0, 1); SBAR();
                  if (c > 0) M2(1, 3, 0);
                  RW(c, 0, 2); SBAR();
                  if (c > 0) M2(1, 3, 2);
                  RW(c, 0, 3); SBAR();
                  M2(0, 0, 0); RX(c, 1, 0); RW(c, 1, 0); SBAR();
                  M2(0, 0, 2); RX(c, 1, 1); RX(c, 1, 2); SBAR();
                  M2(0, 1, 0); RX(c, 1, 3); RW(c, 1, 1); SBAR();
                  M2(0, 1, 2); RW(c, 1, 2); RW(c, 1, 3); SBAR();
                  M2(0, 2, 0); M2(0, 2, 2); M2(0, 3, 0); M2(0, 3, 2);
              }
              M2(1, 0, 0); M2(1, 0, 2); M2(1, 1, 0); M2(1, 1, 2); M2(1, 2, 0); M2(1, 2, 2); M2(1, 3, 0); M2(1, 3, 2);
              { F_LEAVE();
#pragma unroll
                for (int cb = 0; cb < 4; ++cb) {
                    if (cb == 3) { asm volatile("s_waitcnt vmcnt(6)" ::: "memory"); F_RETILE(); }
                    const u32x2 w = *(const LAS u32x2*)(lds + L_B1 + cb * 32 + g4 * 8);
                    const float bb[4] = {U2F(w[0] << 16), U2F(w[0] & 0xffff0000u), U2F(w[1] << 16), U2F(w[1] & 0xffff0000u)};
#pragma unroll
                    for (int rp = 0; rp < 2; ++rp) {
                        float v[8], e[8];
#pragma unroll
                        for (int i = 0; i < 8; ++i) { v[i] = af[cb][2 * rp + (i >> 2)][i & 3] + bb[i & 3];
                            e[i] = v[i] * __builtin_fmaf(v[i] * v[i], -2.f * 0.7978845608028654f * 1.4426950408889634f * 0.044715f, -2.f * 0.7978845608028654f * 1.4426950408889634f); }
                        SBAR();
#pragma unroll
                        for (int i = 0; i < 8; ++i) e[i] = __builtin_amdgcn_exp2f(e[i]);
                        SBAR();
#pragma unroll
                        for (int i = 0; i < 8; ++i) e[i] = 1.f + e[i];
                        SBAR();
#pragma unroll
                        for (int i = 0; i < 8; ++i) e[i] = __builtin_amdgcn_rcpf(e[i]);
                        SBAR();
                        st1(join16((f32x4){v[0] * e[0], v[1] * e[1], v[2] * e[2], v[3] * e[3]}, (f32x4){v[4] * e[4], v[5] * e[5], v[6] * e[6], v[7] * e[7]}, 1.f, 1.f), rs, lane * 16,
                            WS_H + (unsigned)((8 * t + 2 * f + rp) * 112 + 4 * fb + cb) * 1024u);
                    }
                } }
          } else {
              f32x4 az[4] = {};
#pragma unroll
              for (int c = 0; c < 8; ++c) {
                  F_SYNC(c);
                  if (hd == 31) {
#pragma unroll
                      for (int ks = 0; ks < 2; ++ks) { const bf16x8 gf = ldsr(lds + L_GATE + f * 1024 + CS(c) * 128 + ks * 64 + g4 * 16);
#pragma unroll
                          for (int rt = 0; rt < 4; ++rt) { RX(c, ks, rt); az[rt] = MFMA16(gf, xf[ks][rt], az[rt]); } }
                  }
              }
              { F_LEAVE(); drain(); F_RETILE();
                if (hd == 31) {
                    const float z = bfround(g4 == 0 ? az[0][0] : g4 == 1 ? az[1][0] : g4 == 2 ? az[2][0] : az[3][0]);
                    __hip_atomic_store((GAS float*)(ws + WS_G + (256 * t + 64 * f + lane) * 4), bfround(__builtin_amdgcn_rcpf(1.f + __builtin_amdgcn_exp2f(-1.4426950408889634f * z))), RLX_AGENT);
                } }
          }
#undef F_SYNC
#undef F_LEAVE
#undef F_RETILE
#undef RX
#undef RW
#undef M2
        }
        LANES();
        const int freg = (f >> 1) * L_V + (f & 1) * 16384;
        drain();
        if (lds_arrive(sy + SY_F, lane) == 3 && lane == 0) __hip_atomic_store((GAS u64*)(ws + flag_at(1, hd, t)), tag, RLX_AGENT);
        STAMP(3);
        f32x16 acc[4] = {};
        poll_tags<4>(ws, hh ? flag_at(1, cp + 8 * (r32 & 3), r32 >> 2) : flag_at(1, r32, t), true, tag);
        *(LAS float*)(lds + g_at(64 * f + lane)) = __hip_atomic_load((GAS float*)(ws + WS_G + (256 * t + 64 * f + lane) * 4), RLX_AGENT);
        asm volatile("s_waitcnt lgkmcnt(0)" ::: "memory");
        if (lane == 0) __hip_atomic_fetch_add(sy + SY_FSEEN, 1u, RLX_WG);
        __builtin_amdgcn_s_setprio(F2_PRIO);
        STAMP(4);
        const unsigned hb = WS_H + (unsigned)((8 * t + 2 * rq) * 112 + 28 * f) * 1024u, wb = WS_W + (unsigned)(2 * cp * 144 + 28 * f) * 1024u;
        u32x4 fr[4][4];
#define LDK(slot, n) do { fr[slot][0] = ld1(rs, lane * 16, wb + (n) * 1024); fr[slot][1] = ld1(rs, lane * 16, wb + (144 + (n)) * 1024); \
                          fr[slot][2] = ld1(rs, lane * 16, hb + (n) * 1024); fr[slot][3] = ld1(rs, lane * 16, hb + (112 + (n)) * 1024); } while (0)
#define BC(v) __builtin_bit_cast(bf16x8, v)
#define MM4(w0, w1_, h0, h1) do { acc[0] = MFMA(w0, h0, acc[0]); acc[1] = MFMA(w0, h1, acc[1]); acc[2] = MFMA(w1_, h0, acc[2]); acc[3] = MFMA(w1_, h1, acc[3]); PIN(acc[0]); PIN(acc[1]); PIN(acc[2]); PIN(acc[3]); SBAR(); } while (0)
#define FFN2(n0, n1, pace) do { _Pragma("unroll") for (int n = (n0); n < (n1); ++n) { MM4(BC(fr[n & 3][0]), BC(fr[n & 3][1]), BC(fr[n & 3][2]), BC(fr[n & 3][3])); if (n + 4 < (n1)) LDK(n & 3, n + 4); if (pace) __builtin_amdgcn_s_sleep(pace); SBAR(); } } while (0)
#pragma unroll
        for (int n = 0; n < 4; ++n) LDK(n, n);
        FFN2(0, NP1, F2_PACE);
        STAMP(5);
        { u32x4 tmp[16];
#pragma unroll
          for (int i = 0; i < 16; ++i) tmp[i] = ld1(rs, lane * 16, WS_W + (unsigned)((2 * cp + (i & 1)) * 144 + 112 + 8 * f + (i >> 1)) * 1024u);
          __builtin_amdgcn_s_setprio(2);
          lds_wait_ge<16>(sy + SY_PASS, 8);
#pragma unroll
          for (int i = 0; i < 16; ++i) *(LAS u32x4*)(lds + freg + i * 1024 + lane * 16) = tmp[i]; }
        STAMP(6);
        if (NP1 < 28) {
#pragma unroll
            for (int n = NP1; n < NP1 + 4; ++n) LDK(n & 3, n);
            lds_wait_ge<8>(sy + SY_TRIG, 8);
            __builtin_amdgcn_s_setprio(F2_PRIO);
            FFN2(NP1, 28, 0);
        }
#undef LDK
#undef FFN2
        lds_wait_ge(sy + SY_FSEEN, 4);
        { const float og0 = bfround(1.f - *(const LAS float*)(lds + g_at(64 * rq + r32))), og1 = bfround(1.f - *(const LAS float*)(lds + g_at(64 * rq + 32 + r32)));
#pragma unroll
          for (int ct = 0; ct < 2; ++ct)
#pragma unroll
              for (int g = 0; g < 4; ++g) { u32x2 w = *(const LAS u32x2*)(lds + L_B2 + ct * 64 + g * 16 + hh * 8); if (f != 0) w = (u32x2){0u, 0u};
                  const float bb[4] = {U2F(w[0] << 16), U2F(w[0] & 0xffff0000u), U2F(w[1] << 16), U2F(w[1] & 0xffff0000u)};
#pragma unroll
                  for (int i = 0; i < 4; ++i) { acc[2 * ct][4 * g + i] = (acc[2 * ct][4 * g + i] + bb[i]) * og0; acc[2 * ct + 1][4 * g + i] = (acc[2 * ct + 1][4 * g + i] + bb[i]) * og1; } } }
        STAMP(7);
        __builtin_amdgcn_s_setprio(3);
#pragma unroll
        for (int n = 0; n < 8; ++n) { if ((n & 3) == 0) lds_wait_ge<1>(sy + SY_OL + 2 * f + (n >> 2), 1);
            if (n == 4) STAMP(10);
            const LAS char* p = lds + freg + n * 2048 + lane * 16;
            const bf16x8 w0 = ldsr(p), w1_ = ldsr(p + 1024), h0 = ldsr(p + 32768), h1 = ldsr(p + 32768 + 1024); MM4(w0, w1_, h0, h1); }
#undef MM4
#undef BC
        STAMP(8);
        f32x16 own = f == 0 ? acc[0] : f == 1 ? acc[1] : f == 2 ? acc[2] : acc[3];
#pragma unroll
        for (int qd = 0; qd < 4; ++qd) if (qd != f) {
#pragma unroll
            for (int g = 0; g < 4; ++g) *(LAS f32x4*)(lds + freg + qd * 4096 + g * 1024 + lane * 16) = (f32x4){acc[qd][4 * g], acc[qd][4 * g + 1], acc[qd][4 * g + 2], acc[qd][4 * g + 3]}; }
        asm volatile("s_waitcnt lgkmcnt(0)" ::: "memory");
        if (lane == 0) __hip_atomic_fetch_add(sy + SY_RED, 1u, RLX_WG);
        lds_wait_ge<0>(sy + SY_RED, 4);
        unsigned pk[8];
#pragma unroll
        for (int g = 0; g < 4; ++g) { f32x4 s = {own[4 * g], own[4 * g + 1], own[4 * g + 2], own[4 * g + 3]};
#pragma unroll
            for (int k = 1; k < 4; ++k) { const int w = (f + k) & 3; s += *(const LAS f32x4*)(lds + (w >> 1) * L_V + (w & 1) * 16384 + f * 4096 + g * 1024 + lane * 16); }
            pk[2 * g] = pack2(s[0], s[1]); pk[2 * g + 1] = pack2(s[2], s[3]); }
        unsigned short* orow = out + (size_t)(256 * t + 64 * rq + 32 * (f & 1) + r32) * DM + 64 * cp + 32 * (f >> 1) + 8 * hh;
#pragma unroll
        for (int k = 0; k < 2; ++k) {
            auto a = __builtin_amdgcn_permlane32_swap(pk[4 * k], pk[4 * k + 2], false, false), b = __builtin_amdgcn_permlane32_swap(pk[4 * k + 1], pk[4 * k + 3], false, false);
            const u32x4 v_ = {a[0], b[0], a[1], b[1]};
            if (OSC) asm volatile("global_store_dwordx4 %0, %1, off sc0 sc1\n\ts_nop 1" :: "v"(orow + 16 * k), "v"(v_) : "memory"); else *(u32x4*)(orow + 16 * k) = v_;
        }
        if (bx == 0 && f == 0 && lane == 0) __hip_atomic_store((GAS u64*)(ws + WS_FLAGS), tag, RLX_AGENT);
        STAMP(9); STAMP_FLUSH();
    }
}

extern "C" void kernel_launch(void* const* d_in, const int* in_sizes, int n_in, void* d_out, int out_size, void* d_ws, size_t ws_size, hipStream_t stream) {
    (void)in_sizes; (void)n_in; (void)out_size; (void)ws_size;
    hipLaunchKernelGGL(gated_mix_fused, dim3(256), dim3(768), 0, stream, (const unsigned short*)d_in[0], (const unsigned short*)d_in[1], (const unsigned short*)d_in[2],
                       (const unsigned short*)d_in[4], (const unsigned short*)d_in[5], (const unsigned short*)d_in[6], (const unsigned short*)d_in[7], (const unsigned short*)d_in[8],
                       (unsigned short*)d_out, (char*)d_ws);
}
```

```cpp
#include <hip/hip_runtime.h>
#include <hip/hip_bf16.h>
#include <stdint.h>

typedef __attribute__((ext_vector_type(8))) __bf16 bf16x8;
typedef __attribute__((ext_vector_type(16))) __bf16 bf16x16;
typedef __attribute__((ext_vector_type(2))) __bf16 bf16x2;
typedef __attribute__((ext_vector_type(4))) short s16x4;
typedef __attribute__((ext_vector_type(16))) float f32x16;
typedef __attribute__((ext_vector_type(8))) float f32x8;
typedef __attribute__((ext_vector_type(4))) float f32x4;
typedef __attribute__((ext_vector_type(2))) float f32x2;
typedef __attribute__((ext_vector_type(4))) unsigned u32x4;
typedef __attribute__((ext_vector_type(2))) unsigned u32x2;
typedef unsigned long long u64;
#define LAS __attribute__((address_space(3)))
#define GAS __attribute__((address_space(1)))
#if defined(__HIP_DEVICE_COMPILE__) && !defined(ALLOW_PK)
#define NOPK __attribute__((target("no-packed-fp32-ops")))
#else
#define NOPK
#endif
#define DI __device__ __forceinline__ NOPK
#define U2F(x) __builtin_bit_cast(float, (unsigned)(x))
#define F2U(x) __builtin_bit_cast(unsigned, (float)(x))
#define RLX_AGENT __ATOMIC_RELAXED, __HIP_MEMORY_SCOPE_AGENT
#define RLX_WG __ATOMIC_RELAXED, __HIP_MEMORY_SCOPE_WORKGROUP

constexpr int DM = 512, DFF = 1792;
constexpr unsigned WS_FLAGS = 0;
constexpr unsigned WS_FLAG0 = 4096, FLAG_STRIDE = 128;
constexpr unsigned WS_G = 106496;
constexpr unsigned WS_KV = 131072;
constexpr unsigned WS_H = WS_KV + 32u * 8u * 16384u;
constexpr unsigned WS_O = WS_H + 64u * 112u * 1024u;
constexpr unsigned WS_W = WS_O + 64u * 32u * 1024u;
constexpr unsigned WS_END = WS_W + 16u * 144u * 1024u;
constexpr unsigned WS_DIAG = WS_END;
constexpr int XB = 32768, BUFB = XB + 6144 + 8192;
constexpr int L_V = 65536;
constexpr int L_GATE = 3 * BUFB;
constexpr int L_RT = L_GATE + 4096;
constexpr int L_MISC = 163840 - 2048;
constexpr int L_DUMMY = L_RT + 10240;
constexpr int L_B1 = L_MISC + 272, L_B2 = L_MISC + 512 + 272, L_SYNC = L_MISC + 1024 + 272;
static_assert(L_DUMMY + 1024 <= L_MISC && L_V + 3 * 8192 <= 2 * BUFB && 3 * 8192 <= BUFB, "lds");
enum { SY_READY = 0  , SY_KV = 8, SY_DONE = 9, SY_F = 10, SY_PASS = 11, SY_RED = 12, SY_TRIG = 13, SY_FSEEN = 14, SY_AW = 15, SY_FA = 16, SY_PROG = 24  , SY_OL = 32  , SY_N = 40 };
constexpr unsigned SENT = 0xFFFFFFFFu;
constexpr unsigned SPIN_CAP = 1u << 18;

#define MFMA(a, b, c) __builtin_amdgcn_mfma_f32_32x32x16_bf16((a), (b), (c), 0, 0, 0)
#define MFMA16(a, b, c) __builtin_amdgcn_mfma_f32_16x16x32_bf16((a), (b), (c), 0, 0, 0)
#define SBAR() __builtin_amdgcn_sched_barrier(0)
#define CFENCE() asm volatile("" ::: "memory")
#define PIN(x) asm volatile("" : "+v"(x))

#define F2_PRIO 0
#define DSPLIT 4
#define ROTX (2 * ((hd + t) & 3))
#define EARLYW 4
#define F2_PACE 0
#define FDEFER 1
#define OSC 0
#define RTC 7
#define HLATE 0
#define SLP1 12
#define LATE_OK 2
#define XORD 1
#define LATEPOS 0
#define YP 3
#define LEAP 0
#define NP1 22
#define TRIGJ 7
#define STAMP(i) do {} while (0)
#define STAMP_FLUSH() do {} while (0)

DI unsigned pack2(float lo, float hi) { f32x2 v = {lo, hi}; return __builtin_bit_cast(unsigned, __builtin_convertvector(v, bf16x2)); }
DI u32x4 pack8(const f32x16& a, int o, float s) {
    u32x4 r; r[0] = pack2(a[o] * s, a[o + 1] * s); r[1] = pack2(a[o + 2] * s, a[o + 3] * s); r[2] = pack2(a[o + 4] * s, a[o + 5] * s); r[3] = pack2(a[o + 6] * s, a[o + 7] * s); return r; }
DI u32x4 pack8(const f32x16& a, int o) {
    u32x4 r; r[0] = pack2(a[o], a[o + 1]); r[1] = pack2(a[o + 2], a[o + 3]); r[2] = pack2(a[o + 4], a[o + 5]); r[3] = pack2(a[o + 6], a[o + 7]); return r; }
DI float bfround(float v) { return (float)(__bf16)v; }
DI float bf2f(unsigned short u) { return U2F((unsigned)u << 16); }
DI bf16x8 ldsr(const LAS char* p) { return *(const LAS bf16x8*)p; }
DI bf16x8 ldtr(const LAS char* p0, const LAS char* p1) {
    s16x4 a = __builtin_amdgcn_ds_read_tr16_b64_v4i16((LAS s16x4*)p0), b = __builtin_amdgcn_ds_read_tr16_b64_v4i16((LAS s16x4*)p1);
    return __builtin_bit_cast(bf16x8, __builtin_shufflevector(a, b, 0, 1, 2, 3, 4, 5, 6, 7)); }
DI void glds16(unsigned voff, const void* sbase, unsigned lds) {
    unsigned keep; asm volatile("s_mov_b32 %0, m0\n\ts_mov_b32 m0, %3\n\ts_nop 0\n\tglobal_load_lds_dwordx4 %1, %2\n\ts_mov_b32 m0, %0" : "=&s"(keep) : "v"(voff), "s"(sbase), "s"(lds) : "memory"); }
DI void glds4(unsigned voff, const void* sbase, unsigned lds) {
    unsigned keep; asm volatile("s_mov_b32 %0, m0\n\ts_mov_b32 m0, %3\n\ts_nop 0\n\tglobal_load_lds_dword %1, %2\n\ts_mov_b32 m0, %0" : "=&s"(keep) : "v"(voff), "s"(sbase), "s"(lds) : "memory"); }
DI u32x4 ld1(__amdgpu_buffer_rsrc_t rs, unsigned voff, unsigned soff) { return __builtin_amdgcn_raw_buffer_load_b128(rs, voff, soff, 16); }
DI void st1(u32x4 v, __amdgpu_buffer_rsrc_t rs, unsigned voff, unsigned soff) {
    __builtin_amdgcn_raw_buffer_store_b128(v, rs, voff, soff, 16); asm volatile("s_nop 1" :: "v"(v) : "memory"); }
DI u32x4 unmark(u32x4 v) {
#pragma unroll
    for (int i = 0; i < 4; ++i) v[i] = v[i] == SENT ? 0x7FFF7FFFu : v[i];
    return v; }
template <int N> DI void fetch_marked(u32x4 (&d)[N], __amdgpu_buffer_rsrc_t rs, unsigned voff, unsigned soff, unsigned s1, unsigned s2) {
    { const unsigned l = voff >> 4, po = (l & 1) * s1 + ((l & (N - 1)) >> 1) * s2 + 1020;
#pragma nounroll
      for (unsigned s = 0; s < SPIN_CAP; ++s) { if (__builtin_popcountll(__builtin_amdgcn_ballot_w64(__builtin_amdgcn_raw_buffer_load_b32(rs, po, soff, 16) == SENT)) <= LATE_OK * (64 / N)) break; __builtin_amdgcn_s_sleep(1); CFENCE(); } }
    unsigned miss = (1u << N) - 1;
#pragma nounroll
    for (unsigned s = 0; s < SPIN_CAP; ++s) {
#pragma unroll
        for (int i = 0; i < N; ++i) if ((miss >> i) & 1) d[i] = ld1(rs, voff, soff + (i & 1) * s1 + (i >> 1) * s2);
#pragma unroll
        for (int i = 0; i < N; ++i)
            if (__builtin_amdgcn_ballot_w64(__builtin_elementwise_max(__builtin_elementwise_max(d[i][0], d[i][1]), __builtin_elementwise_max(d[i][2], d[i][3])) == SENT) == 0) miss &= ~(1u << i);
        if (miss == 0) break;
        CFENCE();
    } }
DI void drain() { asm volatile("s_waitcnt vmcnt(0)" ::: "memory"); }
DI unsigned lds_arrive(LAS unsigned* w, int lane) {
    unsigned old = 0; if (lane == 0) old = __hip_atomic_fetch_add(w, 1u, RLX_WG); return __builtin_amdgcn_readfirstlane(old); }
template <int SL = 1> DI void lds_wait_ge(LAS unsigned* w, unsigned want) {
    unsigned tv, ts, n;
    asm volatile("s_mov_b32 %2, 0x400000\n"
                 "1:\n\tds_read_b32 %0, %3\n\ts_waitcnt lgkmcnt(0)\n\tv_readfirstlane_b32 %1, %0\n\ts_nop 1\n\ts_cmp_ge_u32 %1, %4\n\ts_cbranch_scc1 2f\n\t"
                 "s_sleep %5\n\ts_sub_u32 %2, %2, 1\n\ts_cmp_lg_u32 %2, 0\n\ts_cbranch_scc1 1b\n"
                 "2:" : "=&v"(tv), "=&s"(ts), "=&s"(n) : "v"((unsigned)(uintptr_t)w), "s"(want), "i"(SL) : "memory", "scc"); }
template <int SL = 1> DI void poll_tags(const char* ws, unsigned flag_off, bool active, u64 tag) {
#pragma nounroll
    for (unsigned s = 0; s < SPIN_CAP; ++s) {
        u64 v = tag; if (active) v = __hip_atomic_load((GAS u64*)(ws + flag_off), RLX_AGENT);
        if (__builtin_amdgcn_ballot_w64(v != tag) == 0) break; __builtin_amdgcn_s_sleep(SL); }
    __builtin_amdgcn_fence(__ATOMIC_ACQUIRE, "wavefront"); CFENCE(); }
DI unsigned flag_at(int kind, int hd, int t) { return WS_FLAG0 + (unsigned)(kind * 256 + hd * 8 + t) * FLAG_STRIDE; }
DI int g_at(int i) { return L_MISC + (i >> 6) * 512 + 16 + (i & 63) * 4; }
DI u32x4 join16(const f32x4& a, const f32x4& b, float sa, float sb) {
    auto r0 = __builtin_amdgcn_permlane16_swap(pack2(a[0] * sa, a[1] * sa), pack2(b[0] * sb, b[1] * sb), false, false);
    auto r1 = __builtin_amdgcn_permlane16_swap(pack2(a[2] * sa, a[3] * sa), pack2(b[2] * sb, b[3] * sb), false, false);
    return (u32x4){r0[0], r1[0], r0[1], r1[1]}; }
DI f32x16 exp16(f32x16 s) {
#pragma unroll
    for (int i = 0; i < 16; ++i) s[i] = __builtin_amdgcn_exp2f(s[i]);
    return s; }
DI f32x16 pv(bf16x8 v0, bf16x8 v1, const f32x16& p, f32x16 o, int idx) {
    const f32x8 lo = __builtin_shufflevector(p, p, 0, 1, 2, 3, 4, 5, 6, 7), hi = __builtin_shufflevector(p, p, 8, 9, 10, 11, 12, 13, 14, 15);
    o = __builtin_amdgcn_smfmac_f32_32x32x32_bf16(v0, __builtin_bit_cast(bf16x16, lo), o, idx, 0, 0);
    o = __builtin_amdgcn_smfmac_f32_32x32x32_bf16(v1, __builtin_bit_cast(bf16x16, hi), o, idx, 0, 0);
    return o; }
DI float rowmax16(const f32x16& s) {
    float m = __builtin_fmaxf(__builtin_fmaxf(s[0], s[1]), s[2]);
#pragma unroll
    for (int i = 3; i < 15; i += 2) m = __builtin_fmaxf(__builtin_fmaxf(m, s[i]), s[i + 1]);
    return __builtin_fmaxf(m, s[15]); }
DI float xhalf_max(float m) { auto r = __builtin_amdgcn_permlane32_swap(F2U(m), F2U(m), false, false); return __builtin_fmaxf(U2F(r[0]), U2F(r[1])); }

__global__ __launch_bounds__(768, 3) NOPK void gated_mix_fused(const unsigned short* __restrict__ x, const unsigned short* __restrict__ wqkv, const unsigned short* __restrict__ wo,
                                                         const unsigned short* __restrict__ w1, const unsigned short* __restrict__ b1, const unsigned short* __restrict__ w2,
                                                         const unsigned short* __restrict__ b2, const unsigned short* __restrict__ gate_w, unsigned short* __restrict__ out, char* ws) {
    __shared__ __attribute__((aligned(1024))) char smem[163840];
    LAS char* lds = (LAS char*)smem;
    const unsigned lds0 = (unsigned)(uintptr_t)lds;
    const int wid = __builtin_amdgcn_readfirstlane(__builtin_amdgcn_workitem_id_x() >> 6);
#define LANES() int lane = __builtin_amdgcn_mbcnt_hi(~0u, __builtin_amdgcn_mbcnt_lo(~0u, 0u)); PIN(lane); const int r32 = lane & 31, hh = lane >> 5, g4 = lane >> 4, r16 = lane & 15, q4 = r16 >> 2, p4 = lane & 3; (void)r32; (void)hh; (void)g4; (void)q4; (void)p4
    const int bx = __builtin_amdgcn_workgroup_id_x(), hd = 8 * (bx & 3) + ((bx >> 3) & 7), t = 4 * ((bx >> 2) & 1) + (bx >> 6);
    const __amdgpu_buffer_rsrc_t rs = __builtin_amdgcn_make_buffer_rsrc(ws, 0, WS_END, 0x00020000);
    LAS unsigned* sy = (LAS unsigned*)(lds + L_SYNC);
    asm volatile("" :: "s"(x), "s"(wqkv), "s"(wo), "s"(w1), "s"(b1), "s"(w2), "s"(b2), "s"(gate_w), "s"(out), "s"(ws));
    u64 tag; u32x2 ep;
#define EPOCH_LOAD() asm volatile("global_load_dwordx2 %0, %1, %2" : "=v"(ep) : "v"(0), "s"(ws) : "memory")
#define EPOCH_TAKE() do { PIN(ep); tag = (((u64)(unsigned)__builtin_amdgcn_readfirstlane(ep[1]) << 32) | (unsigned)__builtin_amdgcn_readfirstlane(ep[0])) + 1ull; } while (0)
    const int cp = hd & 7, rq = hd >> 3;
    const bool hasF = cp != 7; const int fb = hd - rq;
    const int rot = ROTX;
    STAMP(0);
#define CS(c) (((c) + rot) & 7)
#define BUFOF(c) ((((c) + 1) % 3) * BUFB)
#define CHUNK_SYNC(c, n) do { if ((c) < 7) asm volatile("s_waitcnt vmcnt(" #n ") lgkmcnt(0)\n\ts_barrier" ::: "memory"); else asm volatile("s_waitcnt vmcnt(0) lgkmcnt(0)\n\ts_barrier" ::: "memory"); } while (0)

    if (wid < 8) {
        f32x4 aq[2] = {}, ak[2] = {}, av[2] = {};
        { LANES(); const int xsw = g4 ^ (r16 >> 1);
          const int c0 = (lane & 7) ^ (lane >> 4), rho = lane >> 1, r8 = lane >> 3;
          const unsigned dvo0 = r8 * 1024 + c0 * 16, dvo1 = r8 * 1024 + (c0 ^ 4) * 16;
          const unsigned dvq = (8 * ((rho >> 2) & 3) + 4 * (rho >> 4) + (rho & 3)) * 3072 + (lane & 1) * 16;
          const unsigned dvf = (8 * (r8 >> 2) + (r8 & 3)) * 3584 + (((lane & 7) ^ (r8 & 6)) * 16);
          const char* xbase = (const char*)x + (size_t)(256 * t + 32 * wid) * 1024;
          const bool hq = wid < 6, hf = hasF;
          const char* qbase = (const char*)wqkv + hd * 32 + (hq ? (wid >> 1) * 1024 + (wid & 1) * 32 * 3072 : 0);
          const char* fbase = (const char*)w1 + (hf ? fb * 128 : 0) + (32 * (wid >> 2) + 16 * (wid & 1) + 4 * ((wid >> 1) & 1)) * 3584;
          const unsigned vq_ = hq ? dvq : 0u, vf_ = hf ? dvf : 0u;
#define DMA_X(c) do { _Pragma("unroll") for (int i = 0; i < 4; ++i) glds16((i & 1) ? dvo1 : dvo0, xbase + CS(c) * 128 + i * 8192, lds0 + BUFOF(c) + wid * 4096 + i * 1024); } while (0)
#define DMA_ALL(c) do { glds16(vq_, qbase + CS(c) * 64 * 3072, hq ? lds0 + BUFOF(c) + XB + wid * 1024 : lds0 + L_DUMMY); \
              glds16(vf_, fbase + CS(c) * 64 * 3584, hf ? lds0 + BUFOF(c) + XB + 6144 + wid * 1024 : lds0 + L_DUMMY); DMA_X(c); } while (0)
          DMA_ALL(0);
          EPOCH_LOAD();
          if (wid == 0) {
              if (lane < SY_N) sy[lane] = 0;
              if (lane < 8) { const unsigned one = lane < 4 ? 0x3F803F80u : 0u; *(LAS u32x4*)(lds + L_MISC + (lane & 3) * 512 + (lane >> 2) * 496) = (u32x4){one, one, one, one}; }
          }
          __builtin_amdgcn_s_sleep(SLP1);
          DMA_ALL(1);
          const int xo = (32 * wid + r16) * 128, wo_ = XB + (4 * g4 + q4) * 32 + p4 * 8;
          bf16x8 xa[2], xb[2], wq[2], wk[2], wv[2];
#define RX(d, c, ks, rt) d[ks] = ldsr(lds + BUFOF(c) + xo + (rt) * 2048 + ((xsw ^ (4 * (ks))) * 16))
#define RW(d, c, ks, im) d[ks] = ldtr(lds + BUFOF(c) + wo_ + (im) * 2048 + (ks) * 1024, lds + BUFOF(c) + wo_ + (im) * 2048 + (ks) * 1024 + 512)
#define M2(A, W, ks) do { A[0] = MFMA16(W[ks], xa[ks], A[0]); A[1] = MFMA16(W[ks], xb[ks], A[1]); PIN(A[0]); PIN(A[1]); SBAR(); } while (0)
#define MV(ks) do { av[0] = MFMA16(xa[ks], wv[ks], av[0]); av[1] = MFMA16(xb[ks], wv[ks], av[1]); PIN(av[0]); PIN(av[1]); SBAR(); } while (0)
#pragma unroll
          for (int c = 0; c < 8; ++c) {
              CHUNK_SYNC(c, 6);
              if (c == 0 || c == 1 || c == 7) STAMP(12 + c);
              if (c == 7) { EPOCH_TAKE(); __builtin_amdgcn_s_setprio(3); }
              if (c + 2 < 8 && wid >= DSPLIT) DMA_ALL(c + 2);
              if (c > 0) M2(aq, wq, 1);
              RX(xa, c, 0, 0); RX(xb, c, 0, 1); RW(wq, c, 0, 0); SBAR();
              if (c > 0) M2(ak, wk, 1);
              RW(wk, c, 0, 1); RW(wv, c, 0, 2); SBAR();
              if (c > 0) MV(1);
              RX(xa, c, 1, 0); RX(xb, c, 1, 1); RW(wq, c, 1, 0); SBAR();
              M2(aq, wq, 0);
              RW(wk, c, 1, 1); RW(wv, c, 1, 2); SBAR();
              M2(ak, wk, 0);
              MV(0);
              if (c + 2 < 8 && wid < DSPLIT) DMA_ALL(c + 2);
          }
          M2(aq, wq, 1); M2(ak, wk, 1); MV(1);
#undef RX
#undef RW
#undef M2
#undef MV
#undef DMA_ALL
#undef DMA_X
        }
        STAMP(1);
        LANES();
        constexpr float QS = 0.25f * 1.4426950408889634f;
        const bf16x8 qf = __builtin_bit_cast(bf16x8, join16(aq[0], aq[1], QS, QS));
        const unsigned kvg = WS_KV + (unsigned)(hd * 8 + t) * 16384u, og = WS_O + (unsigned)((8 * t + wid) * 32 + hd) * 1024u;
        { const u32x4 kk = join16(ak[0], ak[1], 1.f, 1.f), vv = join16(av[0], av[1], 1.f, 1.f);
          const int ok = wid * 1024 + lane * 16, ov = wid * 1024 + (g4 & 1) * 512 + hh * 256 + r16 * 16;
          *(LAS u32x4*)(lds + ok) = kk; *(LAS u32x4*)(lds + L_V + ov) = vv;
          asm volatile("s_waitcnt lgkmcnt(0)" ::: "memory");
          if (lane == 0) __hip_atomic_fetch_add(sy + SY_AW, 1u, RLX_WG);
          st1(kk, rs, ok, kvg); st1(vv, rs, ov + 8192, kvg); }
        lds_wait_ge<1>(sy + SY_AW, 8);
        STAMP(2);
        drain();
        STAMP(22);
        if (lds_arrive(sy + SY_KV, lane) == 7 && lane == 0) __hip_atomic_store((GAS u64*)(ws + flag_at(0, hd, t)), tag, RLX_AGENT);
        const int tc = XORD ? ((t ^ wid) & 4) | ((t + wid) & 3) : (t + wid) & 7;
        const unsigned rkv = WS_KV + (unsigned)(hd * 8 + tc) * 16384u;
#define FETCH(NB) do { poll_tags(ws, flag_at(0, hd, tc), lane == 0, tag); STAMP(23); \
            _Pragma("unroll") for (int b_ = 0; b_ < 16 / (NB); ++b_) { u32x4 stg[NB]; \
                _Pragma("unroll") for (int i = 0; i < (NB); ++i) stg[i] = ld1(rs, lane * 16 + (b_ * (NB) + i) * 1024, rkv); \
                if (b_ == 0 && wid > 2) lds_wait_ge(sy + SY_FA, 4);              \
                _Pragma("unroll") for (int i = 0; i < (NB); ++i) *(LAS u32x4*)(lds + ((b_ * (NB) + i) >> 3) * L_V + wid * 8192 + ((b_ * (NB) + i) & 7) * 1024 + lane * 16) = stg[i]; } \
            asm volatile("s_waitcnt lgkmcnt(0)" ::: "memory"); \
            if (lane == 0) __hip_atomic_store(sy + SY_READY + wid, 1u, RLX_WG); } while (0)
        if (wid > 0 && wid < EARLYW) FETCH(16);
        const int idx = 0xDDDD;
        const LAS char* kp = lds + lane * 16;
        const bool vl = r32 < 16;
        const LAS char* vp = lds + (vl ? L_V + 512 + hh * 256 + r32 * 16 : (r32 & 11) == 0 ? L_MISC : L_MISC + 496);
        const int vstep = vl ? 2048 : 0;
        f32x16 o = (f32x16){}, negm, sA, sB = (f32x16){};
        bf16x8 kf = ldsr(kp), v0 = ldsr(vl ? vp - 512 : vp), v1 = (bf16x8){};
        { const f32x16 s0 = MFMA(kf, qf, (f32x16){}); const float m0 = -xhalf_max(rowmax16(s0));
#pragma unroll
          for (int i = 0; i < 16; ++i) negm[i] = m0; }
        sA = MFMA(kf, qf, negm);
        kf = ldsr(kp + 1024);
        STAMP(10);
        __builtin_amdgcn_s_setprio(0);
#define EX(S, a, b) do { _Pragma("unroll") for (int i_ = (a); i_ < (b); ++i_) S[i_] = __builtin_amdgcn_exp2f(S[i_]); PIN(S); SBAR(); } while (0)
#define LO8(S) __builtin_bit_cast(bf16x16, __builtin_shufflevector(S, S, 0, 1, 2, 3, 4, 5, 6, 7))
#define HI8(S) __builtin_bit_cast(bf16x16, __builtin_shufflevector(S, S, 8, 9, 10, 11, 12, 13, 14, 15))
#define SM(a, b) do { o = __builtin_amdgcn_smfmac_f32_32x32x32_bf16(a, b, o, idx, 0, 0); PIN(o); SBAR(); } while (0)
#define TILE(CUR, OTH, KO, VO) do { SBAR(); EX(CUR, 0, 2); SM(v1, HI8(OTH)); v1 = ldsr(vp + (VO)); SBAR(); EX(CUR, 2, 6); \
            OTH = MFMA(kf, qf, negm); PIN(OTH); SBAR(); kf = ldsr(kp + (KO)); SBAR(); EX(CUR, 6, 10); SM(v0, LO8(CUR)); v0 = ldsr(vp + (VO) + 512); SBAR(); EX(CUR, 10, 16); } while (0)
#define LEAPFROG(pos) do { if (lane == 0) __hip_atomic_store(sy + SY_PROG + wid, (unsigned)(pos), RLX_WG); \
            if ((unsigned)__builtin_amdgcn_readfirstlane(__hip_atomic_load(sy + SY_PROG + (wid ^ 4), RLX_WG)) >= (unsigned)(pos)) __builtin_amdgcn_s_setprio(1); else __builtin_amdgcn_s_setprio(0); } while (0)
#define PAIR(KO) do { TILE(sA, sB, (KO) + 2048, 0); TILE(sB, sA, (KO) + 3072, 1024); vp += vstep; } while (0)
        PAIR(0);
        if (LATEPOS == 2 && wid >= EARLYW) { __builtin_amdgcn_s_setprio(3); FETCH(8); __builtin_amdgcn_s_setprio(0); }
        PAIR(2048);
        if (LATEPOS == 1 && wid >= EARLYW) { __builtin_amdgcn_s_setprio(3); FETCH(8); __builtin_amdgcn_s_setprio(0); }
        PAIR(4096);
        if (LATEPOS == 3 && wid >= EARLYW) { __builtin_amdgcn_s_setprio(3); FETCH(8); __builtin_amdgcn_s_setprio(0); }
        lds_wait_ge(sy + SY_READY + 1, 1);
        PAIR(6144);
        if (LATEPOS == 0 && wid >= EARLYW) { __builtin_amdgcn_s_setprio(3); FETCH(8); __builtin_amdgcn_s_setprio(YP); }
#undef FETCH
        STAMP(3);
        for (int j = 1; j < 8; ++j) {
            kp += 8192;
            if (LEAP) LEAPFROG(2 * j);
            PAIR(0); PAIR(2048);
            if (LEAP == 2) LEAPFROG(2 * j + 1);
            PAIR(4096);
            if (j < 7) lds_wait_ge(sy + SY_READY + j + 1, 1);
            PAIR(6144);
            if (j == 4 && lane == 0) __hip_atomic_fetch_add(sy + SY_PASS, 1u, RLX_WG);
            if (j == TRIGJ && lane == 0) __hip_atomic_fetch_add(sy + SY_TRIG, 1u, RLX_WG);
        }
        SM(v1, HI8(sB));
#undef PAIR
#undef LEAPFROG
#undef TILE
#undef SM
#undef EX
        asm volatile("s_waitcnt lgkmcnt(0)" ::: "memory");
        if (lane == 0) __hip_atomic_fetch_add(sy + SY_DONE, 1u, RLX_WG);
        STAMP(4);
        { float chk = o[8] * 0.f;
#pragma unroll
          for (int i = 0; i < 8; ++i) chk = __builtin_fmaf(o[i], 0.f, chk);
          if (__builtin_expect(0 != __builtin_amdgcn_ballot_w64(!(chk == 0.f) || o[8] > 1.8e19f), 0)) {
              float m = -__builtin_inff();
#pragma nounroll
              for (int tl = 0; tl < 64; ++tl) { const bf16x8 kg = __builtin_bit_cast(bf16x8, ld1(rs, lane * 16, WS_KV + (unsigned)(hd * 8 + (tl >> 3)) * 16384u + (tl & 7) * 1024));
                  m = __builtin_fmaxf(m, rowmax16(MFMA(kg, qf, (f32x16){}))); }
              m = -xhalf_max(m);
#pragma unroll
              for (int i = 0; i < 16; ++i) negm[i] = m;
              o = (f32x16){};
              const u32x4 ones = {0x3F803F80u, 0x3F803F80u, 0x3F803F80u, 0x3F803F80u};
#pragma nounroll
              for (int tl = 0; tl < 64; ++tl) { const unsigned so = WS_KV + (unsigned)(hd * 8 + (tl >> 3)) * 16384u + (tl & 7) * 1024;
                  const bf16x8 kg = __builtin_bit_cast(bf16x8, ld1(rs, lane * 16, so));
                  const u32x4 a0 = ld1(rs, 8192 + hh * 256 + r16 * 16, so), a1 = ld1(rs, 8192 + 512 + hh * 256 + r16 * 16, so);
                  o = pv(__builtin_bit_cast(bf16x8, vl ? a0 : ones), __builtin_bit_cast(bf16x8, vl ? a1 : ones), exp16(MFMA(kg, qf, negm)), o, idx); }
          } }
        lds_wait_ge(sy + SY_FSEEN, 4);
        { u32x4 ov = pack8(o, 0, *(const LAS float*)(lds + g_at(32 * wid + r32)) * __builtin_amdgcn_rcpf(o[8]));
          st1(unmark(ov), rs, lane * 16, og); }
        STAMP(5);
        { u32x4 of[8];
          fetch_marked(of, rs, lane * 16, WS_O + (unsigned)((8 * t + 2 * rq) * 32 + 4 * wid) * 1024u, 32768, 1024);
          STAMP(11);
          lds_wait_ge(sy + SY_DONE, 8);
#pragma unroll
          for (int i = 0; i < 8; ++i) *(LAS u32x4*)(lds + (wid >> 2) * L_V + 32768 + (wid & 3) * 8192 + i * 1024 + lane * 16) = of[i];
          asm volatile("s_waitcnt lgkmcnt(0)" ::: "memory");
          if (lane == 0) __hip_atomic_store(sy + SY_OL + wid, 1u, RLX_WG); }
        STAMP(21); STAMP_FLUSH();
    } else {
        const int f = wid - 8, i32 = rq + 4 * t;
        { LANES(); const int xsw = g4 ^ (r16 >> 1);
          EPOCH_LOAD();
          if (f == 1 && lane < 32) { if (hasF) glds4(lane * 4, (const char*)b1 + fb * 128, lds0 + L_B1); glds4(lane * 4, (const char*)b2 + cp * 128, lds0 + L_B2); }
          if (hd == 31) { glds16(lane * 16, gate_w, lds0 + L_GATE + f * 1024); asm volatile("s_waitcnt vmcnt(0)" ::: "memory"); }
          const int xo = (64 * f + r16) * 128, fo = XB + 6144 + (4 * g4 + q4) * 128 + p4 * 8, fsw = (2 * g4 + (q4 >> 1)) & 3;
          bf16x8 xf[2][4], wf[2][4];
#define RX(c, ks, rt) xf[ks][rt] = ldsr(lds + BUFOF(c) + xo + (rt) * 2048 + ((xsw ^ (4 * (ks))) * 16))
#define RW(c, ks, cb) wf[ks][cb] = ldtr(lds + BUFOF(c) + fo + (ks) * 4096 + (((cb) ^ fsw) * 32), lds + BUFOF(c) + fo + (ks) * 4096 + 2048 + (((cb) ^ fsw) * 32))
#define M2(ks, cb, r0) do { af[cb][r0] = MFMA16(wf[ks][cb], xf[ks][r0], af[cb][r0]); af[cb][r0 + 1] = MFMA16(wf[ks][cb], xf[ks][r0 + 1], af[cb][r0 + 1]); PIN(af[cb][r0]); PIN(af[cb][r0 + 1]); SBAR(); } while (0)
#define F_SYNC(c) do { if ((c) != RTC && (c) != 0) asm volatile("s_waitcnt lgkmcnt(0)\n\ts_barrier" ::: "memory"); else asm volatile("s_waitcnt vmcnt(0) lgkmcnt(0)\n\ts_barrier" ::: "memory"); \
              if ((c) == 0 || (c) == 7) STAMP(12 + (c)); \
              if ((c) == RTC) { EPOCH_TAKE(); LANES();     \
                  const int u = i32 + 32 * f; const char* src = u < 112 ? (const char*)w2 + (size_t)u * 16384 : (const char*)wo + (size_t)(u - 112) * 16384; \
                  const unsigned vo = (lane >> 3) * 1024 + cp * 128 + (lane & 7) * 16; \
                  glds16(vo, src, lds0 + L_RT + f * 2048); glds16(vo, src + 8192, lds0 + L_RT + f * 2048 + 1024); \
                  if (f == 0 && i32 < 16) { const char* s2 = (const char*)wo + (size_t)(i32 + 16) * 16384; glds16(vo, s2, lds0 + L_RT + 8192); glds16(vo, s2 + 8192, lds0 + L_RT + 8192 + 1024); } } } while (0)
#define F_LEAVE() STAMP(1); LANES(); \
              asm volatile("s_waitcnt lgkmcnt(0)" ::: "memory"); \
              if (lane == 0) __hip_atomic_fetch_add(sy + SY_FA, 1u, RLX_WG); \
              __builtin_amdgcn_s_setprio(2);
#define F_RETILE() do { \
              _Pragma("unroll") for (int i = 0; i < 2; ++i)     \
                  st1((u32x4){SENT, SENT, SENT, SENT}, rs, lane * 16, WS_O + (unsigned)((8 * t + 2 * f + i) * 32 + hd) * 1024u); \
              for (int n = f; n < ((f == 0 && i32 < 16) ? 5 : 4); n += 4) {        \
                  const int u = i32 + 32 * n, r0 = (u < 112 ? 8 : 4) * hh + q4, r1 = u < 112 ? 512 : 1024; \
                  _Pragma("unroll") for (int ct = 0; ct < 2; ++ct) { const LAS char* s = lds + L_RT + n * 2048 + r0 * 128 + (g4 & 1) * 32 + p4 * 8 + ct * 64; \
                      st1(__builtin_bit_cast(u32x4, ldtr(s, s + r1)), rs, lane * 16, WS_W + (unsigned)((2 * cp + ct) * 144 + u) * 1024u); } } } while (0)
          if (hasF) {
              f32x4 af[4][4];
#pragma unroll
              for (int c = 0; c < 8; ++c) {
                  F_SYNC(c);
                  if (c == 0) {
#pragma unroll
                      for (int cb = 0; cb < 4; ++cb) { const u32x2 w = *(const LAS u32x2*)(lds + L_B1 + cb * 32 + g4 * 8);
#pragma unroll
                          for (int rt = 0; rt < 4; ++rt) af[cb][rt] = (f32x4){U2F(w[0] << 16), U2F(w[0] & 0xffff0000u), U2F(w[1] << 16), U2F(w[1] & 0xffff0000u)}; }
                  }
                  if (FDEFER && c == 7) lds_wait_ge<2>(sy + SY_AW, 8);
                  if (c > 0) M2(1, 0, 0);
                  RX(c, 0, 0); SBAR();
                  if (c > 0) M2(1, 0, 2);
                  RW(c, 0, 0); SBAR();
                  if (c > 0) M2(1, 1, 0);
                  RX(c, 0, 1); SBAR();
                  if (c > 0) M2(1, 1, 2);
                  RX(c, 0, 2); SBAR();
                  if (c > 0) M2(1, 2, 0);
                  RX(c, 0, 3); SBAR();
                  if (c > 0) M2(1, 2, 2);
                  RW(c, 0, 1); SBAR();
                  if (c > 0) M2(1, 3, 0);
                  RW(c, 0, 2); SBAR();
                  if (c > 0) M2(1, 3, 2);
                  RW(c, 0, 3); SBAR();
                  M2(0, 0, 0); RX(c, 1, 0); RW(c, 1, 0); SBAR();
                  M2(0, 0, 2); RX(c, 1, 1); RX(c, 1, 2); SBAR();
                  M2(0, 1, 0); RX(c, 1, 3); RW(c, 1, 1); SBAR();
                  M2(0, 1, 2); RW(c, 1, 2); RW(c, 1, 3); SBAR();
                  M2(0, 2, 0); M2(0, 2, 2); M2(0, 3, 0); M2(0, 3, 2);
              }
              M2(1, 0, 0); M2(1, 0, 2); M2(1, 1, 0); M2(1, 1, 2); M2(1, 2, 0); M2(1, 2, 2); M2(1, 3, 0); M2(1, 3, 2);
              { F_LEAVE();
                u32x4 hp[4][2];
#pragma unroll
                for (int cb = 0; cb < 4; ++cb) {
#pragma unroll
                    for (int rp = 0; rp < 2; ++rp) {
                        float v[8], e[8];
#pragma unroll
                        for (int i = 0; i < 8; ++i) { v[i] = af[cb][2 * rp + (i >> 2)][i & 3];
                            e[i] = v[i] * __builtin_fmaf(v[i] * v[i], -2.f * 0.7978845608028654f * 1.4426950408889634f * 0.044715f, -2.f * 0.7978845608028654f * 1.4426950408889634f); }
                        SBAR();
#pragma unroll
                        for (int i = 0; i < 8; ++i) e[i] = __builtin_amdgcn_exp2f(e[i]);
                        SBAR();
#pragma unroll
                        for (int i = 0; i < 8; ++i) e[i] = 1.f + e[i];
                        SBAR();
#pragma unroll
                        for (int i = 0; i < 8; ++i) e[i] = __builtin_amdgcn_rcpf(e[i]);
                        SBAR();
                        hp[cb][rp] = join16((f32x4){v[0] * e[0], v[1] * e[1], v[2] * e[2], v[3] * e[3]}, (f32x4){v[4] * e[4], v[5] * e[5], v[6] * e[6], v[7] * e[7]}, 1.f, 1.f);
                        if (!HLATE) st1(hp[cb][rp], rs, lane * 16, WS_H + (unsigned)((8 * t + 2 * f + rp) * 112 + 4 * fb + cb) * 1024u);
                        PIN(hp[cb][rp]); SBAR();
                    }
                }
                STAMP(2);
                if (HLATE) { drain();
#pragma unroll
                    for (int i = 0; i < 8; ++i) st1(hp[i >> 1][i & 1], rs, lane * 16, WS_H + (unsigned)((8 * t + 2 * f + (i & 1)) * 112 + 4 * fb + (i >> 1)) * 1024u);
                } else asm volatile("s_waitcnt vmcnt(8)" ::: "memory");
                F_RETILE(); }
          } else {
              f32x4 az[4] = {};
#pragma unroll
              for (int c = 0; c < 8; ++c) {
                  F_SYNC(c);
                  if (hd == 31) {
#pragma unroll
                      for (int ks = 0; ks < 2; ++ks) { const bf16x8 gf = ldsr(lds + L_GATE + f * 1024 + CS(c) * 128 + ks * 64 + g4 * 16);
#pragma unroll
                          for (int rt = 0; rt < 4; ++rt) { RX(c, ks, rt); az[rt] = MFMA16(gf, xf[ks][rt], az[rt]); } }
                  }
              }
              { F_LEAVE(); drain(); F_RETILE();
                if (hd == 31) {
                    const float z = bfround(g4 == 0 ? az[0][0] : g4 == 1 ? az[1][0] : g4 == 2 ? az[2][0] : az[3][0]);
                    __hip_atomic_store((GAS float*)(ws + WS_G + (256 * t + 64 * f + lane) * 4), bfround(__builtin_amdgcn_rcpf(1.f + __builtin_amdgcn_exp2f(-1.4426950408889634f * z))), RLX_AGENT);
                } }
          }
#undef F_SYNC
#undef F_LEAVE
#undef F_RETILE
#undef RX
#undef RW
#undef M2
        }
        LANES();
        const int freg = (f >> 1) * L_V + (f & 1) * 16384;
        drain();
        if (lds_arrive(sy + SY_F, lane) == 3 && lane == 0) __hip_atomic_store((GAS u64*)(ws + flag_at(1, hd, t)), tag, RLX_AGENT);
        STAMP(3);
        f32x16 acc[4] = {};
        poll_tags<4>(ws, hh ? flag_at(1, cp + 8 * (r32 & 3), r32 >> 2) : flag_at(1, r32, t), true, tag);
        *(LAS float*)(lds + g_at(64 * f + lane)) = __hip_atomic_load((GAS float*)(ws + WS_G + (256 * t + 64 * f + lane) * 4), RLX_AGENT);
        asm volatile("s_waitcnt lgkmcnt(0)" ::: "memory");
        if (lane == 0) __hip_atomic_fetch_add(sy + SY_FSEEN, 1u, RLX_WG);
        __builtin_amdgcn_s_setprio(F2_PRIO);
        STAMP(4);
        const unsigned hb = WS_H + (unsigned)((8 * t + 2 * rq) * 112 + 28 * f) * 1024u, wb = WS_W + (unsigned)(2 * cp * 144 + 28 * f) * 1024u;
        u32x4 fr[4][4];
#define LDK(slot, n) do { fr[slot][0] = ld1(rs, lane * 16, wb + (n) * 1024); fr[slot][1] = ld1(rs, lane * 16, wb + (144 + (n)) * 1024); \
                          fr[slot][2] = ld1(rs, lane * 16, hb + (n) * 1024); fr[slot][3] = ld1(rs, lane * 16, hb + (112 + (n)) * 1024); } while (0)
#define BC(v) __builtin_bit_cast(bf16x8, v)
#define MM4(w0, w1_, h0, h1) do { acc[0] = MFMA(w0, h0, acc[0]); acc[1] = MFMA(w0, h1, acc[1]); acc[2] = MFMA(w1_, h0, acc[2]); acc[3] = MFMA(w1_, h1, acc[3]); PIN(acc[0]); PIN(acc[1]); PIN(acc[2]); PIN(acc[3]); SBAR(); } while (0)
#define FFN2(n0, n1, pace) do { _Pragma("unroll") for (int n = (n0); n < (n1); ++n) { MM4(BC(fr[n & 3][0]), BC(fr[n & 3][1]), BC(fr[n & 3][2]), BC(fr[n & 3][3])); if (n + 4 < (n1)) LDK(n & 3, n + 4); if (pace) __builtin_amdgcn_s_sleep(pace); SBAR(); } } while (0)
#pragma unroll
        for (int n = 0; n < 4; ++n) LDK(n, n);
        FFN2(0, NP1, F2_PACE);
        STAMP(5);
        { u32x4 tmp[16];
#pragma unroll
          for (int i = 0; i < 16; ++i) tmp[i] = ld1(rs, lane * 16, WS_W + (unsigned)((2 * cp + (i & 1)) * 144 + 112 + 8 * f + (i >> 1)) * 1024u);
          __builtin_amdgcn_s_setprio(2);
          lds_wait_ge<16>(sy + SY_PASS, 8);
#pragma unroll
          for (int i = 0; i < 16; ++i) *(LAS u32x4*)(lds + freg + i * 1024 + lane * 16) = tmp[i]; }
        STAMP(6);
        if (NP1 < 28) {
#pragma unroll
            for (int n = NP1; n < NP1 + 4; ++n) LDK(n & 3, n);
            lds_wait_ge<8>(sy + SY_TRIG, 8);
            __builtin_amdgcn_s_setprio(F2_PRIO);
            FFN2(NP1, 28, 0);
        }
#undef LDK
#undef FFN2
        lds_wait_ge(sy + SY_FSEEN, 4);
        { const float og0 = bfround(1.f - *(const LAS float*)(lds + g_at(64 * rq + r32))), og1 = bfround(1.f - *(const LAS float*)(lds + g_at(64 * rq + 32 + r32)));
#pragma unroll
          for (int ct = 0; ct < 2; ++ct)
#pragma unroll
              for (int g = 0; g < 4; ++g) { u32x2 w = *(const LAS u32x2*)(lds + L_B2 + ct * 64 + g * 16 + hh * 8); if (f != 0) w = (u32x2){0u, 0u};
                  const float bb[4] = {U2F(w[0] << 16), U2F(w[0] & 0xffff0000u), U2F(w[1] << 16), U2F(w[1] & 0xffff0000u)};
#pragma unroll
                  for (int i = 0; i < 4; ++i) { acc[2 * ct][4 * g + i] = (acc[2 * ct][4 * g + i] + bb[i]) * og0; acc[2 * ct + 1][4 * g + i] = (acc[2 * ct + 1][4 * g + i] + bb[i]) * og1; } } }
        STAMP(7);
        __builtin_amdgcn_s_setprio(3);
#pragma unroll
        for (int n = 0; n < 8; ++n) { if ((n & 3) == 0) lds_wait_ge<1>(sy + SY_OL + 2 * f + (n >> 2), 1);
            if (n == 4) STAMP(10);
            const LAS char* p = lds + freg + n * 2048 + lane * 16;
            const bf16x8 w0 = ldsr(p), w1_ = ldsr(p + 1024), h0 = ldsr(p + 32768), h1 = ldsr(p + 32768 + 1024); MM4(w0, w1_, h0, h1); }
#undef MM4
#undef BC
        STAMP(8);
        f32x16 own = f == 0 ? acc[0] : f == 1 ? acc[1] : f == 2 ? acc[2] : acc[3];
#pragma unroll
        for (int qd = 0; qd < 4; ++qd) if (qd != f) {
#pragma unroll
            for (int g = 0; g < 4; ++g) *(LAS f32x4*)(lds + freg + qd * 4096 + g * 1024 + lane * 16) = (f32x4){acc[qd][4 * g], acc[qd][4 * g + 1], acc[qd][4 * g + 2], acc[qd][4 * g + 3]}; }
        asm volatile("s_waitcnt lgkmcnt(0)" ::: "memory");
        if (lane == 0) __hip_atomic_fetch_add(sy + SY_RED, 1u, RLX_WG);
        lds_wait_ge<0>(sy + SY_RED, 4);
        unsigned pk[8];
#pragma unroll
        for (int g = 0; g < 4; ++g) { f32x4 s = {own[4 * g], own[4 * g + 1], own[4 * g + 2], own[4 * g + 3]};
#pragma unroll
            for (int k = 1; k < 4; ++k) { const int w = (f + k) & 3; s += *(const LAS f32x4*)(lds + (w >> 1) * L_V + (w & 1) * 16384 + f * 4096 + g * 1024 + lane * 16); }
            pk[2 * g] = pack2(s[0], s[1]); pk[2 * g + 1] = pack2(s[2], s[3]); }
        unsigned short* orow = out + (size_t)(256 * t + 64 * rq + 32 * (f & 1) + r32) * DM + 64 * cp + 32 * (f >> 1) + 8 * hh;
#pragma unroll
        for (int k = 0; k < 2; ++k) {
            auto a = __builtin_amdgcn_permlane32_swap(pk[4 * k], pk[4 * k + 2], false, false), b = __builtin_amdgcn_permlane32_swap(pk[4 * k + 1], pk[4 * k + 3], false, false);
            const u32x4 v_ = {a[0], b[0], a[1], b[1]};
            if (OSC) asm volatile("global_store_dwordx4 %0, %1, off sc0 sc1\n\ts_nop 1" :: "v"(orow + 16 * k), "v"(v_) : "memory"); else *(u32x4*)(orow + 16 * k) = v_;
        }
        if (bx == 0 && f == 0 && lane == 0) __hip_atomic_store((GAS u64*)(ws + WS_FLAGS), tag, RLX_AGENT);
        STAMP(9); STAMP_FLUSH();
    }
}

extern "C" void kernel_launch(void* const* d_in, const int* in_sizes, int n_in, void* d_out, int out_size, void* d_ws, size_t ws_size, hipStream_t stream) {
    (void)in_sizes; (void)n_in; (void)out_size; (void)ws_size;
    hipLaunchKernelGGL(gated_mix_fused, dim3(256), dim3(768), 0, stream, (const unsigned short*)d_in[0], (const unsigned short*)d_in[1], (const unsigned short*)d_in[2],
                       (const unsigned short*)d_in[4], (const unsigned short*)d_in[5], (const unsigned short*)d_in[6], (const unsigned short*)d_in[7], (const unsigned short*)d_in[8],
                       (unsigned short*)d_out, (char*)d_ws);
}
```

```cpp
#include <hip/hip_runtime.h>
#include <hip/hip_bf16.h>
#include <stdint.h>

typedef __attribute__((ext_vector_type(8))) __bf16 bf16x8;
typedef __attribute__((ext_vector_type(16))) __bf16 bf16x16;
typedef __attribute__((ext_vector_type(2))) __bf16 bf16x2;
typedef __attribute__((ext_vector_type(4))) short s16x4;
typedef __attribute__((ext_vector_type(16))) float f32x16;
typedef __attribute__((ext_vector_type(8))) float f32x8;
typedef __attribute__((ext_vector_type(4))) float f32x4;
typedef __attribute__((ext_vector_type(2))) float f32x2;
typedef __attribute__((ext_vector_type(4))) unsigned u32x4;
typedef __attribute__((ext_vector_type(2))) unsigned u32x2;
typedef unsigned long long u64;
#define LAS __attribute__((address_space(3)))
#define GAS __attribute__((address_space(1)))
#if defined(__HIP_DEVICE_COMPILE__) && !defined(ALLOW_PK)
#define NOPK __attribute__((target("no-packed-fp32-ops")))
#else
#define NOPK
#endif
#define DI __device__ __forceinline__ NOPK
#define U2F(x) __builtin_bit_cast(float, (unsigned)(x))
#define F2U(x) __builtin_bit_cast(unsigned, (float)(x))
#define RLX_AGENT __ATOMIC_RELAXED, __HIP_MEMORY_SCOPE_AGENT
#define RLX_WG __ATOMIC_RELAXED, __HIP_MEMORY_SCOPE_WORKGROUP

constexpr int DM = 512, DFF = 1792;
constexpr unsigned WS_FLAGS = 0;
constexpr unsigned WS_FLAG0 = 4096, FLAG_STRIDE = 128;
constexpr unsigned WS_G = 106496;
constexpr unsigned WS_KV = 131072;
constexpr unsigned WS_H = WS_KV + 32u * 8u * 16384u;
constexpr unsigned WS_O = WS_H + 64u * 112u * 1024u;
constexpr unsigned WS_W = WS_O + 64u * 32u * 1024u;
constexpr unsigned WS_END = WS_W + 16u * 144u * 1024u;
constexpr unsigned WS_DIAG = WS_END;
constexpr int XB = 32768, BUFB = XB + 6144 + 8192;
constexpr int L_V = 65536;
constexpr int L_GATE = 3 * BUFB;
constexpr int L_RT = L_GATE + 4096;
constexpr int L_MISC = 163840 - 2048;
constexpr int L_DUMMY = L_RT + 10240;
constexpr int L_B1 = L_MISC + 272, L_B2 = L_MISC + 512 + 272, L_SYNC = L_MISC + 1024 + 272;
static_assert(L_DUMMY + 1024 <= L_MISC && L_V + 3 * 8192 <= 2 * BUFB && 3 * 8192 <= BUFB, "lds");
enum { SY_READY = 0  , SY_KV = 8, SY_DONE = 9, SY_F = 10, SY_PASS = 11, SY_TRIG = 13, SY_FSEEN = 14, SY_AW = 15, SY_FA = 16, SY_RED = 18  , SY_FP = 20  , SY_PROG = 24  , SY_N = 32 };
constexpr unsigned SENT = 0xFFFFFFFFu;
constexpr unsigned SPIN_CAP = 1u << 18;

#define MFMA(a, b, c) __builtin_amdgcn_mfma_f32_32x32x16_bf16((a), (b), (c), 0, 0, 0)
#define MFMA16(a, b, c) __builtin_amdgcn_mfma_f32_16x16x32_bf16((a), (b), (c), 0, 0, 0)
#define SBAR() __builtin_amdgcn_sched_barrier(0)
#define CFENCE() asm volatile("" ::: "memory")
#define PIN(x) asm volatile("" : "+v"(x))

#define F2_PRIO 0
#define DSPLIT 4
#define ROTX (2 * ((hd + t) & 3))
#define EARLYW 4
#define F2_PACE 0
#define FDEFER 1
#define RTC 7
#define HLATE 0
#define SLP1 12
#define LATE_OK 2
#define XORD 1
#define LATEPOS 0
#define YP 3
#define LEAP 0
#define NP1 24
#define TRIGJ 6
#define STAMP(i) do {} while (0)
#define STAMP_FLUSH() do {} while (0)

DI unsigned pack2(float lo, float hi) { f32x2 v = {lo, hi}; return __builtin_bit_cast(unsigned, __builtin_convertvector(v, bf16x2)); }
DI u32x4 pack8(const f32x16& a, int o, float s) {
    u32x4 r; r[0] = pack2(a[o] * s, a[o + 1] * s); r[1] = pack2(a[o + 2] * s, a[o + 3] * s); r[2] = pack2(a[o + 4] * s, a[o + 5] * s); r[3] = pack2(a[o + 6] * s, a[o + 7] * s); return r; }
DI u32x4 pack8(const f32x16& a, int o) {
    u32x4 r; r[0] = pack2(a[o], a[o + 1]); r[1] = pack2(a[o + 2], a[o + 3]); r[2] = pack2(a[o + 4], a[o + 5]); r[3] = pack2(a[o + 6], a[o + 7]); return r; }
DI float bfround(float v) { return (float)(__bf16)v; }
DI float bf2f(unsigned short u) { return U2F((unsigned)u << 16); }
DI bf16x8 ldsr(const LAS char* p) { return *(const LAS bf16x8*)p; }
DI bf16x8 ldtr(const LAS char* p0, const LAS char* p1) {
    s16x4 a = __builtin_amdgcn_ds_read_tr16_b64_v4i16((LAS s16x4*)p0), b = __builtin_amdgcn_ds_read_tr16_b64_v4i16((LAS s16x4*)p1);
    return __builtin_bit_cast(bf16x8, __builtin_shufflevector(a, b, 0, 1, 2, 3, 4, 5, 6, 7)); }
DI void glds16(unsigned voff, const void* sbase, unsigned lds) {
    unsigned keep; asm volatile("s_mov_b32 %0, m0\n\ts_mov_b32 m0, %3\n\ts_nop 0\n\tglobal_load_lds_dwordx4 %1, %2\n\ts_mov_b32 m0, %0" : "=&s"(keep) : "v"(voff), "s"(sbase), "s"(lds) : "memory"); }
DI void glds4(unsigned voff, const void* sbase, unsigned lds) {
    unsigned keep; asm volatile("s_mov_b32 %0, m0\n\ts_mov_b32 m0, %3\n\ts_nop 0\n\tglobal_load_lds_dword %1, %2\n\ts_mov_b32 m0, %0" : "=&s"(keep) : "v"(voff), "s"(sbase), "s"(lds) : "memory"); }
DI u32x4 ld1(__amdgpu_buffer_rsrc_t rs, unsigned voff, unsigned soff) { return __builtin_amdgcn_raw_buffer_load_b128(rs, voff, soff, 16); }
DI void st1(u32x4 v, __amdgpu_buffer_rsrc_t rs, unsigned voff, unsigned soff) {
    __builtin_amdgcn_raw_buffer_store_b128(v, rs, voff, soff, 16); asm volatile("s_nop 1" :: "v"(v) : "memory"); }
DI u32x4 unmark(u32x4 v) {
#pragma unroll
    for (int i = 0; i < 4; ++i) v[i] = v[i] == SENT ? 0x7FFF7FFFu : v[i];
    return v; }
DI bool marked(const u32x4& d) { return __builtin_amdgcn_ballot_w64(__builtin_elementwise_max(__builtin_elementwise_max(d[0], d[1]), __builtin_elementwise_max(d[2], d[3])) == SENT) != 0; }
DI void drain() { asm volatile("s_waitcnt vmcnt(0)" ::: "memory"); }
DI unsigned lds_arrive(LAS unsigned* w, int lane) {
    unsigned old = 0; if (lane == 0) old = __hip_atomic_fetch_add(w, 1u, RLX_WG); return __builtin_amdgcn_readfirstlane(old); }
template <int SL = 1> DI void lds_wait_ge(LAS unsigned* w, unsigned want) {
    unsigned tv, ts, n;
    asm volatile("s_mov_b32 %2, 0x400000\n"
                 "1:\n\tds_read_b32 %0, %3\n\ts_waitcnt lgkmcnt(0)\n\tv_readfirstlane_b32 %1, %0\n\ts_nop 1\n\ts_cmp_ge_u32 %1, %4\n\ts_cbranch_scc1 2f\n\t"
                 "s_sleep %5\n\ts_sub_u32 %2, %2, 1\n\ts_cmp_lg_u32 %2, 0\n\ts_cbranch_scc1 1b\n"
                 "2:" : "=&v"(tv), "=&s"(ts), "=&s"(n) : "v"((unsigned)(uintptr_t)w), "s"(want), "i"(SL) : "memory", "scc"); }
template <int SL = 1> DI void poll_tags(const char* ws, unsigned flag_off, bool active, u64 tag) {
#pragma nounroll
    for (unsigned s = 0; s < SPIN_CAP; ++s) {
        u64 v = tag; if (active) v = __hip_atomic_load((GAS u64*)(ws + flag_off), RLX_AGENT);
        if (__builtin_amdgcn_ballot_w64(v != tag) == 0) break; __builtin_amdgcn_s_sleep(SL); }
    __builtin_amdgcn_fence(__ATOMIC_ACQUIRE, "wavefront"); CFENCE(); }
DI unsigned flag_at(int kind, int hd, int t) { return WS_FLAG0 + (unsigned)(kind * 256 + hd * 8 + t) * FLAG_STRIDE; }
DI int g_at(int i) { return L_MISC + (i >> 6) * 512 + 16 + (i & 63) * 4; }
DI u32x4 join16(const f32x4& a, const f32x4& b, float sa, float sb) {
    auto r0 = __builtin_amdgcn_permlane16_swap(pack2(a[0] * sa, a[1] * sa), pack2(b[0] * sb, b[1] * sb), false, false);
    auto r1 = __builtin_amdgcn_permlane16_swap(pack2(a[2] * sa, a[3] * sa), pack2(b[2] * sb, b[3] * sb), false, false);
    return (u32x4){r0[0], r1[0], r0[1], r1[1]}; }
DI f32x16 exp16(f32x16 s) {
#pragma unroll
    for (int i = 0; i < 16; ++i) s[i] = __builtin_amdgcn_exp2f(s[i]);
    return s; }
DI f32x16 pv(bf16x8 v0, bf16x8 v1, const f32x16& p, f32x16 o, int idx) {
    const f32x8 lo = __builtin_shufflevector(p, p, 0, 1, 2, 3, 4, 5, 6, 7), hi = __builtin_shufflevector(p, p, 8, 9, 10, 11, 12, 13, 14, 15);
    o = __builtin_amdgcn_smfmac_f32_32x32x32_bf16(v0, __builtin_bit_cast(bf16x16, lo), o, idx, 0, 0);
    o = __builtin_amdgcn_smfmac_f32_32x32x32_bf16(v1, __builtin_bit_cast(bf16x16, hi), o, idx, 0, 0);
    return o; }
DI float rowmax16(const f32x16& s) {
    float m = __builtin_fmaxf(__builtin_fmaxf(s[0], s[1]), s[2]);
#pragma unroll
    for (int i = 3; i < 15; i += 2) m = __builtin_fmaxf(__builtin_fmaxf(m, s[i]), s[i + 1]);
    return __builtin_fmaxf(m, s[15]); }
DI float xhalf_max(float m) { auto r = __builtin_amdgcn_permlane32_swap(F2U(m), F2U(m), false, false); return __builtin_fmaxf(U2F(r[0]), U2F(r[1])); }

__global__ __launch_bounds__(768, 3) NOPK void gated_mix_fused(const unsigned short* __restrict__ x, const unsigned short* __restrict__ wqkv, const unsigned short* __restrict__ wo,
                                                         const unsigned short* __restrict__ w1, const unsigned short* __restrict__ b1, const unsigned short* __restrict__ w2,
                                                         const unsigned short* __restrict__ b2, const unsigned short* __restrict__ gate_w, unsigned short* __restrict__ out, char* ws) {
    __shared__ __attribute__((aligned(1024))) char smem[163840];
    LAS char* lds = (LAS char*)smem;
    const unsigned lds0 = (unsigned)(uintptr_t)lds;
    const int wid = __builtin_amdgcn_readfirstlane(__builtin_amdgcn_workitem_id_x() >> 6);
#define LANES() int lane = __builtin_amdgcn_mbcnt_hi(~0u, __builtin_amdgcn_mbcnt_lo(~0u, 0u)); PIN(lane); const int r32 = lane & 31, hh = lane >> 5, g4 = lane >> 4, r16 = lane & 15, q4 = r16 >> 2, p4 = lane & 3; (void)r32; (void)hh; (void)g4; (void)q4; (void)p4
    const int bx = __builtin_amdgcn_workgroup_id_x(), hd = 8 * (bx & 3) + ((bx >> 3) & 7), t = 4 * ((bx >> 2) & 1) + (bx >> 6);
    const __amdgpu_buffer_rsrc_t rs = __builtin_amdgcn_make_buffer_rsrc(ws, 0, WS_END, 0x00020000);
    LAS unsigned* sy = (LAS unsigned*)(lds + L_SYNC);
    asm volatile("" :: "s"(x), "s"(wqkv), "s"(wo), "s"(w1), "s"(b1), "s"(w2), "s"(b2), "s"(gate_w), "s"(out), "s"(ws));
    u64 tag; u32x2 ep;
#define EPOCH_LOAD() asm volatile("global_load_dwordx2 %0, %1, %2" : "=v"(ep) : "v"(0), "s"(ws) : "memory")
#define EPOCH_TAKE() do { PIN(ep); tag = (((u64)(unsigned)__builtin_amdgcn_readfirstlane(ep[1]) << 32) | (unsigned)__builtin_amdgcn_readfirstlane(ep[0])) + 1ull; } while (0)
    const int cp = hd & 7, rq = hd >> 3;
    const bool hasF = cp != 7; const int fb = hd - rq;
    const int rot = ROTX;
    STAMP(0);
#define CS(c) (((c) + rot) & 7)
#define BUFOF(c) ((((c) + 1) % 3) * BUFB)
#define CHUNK_SYNC(c, n) do { if ((c) < 7) asm volatile("s_waitcnt vmcnt(" #n ") lgkmcnt(0)\n\ts_barrier" ::: "memory"); else asm volatile("s_waitcnt vmcnt(0) lgkmcnt(0)\n\ts_barrier" ::: "memory"); } while (0)

    if (wid < 8) {
        f32x4 aq[2] = {}, ak[2] = {}, av[2] = {};
        { LANES(); const int xsw = g4 ^ (r16 >> 1);
          const int c0 = (lane & 7) ^ (lane >> 4), rho = lane >> 1, r8 = lane >> 3;
          const unsigned dvo0 = r8 * 1024 + c0 * 16, dvo1 = r8 * 1024 + (c0 ^ 4) * 16;
          const unsigned dvq = (8 * ((rho >> 2) & 3) + 4 * (rho >> 4) + (rho & 3)) * 3072 + (lane & 1) * 16;
          const unsigned dvf = (8 * (r8 >> 2) + (r8 & 3)) * 3584 + (((lane & 7) ^ (r8 & 6)) * 16);
          const char* xbase = (const char*)x + (size_t)(256 * t + 32 * wid) * 1024;
          const bool hq = wid < 6, hf = hasF;
          const char* qbase = (const char*)wqkv + hd * 32 + (hq ? (wid >> 1) * 1024 + (wid & 1) * 32 * 3072 : 0);
          const char* fbase = (const char*)w1 + (hf ? fb * 128 : 0) + (32 * (wid >> 2) + 16 * (wid & 1) + 4 * ((wid >> 1) & 1)) * 3584;
          const unsigned vq_ = hq ? dvq : 0u, vf_ = hf ? dvf : 0u;
#define DMA_X(c) do { _Pragma("unroll") for (int i = 0; i < 4; ++i) glds16((i & 1) ? dvo1 : dvo0, xbase + CS(c) * 128 + i * 8192, lds0 + BUFOF(c) + wid * 4096 + i * 1024); } while (0)
#define DMA_ALL(c) do { glds16(vq_, qbase + CS(c) * 64 * 3072, hq ? lds0 + BUFOF(c) + XB + wid * 1024 : lds0 + L_DUMMY); \
              glds16(vf_, fbase + CS(c) * 64 * 3584, hf ? lds0 + BUFOF(c) + XB + 6144 + wid * 1024 : lds0 + L_DUMMY); DMA_X(c); } while (0)
          DMA_ALL(0);
          EPOCH_LOAD();
          if (wid == 0) {
              if (lane < SY_N) sy[lane] = 0;
              if (lane < 8) { const unsigned one = lane < 4 ? 0x3F803F80u : 0u; *(LAS u32x4*)(lds + L_MISC + (lane & 3) * 512 + (lane >> 2) * 496) = (u32x4){one, one, one, one}; }
          }
          __builtin_amdgcn_s_sleep(SLP1);
          DMA_ALL(1);
          const int xo = (32 * wid + r16) * 128, wo_ = XB + (4 * g4 + q4) * 32 + p4 * 8;
          bf16x8 xa[2], xb[2], wq[2], wk[2], wv[2];
#define RX(d, c, ks, rt) d[ks] = ldsr(lds + BUFOF(c) + xo + (rt) * 2048 + ((xsw ^ (4 * (ks))) * 16))
#define RW(d, c, ks, im) d[ks] = ldtr(lds + BUFOF(c) + wo_ + (im) * 2048 + (ks) * 1024, lds + BUFOF(c) + wo_ + (im) * 2048 + (ks) * 1024 + 512)
#define M2(A, W, ks) do { A[0] = MFMA16(W[ks], xa[ks], A[0]); A[1] = MFMA16(W[ks], xb[ks], A[1]); PIN(A[0]); PIN(A[1]); SBAR(); } while (0)
#define MV(ks) do { av[0] = MFMA16(xa[ks], wv[ks], av[0]); av[1] = MFMA16(xb[ks], wv[ks], av[1]); PIN(av[0]); PIN(av[1]); SBAR(); } while (0)
#pragma unroll
          for (int c = 0; c < 8; ++c) {
              CHUNK_SYNC(c, 6);
              if (c == 0 || c == 1 || c == 7) STAMP(12 + c);
              if (c == 7) { EPOCH_TAKE(); __builtin_amdgcn_s_setprio(3); }
              if (c + 2 < 8 && wid >= DSPLIT) DMA_ALL(c + 2);
              if (c > 0) M2(aq, wq, 1);
              RX(xa, c, 0, 0); RX(xb, c, 0, 1); RW(wq, c, 0, 0); SBAR();
              if (c > 0) M2(ak, wk, 1);
              RW(wk, c, 0, 1); RW(wv, c, 0, 2); SBAR();
              if (c > 0) MV(1);
              RX(xa, c, 1, 0); RX(xb, c, 1, 1); RW(wq, c, 1, 0); SBAR();
              M2(aq, wq, 0);
              RW(wk, c, 1, 1); RW(wv, c, 1, 2); SBAR();
              M2(ak, wk, 0);
              MV(0);
              if (c + 2 < 8 && wid < DSPLIT) DMA_ALL(c + 2);
          }
          M2(aq, wq, 1); M2(ak, wk, 1); MV(1);
#undef RX
#undef RW
#undef M2
#undef MV
#undef DMA_ALL
#undef DMA_X
        }
        STAMP(1);
        LANES();
        constexpr float QS = 0.25f * 1.4426950408889634f;
        const bf16x8 qf = __builtin_bit_cast(bf16x8, join16(aq[0], aq[1], QS, QS));
        const unsigned kvg = WS_KV + (unsigned)(hd * 8 + t) * 16384u, og = WS_O + (unsigned)((8 * t + wid) * 32 + hd) * 1024u;
        { const u32x4 kk = join16(ak[0], ak[1], 1.f, 1.f), vv = join16(av[0], av[1], 1.f, 1.f);
          const int ok = wid * 1024 + lane * 16, ov = wid * 1024 + (g4 & 1) * 512 + hh * 256 + r16 * 16;
          *(LAS u32x4*)(lds + ok) = kk; *(LAS u32x4*)(lds + L_V + ov) = vv;
          asm volatile("s_waitcnt lgkmcnt(0)" ::: "memory");
          if (lane == 0) __hip_atomic_fetch_add(sy + SY_AW, 1u, RLX_WG);
          st1(kk, rs, ok, kvg); st1(vv, rs, ov + 8192, kvg); }
        lds_wait_ge<1>(sy + SY_AW, 8);
        STAMP(2);
        drain();
        STAMP(22);
        if (lds_arrive(sy + SY_KV, lane) == 7 && lane == 0) __hip_atomic_store((GAS u64*)(ws + flag_at(0, hd, t)), tag, RLX_AGENT);
        const int tc = XORD ? ((t ^ wid) & 4) | ((t + wid) & 3) : (t + wid) & 7;
        const unsigned rkv = WS_KV + (unsigned)(hd * 8 + tc) * 16384u;
#define FETCH(NB) do { poll_tags(ws, flag_at(0, hd, tc), lane == 0, tag); STAMP(23); \
            _Pragma("unroll") for (int b_ = 0; b_ < 16 / (NB); ++b_) { u32x4 stg[NB]; \
                _Pragma("unroll") for (int i = 0; i < (NB); ++i) stg[i] = ld1(rs, lane * 16 + (b_ * (NB) + i) * 1024, rkv); \
                if (b_ == 0 && wid > 2) lds_wait_ge(sy + SY_FA, 4);              \
                _Pragma("unroll") for (int i = 0; i < (NB); ++i) *(LAS u32x4*)(lds + ((b_ * (NB) + i) >> 3) * L_V + wid * 8192 + ((b_ * (NB) + i) & 7) * 1024 + lane * 16) = stg[i]; } \
            asm volatile("s_waitcnt lgkmcnt(0)" ::: "memory"); \
            if (lane == 0) __hip_atomic_store(sy + SY_READY + wid, 1u, RLX_WG); } while (0)
        if (wid > 0 && wid < EARLYW) FETCH(16);
        const int idx = 0xDDDD;
        const LAS char* kp = lds + lane * 16;
        const bool vl = r32 < 16;
        const LAS char* vp = lds + (vl ? L_V + 512 + hh * 256 + r32 * 16 : (r32 & 11) == 0 ? L_MISC : L_MISC + 496);
        const int vstep = vl ? 2048 : 0;
        f32x16 o = (f32x16){}, negm, sA, sB = (f32x16){}; float gate = 0.f;
        bf16x8 kf = ldsr(kp), v0 = ldsr(vl ? vp - 512 : vp), v1 = (bf16x8){};
        { const f32x16 s0 = MFMA(kf, qf, (f32x16){}); const float m0 = -xhalf_max(rowmax16(s0));
#pragma unroll
          for (int i = 0; i < 16; ++i) negm[i] = m0; }
        sA = MFMA(kf, qf, negm);
        kf = ldsr(kp + 1024);
        STAMP(10);
        __builtin_amdgcn_s_setprio(0);
#define EX(S, a, b) do { _Pragma("unroll") for (int i_ = (a); i_ < (b); ++i_) S[i_] = __builtin_amdgcn_exp2f(S[i_]); PIN(S); SBAR(); } while (0)
#define LO8(S) __builtin_bit_cast(bf16x16, __builtin_shufflevector(S, S, 0, 1, 2, 3, 4, 5, 6, 7))
#define HI8(S) __builtin_bit_cast(bf16x16, __builtin_shufflevector(S, S, 8, 9, 10, 11, 12, 13, 14, 15))
#define SM(a, b) do { o = __builtin_amdgcn_smfmac_f32_32x32x32_bf16(a, b, o, idx, 0, 0); PIN(o); SBAR(); } while (0)
#define TILE(CUR, OTH, KO, VO) do { SBAR(); EX(CUR, 0, 2); SM(v1, HI8(OTH)); v1 = ldsr(vp + (VO)); SBAR(); EX(CUR, 2, 6); \
            OTH = MFMA(kf, qf, negm); PIN(OTH); SBAR(); kf = ldsr(kp + (KO)); SBAR(); EX(CUR, 6, 10); SM(v0, LO8(CUR)); v0 = ldsr(vp + (VO) + 512); SBAR(); EX(CUR, 10, 16); } while (0)
#define LEAPFROG(pos) do { if (lane == 0) __hip_atomic_store(sy + SY_PROG + wid, (unsigned)(pos), RLX_WG); \
            if ((unsigned)__builtin_amdgcn_readfirstlane(__hip_atomic_load(sy + SY_PROG + (wid ^ 4), RLX_WG)) >= (unsigned)(pos)) __builtin_amdgcn_s_setprio(1); else __builtin_amdgcn_s_setprio(0); } while (0)
#define PAIR(KO) do { TILE(sA, sB, (KO) + 2048, 0); TILE(sB, sA, (KO) + 3072, 1024); vp += vstep; } while (0)
        PAIR(0);
        if (LATEPOS == 2 && wid >= EARLYW) { __builtin_amdgcn_s_setprio(3); FETCH(8); __builtin_amdgcn_s_setprio(0); }
        PAIR(2048);
        if (LATEPOS == 1 && wid >= EARLYW) { __builtin_amdgcn_s_setprio(3); FETCH(8); __builtin_amdgcn_s_setprio(0); }
        PAIR(4096);
        if (LATEPOS == 3 && wid >= EARLYW) { __builtin_amdgcn_s_setprio(3); FETCH(8); __builtin_amdgcn_s_setprio(0); }
        lds_wait_ge(sy + SY_READY + 1, 1);
        PAIR(6144);
        if (LATEPOS == 0 && wid >= EARLYW) { __builtin_amdgcn_s_setprio(3); FETCH(8); __builtin_amdgcn_s_setprio(YP); }
#undef FETCH
        STAMP(3);
        for (int j = 1; j < 8; ++j) {
            kp += 8192;
            if (LEAP) LEAPFROG(2 * j);
            PAIR(0); PAIR(2048);
            if (LEAP == 2) LEAPFROG(2 * j + 1);
            PAIR(4096);
            if (j < 7) lds_wait_ge(sy + SY_READY + j + 1, 1);
            PAIR(6144);
            if (j == 4 && lane == 0) __hip_atomic_fetch_add(sy + SY_PASS, 1u, RLX_WG);
            if (j == TRIGJ && lane == 0) __hip_atomic_fetch_add(sy + SY_TRIG, 1u, RLX_WG);
            if (j == 6) { lds_wait_ge(sy + SY_FSEEN, 4); gate = *(const LAS float*)(lds + g_at(32 * wid + r32)); }
        }
        SM(v1, HI8(sB));
#undef PAIR
#undef LEAPFROG
#undef TILE
#undef SM
#undef EX
        asm volatile("s_waitcnt lgkmcnt(0)" ::: "memory");
        if (lane == 0) __hip_atomic_fetch_add(sy + SY_DONE, 1u, RLX_WG);
        STAMP(4);
        { float chk = o[8] * 0.f;
#pragma unroll
          for (int i = 0; i < 8; ++i) chk = __builtin_fmaf(o[i], 0.f, chk);
          if (__builtin_expect(0 != __builtin_amdgcn_ballot_w64(!(chk == 0.f) || o[8] > 1.8e19f), 0)) {
              float m = -__builtin_inff();
#pragma nounroll
              for (int tl = 0; tl < 64; ++tl) { const bf16x8 kg = __builtin_bit_cast(bf16x8, ld1(rs, lane * 16, WS_KV + (unsigned)(hd * 8 + (tl >> 3)) * 16384u + (tl & 7) * 1024));
                  m = __builtin_fmaxf(m, rowmax16(MFMA(kg, qf, (f32x16){}))); }
              m = -xhalf_max(m);
#pragma unroll
              for (int i = 0; i < 16; ++i) negm[i] = m;
              o = (f32x16){};
              const u32x4 ones = {0x3F803F80u, 0x3F803F80u, 0x3F803F80u, 0x3F803F80u};
#pragma nounroll
              for (int tl = 0; tl < 64; ++tl) { const unsigned so = WS_KV + (unsigned)(hd * 8 + (tl >> 3)) * 16384u + (tl & 7) * 1024;
                  const bf16x8 kg = __builtin_bit_cast(bf16x8, ld1(rs, lane * 16, so));
                  const u32x4 a0 = ld1(rs, 8192 + hh * 256 + r16 * 16, so), a1 = ld1(rs, 8192 + 512 + hh * 256 + r16 * 16, so);
                  o = pv(__builtin_bit_cast(bf16x8, vl ? a0 : ones), __builtin_bit_cast(bf16x8, vl ? a1 : ones), exp16(MFMA(kg, qf, negm)), o, idx); }
          } }
        { const u32x4 ov = pack8(o, 0, gate * __builtin_amdgcn_rcpf(o[8]));
          st1(unmark(ov), rs, lane * 16, og); }
        STAMP(5);
        { const int rt = wid & 1, kq = wid >> 1, kreg = (kq >> 1) * L_V + (kq & 1) * 16384;
          const unsigned ob = WS_O + (unsigned)((8 * t + 2 * rq + rt) * 32 + 8 * kq) * 1024u;
#pragma nounroll
          for (unsigned s = 0; s < SPIN_CAP; ++s) {
              if (__builtin_popcountll(__builtin_amdgcn_ballot_w64(__builtin_amdgcn_raw_buffer_load_b32(rs, (lane & 7) * 1024 + 1020, ob, 16) == SENT)) <= 8 * LATE_OK) break;
              __builtin_amdgcn_s_sleep(1); CFENCE(); }
          u32x4 of[8];
#pragma unroll
          for (int i = 0; i < 8; ++i) of[i] = ld1(rs, lane * 16, ob + i * 1024);
          lds_wait_ge<1>(sy + SY_FP + kq, 1);
          f32x16 c[2];
#pragma unroll
          for (int ct = 0; ct < 2; ++ct)
#pragma unroll
              for (int g = 0; g < 4; ++g) { const f32x4 v = *(const LAS f32x4*)(lds + kreg + 32768 + (2 * rt + ct) * 4096 + g * 1024 + lane * 16);
#pragma unroll
                  for (int i = 0; i < 4; ++i) c[ct][4 * g + i] = v[i]; }
          PIN(c[0]); PIN(c[1]);
#pragma nounroll
          for (unsigned s = 0; s < SPIN_CAP; ++s) {
              unsigned miss = 0;
#pragma unroll
              for (int i = 0; i < 8; ++i) if (marked(of[i])) miss |= 1u << i;
              if (miss == 0) break;
              CFENCE();
#pragma unroll
              for (int i = 0; i < 8; ++i) if ((miss >> i) & 1) of[i] = ld1(rs, lane * 16, ob + i * 1024);
          }
          STAMP(11);
#pragma unroll
          for (int n = 0; n < 8; ++n) { const LAS char* p = lds + kreg + n * 2048 + lane * 16;
              c[0] = MFMA(ldsr(p), __builtin_bit_cast(bf16x8, of[n]), c[0]); c[1] = MFMA(ldsr(p + 1024), __builtin_bit_cast(bf16x8, of[n]), c[1]); }
          f32x8 own;
#pragma unroll
          for (int pc = 0; pc < 4; ++pc) {
              if (pc == kq) {
#pragma unroll
                  for (int i = 0; i < 8; ++i) own[i] = c[pc >> 1][8 * (pc & 1) + i];
              } else {
#pragma unroll
                  for (int g = 0; g < 2; ++g) { const int r = 8 * (pc & 1) + 4 * g; *(LAS f32x4*)(lds + kreg + 32768 + rt * 8192 + pc * 2048 + g * 1024 + lane * 16) = (f32x4){c[pc >> 1][r], c[pc >> 1][r + 1], c[pc >> 1][r + 2], c[pc >> 1][r + 3]}; }
              } }
          asm volatile("s_waitcnt lgkmcnt(0)" ::: "memory");
          if (lane == 0) __hip_atomic_fetch_add(sy + SY_RED + rt, 1u, RLX_WG);
          lds_wait_ge<0>(sy + SY_RED + rt, 4);
          STAMP(21);
#pragma unroll
          for (int k = 1; k < 4; ++k) { const int w = (kq + k) & 3;
#pragma unroll
              for (int g = 0; g < 2; ++g) { const f32x4 v = *(const LAS f32x4*)(lds + (w >> 1) * L_V + (w & 1) * 16384 + 32768 + rt * 8192 + kq * 2048 + g * 1024 + lane * 16);
#pragma unroll
                  for (int i = 0; i < 4; ++i) own[4 * g + i] += v[i]; } }
          auto a = __builtin_amdgcn_permlane32_swap(pack2(own[0], own[1]), pack2(own[4], own[5]), false, false), b = __builtin_amdgcn_permlane32_swap(pack2(own[2], own[3]), pack2(own[6], own[7]), false, false);
          *(u32x4*)(out + (size_t)(256 * t + 64 * rq + 32 * rt + r32) * DM + 64 * cp + 16 * kq + 8 * hh) = (u32x4){a[0], b[0], a[1], b[1]};
          if (bx == 0 && wid == 0 && lane == 0) __hip_atomic_store((GAS u64*)(ws + WS_FLAGS), tag, RLX_AGENT);
        }
        STAMP(9); STAMP_FLUSH();
    } else {
        const int f = wid - 8, i32 = rq + 4 * t;
        { LANES(); const int xsw = g4 ^ (r16 >> 1);
          EPOCH_LOAD();
          if (f == 1 && lane < 32) { if (hasF) glds4(lane * 4, (const char*)b1 + fb * 128, lds0 + L_B1); glds4(lane * 4, (const char*)b2 + cp * 128, lds0 + L_B2); }
          if (hd == 31) { glds16(lane * 16, gate_w, lds0 + L_GATE + f * 1024); asm volatile("s_waitcnt vmcnt(0)" ::: "memory"); }
          const int xo = (64 * f + r16) * 128, fo = XB + 6144 + (4 * g4 + q4) * 128 + p4 * 8, fsw = (2 * g4 + (q4 >> 1)) & 3;
          bf16x8 xf[2][4], wf[2][4];
#define RX(c, ks, rt) xf[ks][rt] = ldsr(lds + BUFOF(c) + xo + (rt) * 2048 + ((xsw ^ (4 * (ks))) * 16))
#define RW(c, ks, cb) wf[ks][cb] = ldtr(lds + BUFOF(c) + fo + (ks) * 4096 + (((cb) ^ fsw) * 32), lds + BUFOF(c) + fo + (ks) * 4096 + 2048 + (((cb) ^ fsw) * 32))
#define M2(ks, cb, r0) do { af[cb][r0] = MFMA16(wf[ks][cb], xf[ks][r0], af[cb][r0]); af[cb][r0 + 1] = MFMA16(wf[ks][cb], xf[ks][r0 + 1], af[cb][r0 + 1]); PIN(af[cb][r0]); PIN(af[cb][r0 + 1]); SBAR(); } while (0)
#define F_SYNC(c) do { if ((c) != RTC && (c) != 0) asm volatile("s_waitcnt lgkmcnt(0)\n\ts_barrier" ::: "memory"); else asm volatile("s_waitcnt vmcnt(0) lgkmcnt(0)\n\ts_barrier" ::: "memory"); \
              if ((c) == 0 || (c) == 7) STAMP(12 + (c)); \
              if ((c) == RTC) { EPOCH_TAKE(); LANES();     \
                  const int u = i32 + 32 * f; const char* src = u < 112 ? (const char*)w2 + (size_t)u * 16384 : (const char*)wo + (size_t)(u - 112) * 16384; \
                  const unsigned vo = (lane >> 3) * 1024 + cp * 128 + (lane & 7) * 16; \
                  glds16(vo, src, lds0 + L_RT + f * 2048); glds16(vo, src + 8192, lds0 + L_RT + f * 2048 + 1024); \
                  if (f == 0 && i32 < 16) { const char* s2 = (const char*)wo + (size_t)(i32 + 16) * 16384; glds16(vo, s2, lds0 + L_RT + 8192); glds16(vo, s2 + 8192, lds0 + L_RT + 8192 + 1024); } } } while (0)
#define F_LEAVE() STAMP(1); LANES(); \
              asm volatile("s_waitcnt lgkmcnt(0)" ::: "memory"); \
              if (lane == 0) __hip_atomic_fetch_add(sy + SY_FA, 1u, RLX_WG); \
              __builtin_amdgcn_s_setprio(2);
#define F_RETILE() do { \
              _Pragma("unroll") for (int i = 0; i < 2; ++i)     \
                  st1((u32x4){SENT, SENT, SENT, SENT}, rs, lane * 16, WS_O + (unsigned)((8 * t + 2 * f + i) * 32 + hd) * 1024u); \
              for (int n = f; n < ((f == 0 && i32 < 16) ? 5 : 4); n += 4) {        \
                  const int u = i32 + 32 * n, r0 = (u < 112 ? 8 : 4) * hh + q4, r1 = u < 112 ? 512 : 1024; \
                  _Pragma("unroll") for (int ct = 0; ct < 2; ++ct) { const LAS char* s = lds + L_RT + n * 2048 + r0 * 128 + (g4 & 1) * 32 + p4 * 8 + ct * 64; \
                      st1(__builtin_bit_cast(u32x4, ldtr(s, s + r1)), rs, lane * 16, WS_W + (unsigned)((2 * cp + ct) * 144 + u) * 1024u); } } } while (0)
          if (hasF) {
              f32x4 af[4][4];
#pragma unroll
              for (int c = 0; c < 8; ++c) {
                  F_SYNC(c);
                  if (c == 0) {
#pragma unroll
                      for (int cb = 0; cb < 4; ++cb) { const u32x2 w = *(const LAS u32x2*)(lds + L_B1 + cb * 32 + g4 * 8);
#pragma unroll
                          for (int rt = 0; rt < 4; ++rt) af[cb][rt] = (f32x4){U2F(w[0] << 16), U2F(w[0] & 0xffff0000u), U2F(w[1] << 16), U2F(w[1] & 0xffff0000u)}; }
                  }
                  if (FDEFER && c == 7) lds_wait_ge<2>(sy + SY_AW, 8);
                  if (c > 0) M2(1, 0, 0);
                  RX(c, 0, 0); SBAR();
                  if (c > 0) M2(1, 0, 2);
                  RW(c, 0, 0); SBAR();
                  if (c > 0) M2(1, 1, 0);
                  RX(c, 0, 1); SBAR();
                  if (c > 0) M2(1, 1, 2);
                  RX(c, 0, 2); SBAR();
                  if (c > 0) M2(1, 2, 0);
                  RX(c, 0, 3); SBAR();
                  if (c > 0) M2(1, 2, 2);
                  RW(c, 0, 1); SBAR();
                  if (c > 0) M2(1, 3, 0);
                  RW(c, 0, 2); SBAR();
                  if (c > 0) M2(1, 3, 2);
                  RW(c, 0, 3); SBAR();
                  M2(0, 0, 0); RX(c, 1, 0); RW(c, 1, 0); SBAR();
                  M2(0, 0, 2); RX(c, 1, 1); RX(c, 1, 2); SBAR();
                  M2(0, 1, 0); RX(c, 1, 3); RW(c, 1, 1); SBAR();
                  M2(0, 1, 2); RW(c, 1, 2); RW(c, 1, 3); SBAR();
                  M2(0, 2, 0); M2(0, 2, 2); M2(0, 3, 0); M2(0, 3, 2);
              }
              M2(1, 0, 0); M2(1, 0, 2); M2(1, 1, 0); M2(1, 1, 2); M2(1, 2, 0); M2(1, 2, 2); M2(1, 3, 0); M2(1, 3, 2);
              { F_LEAVE();
                u32x4 hp[4][2];
#pragma unroll
                for (int cb = 0; cb < 4; ++cb) {
#pragma unroll
                    for (int rp = 0; rp < 2; ++rp) {
                        float v[8], e[8];
#pragma unroll
                        for (int i = 0; i < 8; ++i) { v[i] = af[cb][2 * rp + (i >> 2)][i & 3];
                            e[i] = v[i] * __builtin_fmaf(v[i] * v[i], -2.f * 0.7978845608028654f * 1.4426950408889634f * 0.044715f, -2.f * 0.7978845608028654f * 1.4426950408889634f); }
                        SBAR();
#pragma unroll
                        for (int i = 0; i < 8; ++i) e[i] = __builtin_amdgcn_exp2f(e[i]);
                        SBAR();
#pragma unroll
                        for (int i = 0; i < 8; ++i) e[i] = 1.f + e[i];
                        SBAR();
#pragma unroll
                        for (int i = 0; i < 8; ++i) e[i] = __builtin_amdgcn_rcpf(e[i]);
                        SBAR();
                        hp[cb][rp] = join16((f32x4){v[0] * e[0], v[1] * e[1], v[2] * e[2], v[3] * e[3]}, (f32x4){v[4] * e[4], v[5] * e[5], v[6] * e[6], v[7] * e[7]}, 1.f, 1.f);
                        if (!HLATE) st1(hp[cb][rp], rs, lane * 16, WS_H + (unsigned)((8 * t + 2 * f + rp) * 112 + 4 * fb + cb) * 1024u);
                        PIN(hp[cb][rp]); SBAR();
                    }
                }
                STAMP(2);
                if (HLATE) { drain();
#pragma unroll
                    for (int i = 0; i < 8; ++i) st1(hp[i >> 1][i & 1], rs, lane * 16, WS_H + (unsigned)((8 * t + 2 * f + (i & 1)) * 112 + 4 * fb + (i >> 1)) * 1024u);
                } else asm volatile("s_waitcnt vmcnt(8)" ::: "memory");
                F_RETILE(); }
          } else {
              f32x4 az[4] = {};
#pragma unroll
              for (int c = 0; c < 8; ++c) {
                  F_SYNC(c);
                  if (hd == 31) {
#pragma unroll
                      for (int ks = 0; ks < 2; ++ks) { const bf16x8 gf = ldsr(lds + L_GATE + f * 1024 + CS(c) * 128 + ks * 64 + g4 * 16);
#pragma unroll
                          for (int rt = 0; rt < 4; ++rt) { RX(c, ks, rt); az[rt] = MFMA16(gf, xf[ks][rt], az[rt]); } }
                  }
              }
              { F_LEAVE(); drain(); F_RETILE();
                if (hd == 31) {
                    const float z = bfround(g4 == 0 ? az[0][0] : g4 == 1 ? az[1][0] : g4 == 2 ? az[2][0] : az[3][0]);
                    __hip_atomic_store((GAS float*)(ws + WS_G + (256 * t + 64 * f + lane) * 4), bfround(__builtin_amdgcn_rcpf(1.f + __builtin_amdgcn_exp2f(-1.4426950408889634f * z))), RLX_AGENT);
                } }
          }
#undef F_SYNC
#undef F_LEAVE
#undef F_RETILE
#undef RX
#undef RW
#undef M2
        }
        LANES();
        const int freg = (f >> 1) * L_V + (f & 1) * 16384;
        drain();
        if (lds_arrive(sy + SY_F, lane) == 3 && lane == 0) __hip_atomic_store((GAS u64*)(ws + flag_at(1, hd, t)), tag, RLX_AGENT);
        STAMP(3);
        f32x16 acc[4] = {};
        poll_tags<4>(ws, hh ? flag_at(1, cp + 8 * (r32 & 3), r32 >> 2) : flag_at(1, r32, t), true, tag);
        *(LAS float*)(lds + g_at(64 * f + lane)) = __hip_atomic_load((GAS float*)(ws + WS_G + (256 * t + 64 * f + lane) * 4), RLX_AGENT);
        asm volatile("s_waitcnt lgkmcnt(0)" ::: "memory");
        if (lane == 0) __hip_atomic_fetch_add(sy + SY_FSEEN, 1u, RLX_WG);
        __builtin_amdgcn_s_setprio(F2_PRIO);
        STAMP(4);
        const unsigned hb = WS_H + (unsigned)((8 * t + 2 * rq) * 112 + 28 * f) * 1024u, wb = WS_W + (unsigned)(2 * cp * 144 + 28 * f) * 1024u;
        u32x4 fr[4][4];
#define LDK(slot, n) do { fr[slot][0] = ld1(rs, lane * 16, wb + (n) * 1024); fr[slot][1] = ld1(rs, lane * 16, wb + (144 + (n)) * 1024); \
                          fr[slot][2] = ld1(rs, lane * 16, hb + (n) * 1024); fr[slot][3] = ld1(rs, lane * 16, hb + (112 + (n)) * 1024); } while (0)
#define BC(v) __builtin_bit_cast(bf16x8, v)
#define MM4(w0, w1_, h0, h1) do { acc[0] = MFMA(w0, h0, acc[0]); acc[1] = MFMA(w0, h1, acc[1]); acc[2] = MFMA(w1_, h0, acc[2]); acc[3] = MFMA(w1_, h1, acc[3]); PIN(acc[0]); PIN(acc[1]); PIN(acc[2]); PIN(acc[3]); SBAR(); } while (0)
#define FFN2(n0, n1, pace) do { _Pragma("unroll") for (int n = (n0); n < (n1); ++n) { MM4(BC(fr[n & 3][0]), BC(fr[n & 3][1]), BC(fr[n & 3][2]), BC(fr[n & 3][3])); if (n + 4 < (n1)) LDK(n & 3, n + 4); if (pace) __builtin_amdgcn_s_sleep(pace); SBAR(); } } while (0)
#pragma unroll
        for (int n = 0; n < 4; ++n) LDK(n, n);
        FFN2(0, NP1, F2_PACE);
        STAMP(5);
        { u32x4 tmp[16];
#pragma unroll
          for (int i = 0; i < 16; ++i) tmp[i] = ld1(rs, lane * 16, WS_W + (unsigned)((2 * cp + (i & 1)) * 144 + 112 + 8 * f + (i >> 1)) * 1024u);
          __builtin_amdgcn_s_setprio(2);
          lds_wait_ge<16>(sy + SY_PASS, 8);
#pragma unroll
          for (int i = 0; i < 16; ++i) *(LAS u32x4*)(lds + freg + i * 1024 + lane * 16) = tmp[i]; }
        STAMP(6);
        if (NP1 < 28) {
#pragma unroll
            for (int n = NP1; n < NP1 + 4; ++n) LDK(n & 3, n);
            lds_wait_ge<8>(sy + SY_TRIG, 8);
            __builtin_amdgcn_s_setprio(F2_PRIO);
            FFN2(NP1, 28, 0);
        }
#undef LDK
#undef FFN2
        lds_wait_ge(sy + SY_FSEEN, 4);
        { const float og0 = bfround(1.f - *(const LAS float*)(lds + g_at(64 * rq + r32))), og1 = bfround(1.f - *(const LAS float*)(lds + g_at(64 * rq + 32 + r32)));
#pragma unroll
          for (int ct = 0; ct < 2; ++ct)
#pragma unroll
              for (int g = 0; g < 4; ++g) { u32x2 w = *(const LAS u32x2*)(lds + L_B2 + ct * 64 + g * 16 + hh * 8); if (f != 0) w = (u32x2){0u, 0u};
                  const float bb[4] = {U2F(w[0] << 16), U2F(w[0] & 0xffff0000u), U2F(w[1] << 16), U2F(w[1] & 0xffff0000u)};
#pragma unroll
                  for (int i = 0; i < 4; ++i) { acc[2 * ct][4 * g + i] = (acc[2 * ct][4 * g + i] + bb[i]) * og0; acc[2 * ct + 1][4 * g + i] = (acc[2 * ct + 1][4 * g + i] + bb[i]) * og1; } } }
#undef MM4
#undef BC
        STAMP(7);
        lds_wait_ge<1>(sy + SY_DONE, 8);
#pragma unroll
        for (int qd = 0; qd < 4; ++qd)
#pragma unroll
            for (int g = 0; g < 4; ++g) *(LAS f32x4*)(lds + freg + 32768 + (2 * (qd & 1) + (qd >> 1)) * 4096 + g * 1024 + lane * 16) = (f32x4){acc[qd][4 * g], acc[qd][4 * g + 1], acc[qd][4 * g + 2], acc[qd][4 * g + 3]};
        asm volatile("s_waitcnt lgkmcnt(0)" ::: "memory");
        if (lane == 0) __hip_atomic_store(sy + SY_FP + f, 1u, RLX_WG);
        STAMP(9); STAMP_FLUSH();
    }
}

extern "C" void kernel_launch(void* const* d_in, const int* in_sizes, int n_in, void* d_out, int out_size, void* d_ws, size_t ws_size, hipStream_t stream) {
    (void)in_sizes; (void)n_in; (void)out_size; (void)ws_size;
    hipLaunchKernelGGL(gated_mix_fused, dim3(256), dim3(768), 0, stream, (const unsigned short*)d_in[0], (const unsigned short*)d_in[1], (const unsigned short*)d_in[2],
                       (const unsigned short*)d_in[4], (const unsigned short*)d_in[5], (const unsigned short*)d_in[6], (const unsigned short*)d_in[7], (const unsigned short*)d_in[8],
                       (unsigned short*)d_out, (char*)d_ws);
}
```

```cpp
#include <hip/hip_runtime.h>
#include <hip/hip_bf16.h>
#include <stdint.h>

typedef __attribute__((ext_vector_type(8))) __bf16 bf16x8;
typedef __attribute__((ext_vector_type(16))) __bf16 bf16x16;
typedef __attribute__((ext_vector_type(2))) __bf16 bf16x2;
typedef __attribute__((ext_vector_type(4))) short s16x4;
typedef __attribute__((ext_vector_type(16))) float f32x16;
typedef __attribute__((ext_vector_type(8))) float f32x8;
typedef __attribute__((ext_vector_type(4))) float f32x4;
typedef __attribute__((ext_vector_type(2))) float f32x2;
typedef __attribute__((ext_vector_type(4))) unsigned u32x4;
typedef __attribute__((ext_vector_type(2))) unsigned u32x2;
typedef unsigned long long u64;
#define LAS __attribute__((address_space(3)))
#define GAS __attribute__((address_space(1)))
#if defined(__HIP_DEVICE_COMPILE__) && !defined(ALLOW_PK)
#define NOPK __attribute__((target("no-packed-fp32-ops")))
#else
#define NOPK
#endif
#define DI __device__ __forceinline__ NOPK
#define U2F(x) __builtin_bit_cast(float, (unsigned)(x))
#define F2U(x) __builtin_bit_cast(unsigned, (float)(x))
#define RLX_AGENT __ATOMIC_RELAXED, __HIP_MEMORY_SCOPE_AGENT
#define RLX_WG __ATOMIC_RELAXED, __HIP_MEMORY_SCOPE_WORKGROUP

constexpr int DM = 512, DFF = 1792;
constexpr unsigned WS_FLAGS = 0;
constexpr unsigned WS_FLAG0 = 4096, FLAG_STRIDE = 128;
constexpr unsigned WS_G = 106496;
constexpr unsigned WS_KV = 131072;
constexpr unsigned WS_H = WS_KV + 32u * 8u * 16384u;
constexpr unsigned WS_O = WS_H + 64u * 112u * 1024u;
constexpr unsigned WS_W = WS_O + 64u * 32u * 1024u;
constexpr unsigned WS_END = WS_W + 16u * 144u * 1024u;
constexpr unsigned WS_DIAG = WS_END;
constexpr int XB = 32768, BUFB = XB + 6144 + 8192;
constexpr int L_V = 65536;
constexpr int L_GATE = 3 * BUFB;
constexpr int L_RT = L_GATE + 4096;
constexpr int L_MISC = 163840 - 2048;
constexpr int L_DUMMY = L_RT + 10240;
constexpr int L_B1 = L_MISC + 272, L_B2 = L_MISC + 512 + 272, L_SYNC = L_MISC + 1024 + 272;
static_assert(L_DUMMY + 1024 <= L_MISC && L_V + 3 * 8192 <= 2 * BUFB && 3 * 8192 <= BUFB, "lds");
enum { SY_READY = 0  , SY_KV = 8, SY_DONE = 9, SY_F = 10, SY_PASS = 11, SY_TRIG = 13, SY_FSEEN = 14, SY_AW = 15, SY_FA = 16, SY_RED = 18  , SY_FP = 20  , SY_PROG = 24  , SY_N = 32 };
constexpr unsigned SENT = 0xFFFFFFFFu;
constexpr unsigned SPIN_CAP = 1u << 18;

#define MFMA(a, b, c) __builtin_amdgcn_mfma_f32_32x32x16_bf16((a), (b), (c), 0, 0, 0)
#define MFMA16(a, b, c) __builtin_amdgcn_mfma_f32_16x16x32_bf16((a), (b), (c), 0, 0, 0)
#define SBAR() __builtin_amdgcn_sched_barrier(0)
#define CFENCE() asm volatile("" ::: "memory")
#define PIN(x) asm volatile("" : "+v"(x))

#define F2_PRIO 0
#define DSPLIT 4
#define ROTX (2 * ((hd + t) & 3))
#define EARLYW 4
#define F2_PACE 0
#define FDEFER 1
#define RTC 7
#define HLATE 0
#define SLP1 12
#define LATE_OK 2
#define XORD 1
#define LATEPOS 0
#define YP 3
#define SCHED 0
#define LEAP 0
#define NP1 24
#define TRIGJ 6
#define STAMP(i) do {} while (0)
#define STAMP_FLUSH() do {} while (0)

DI unsigned pack2(float lo, float hi) { f32x2 v = {lo, hi}; return __builtin_bit_cast(unsigned, __builtin_convertvector(v, bf16x2)); }
DI u32x4 pack8(const f32x16& a, int o, float s) {
    u32x4 r; r[0] = pack2(a[o] * s, a[o + 1] * s); r[1] = pack2(a[o + 2] * s, a[o + 3] * s); r[2] = pack2(a[o + 4] * s, a[o + 5] * s); r[3] = pack2(a[o + 6] * s, a[o + 7] * s); return r; }
DI u32x4 pack8(const f32x16& a, int o) {
    u32x4 r; r[0] = pack2(a[o], a[o + 1]); r[1] = pack2(a[o + 2], a[o + 3]); r[2] = pack2(a[o + 4], a[o + 5]); r[3] = pack2(a[o + 6], a[o + 7]); return r; }
DI float bfround(float v) { return (float)(__bf16)v; }
DI float bf2f(unsigned short u) { return U2F((unsigned)u << 16); }
DI bf16x8 ldsr(const LAS char* p) { return *(const LAS bf16x8*)p; }
DI bf16x8 ldtr(const LAS char* p0, const LAS char* p1) {
    s16x4 a = __builtin_amdgcn_ds_read_tr16_b64_v4i16((LAS s16x4*)p0), b = __builtin_amdgcn_ds_read_tr16_b64_v4i16((LAS s16x4*)p1);
    return __builtin_bit_cast(bf16x8, __builtin_shufflevector(a, b, 0, 1, 2, 3, 4, 5, 6, 7)); }
DI void glds16(unsigned voff, const void* sbase, unsigned lds) {
    unsigned keep; asm volatile("s_mov_b32 %0, m0\n\ts_mov_b32 m0, %3\n\ts_nop 0\n\tglobal_load_lds_dwordx4 %1, %2\n\ts_mov_b32 m0, %0" : "=&s"(keep) : "v"(voff), "s"(sbase), "s"(lds) : "memory"); }
DI void glds4(unsigned voff, const void* sbase, unsigned lds) {
    unsigned keep; asm volatile("s_mov_b32 %0, m0\n\ts_mov_b32 m0, %3\n\ts_nop 0\n\tglobal_load_lds_dword %1, %2\n\ts_mov_b32 m0, %0" : "=&s"(keep) : "v"(voff), "s"(sbase), "s"(lds) : "memory"); }
DI u32x4 ld1(__amdgpu_buffer_rsrc_t rs, unsigned voff, unsigned soff) { return __builtin_amdgcn_raw_buffer_load_b128(rs, voff, soff, 16); }
DI void st1(u32x4 v, __amdgpu_buffer_rsrc_t rs, unsigned voff, unsigned soff) {
    __builtin_amdgcn_raw_buffer_store_b128(v, rs, voff, soff, 16); asm volatile("s_nop 1" :: "v"(v) : "memory"); }
DI u32x4 unmark(u32x4 v) {
#pragma unroll
    for (int i = 0; i < 4; ++i) v[i] = v[i] == SENT ? 0x7FFF7FFFu : v[i];
    return v; }
DI bool marked(const u32x4& d) { return __builtin_amdgcn_ballot_w64(__builtin_elementwise_max(__builtin_elementwise_max(d[0], d[1]), __builtin_elementwise_max(d[2], d[3])) == SENT) != 0; }
DI void drain() { asm volatile("s_waitcnt vmcnt(0)" ::: "memory"); }
DI unsigned lds_arrive(LAS unsigned* w, int lane) {
    unsigned old = 0; if (lane == 0) old = __hip_atomic_fetch_add(w, 1u, RLX_WG); return __builtin_amdgcn_readfirstlane(old); }
template <int SL = 1> DI void lds_wait_ge(LAS unsigned* w, unsigned want) {
    unsigned tv, ts, n;
    asm volatile("s_mov_b32 %2, 0x400000\n"
                 "1:\n\tds_read_b32 %0, %3\n\ts_waitcnt lgkmcnt(0)\n\tv_readfirstlane_b32 %1, %0\n\ts_nop 1\n\ts_cmp_ge_u32 %1, %4\n\ts_cbranch_scc1 2f\n\t"
                 "s_sleep %5\n\ts_sub_u32 %2, %2, 1\n\ts_cmp_lg_u32 %2, 0\n\ts_cbranch_scc1 1b\n"
                 "2:" : "=&v"(tv), "=&s"(ts), "=&s"(n) : "v"((unsigned)(uintptr_t)w), "s"(want), "i"(SL) : "memory", "scc"); }
template <int SL = 1> DI void poll_tags(const char* ws, unsigned flag_off, bool active, u64 tag) {
#pragma nounroll
    for (unsigned s = 0; s < SPIN_CAP; ++s) {
        u64 v = tag; if (active) v = __hip_atomic_load((GAS u64*)(ws + flag_off), RLX_AGENT);
        if (__builtin_amdgcn_ballot_w64(v != tag) == 0) break; __builtin_amdgcn_s_sleep(SL); }
    __builtin_amdgcn_fence(__ATOMIC_ACQUIRE, "wavefront"); CFENCE(); }
DI unsigned flag_at(int kind, int hd, int t) { return WS_FLAG0 + (unsigned)(kind * 256 + hd * 8 + t) * FLAG_STRIDE; }
DI int g_at(int i) { return L_MISC + (i >> 6) * 512 + 16 + (i & 63) * 4; }
DI u32x4 join16(const f32x4& a, const f32x4& b, float sa, float sb) {
    auto r0 = __builtin_amdgcn_permlane16_swap(pack2(a[0] * sa, a[1] * sa), pack2(b[0] * sb, b[1] * sb), false, false);
    auto r1 = __builtin_amdgcn_permlane16_swap(pack2(a[2] * sa, a[3] * sa), pack2(b[2] * sb, b[3] * sb), false, false);
    return (u32x4){r0[0], r1[0], r0[1], r1[1]}; }
DI f32x16 exp16(f32x16 s) {
#pragma unroll
    for (int i = 0; i < 16; ++i) s[i] = __builtin_amdgcn_exp2f(s[i]);
    return s; }
DI f32x16 pv(bf16x8 v0, bf16x8 v1, const f32x16& p, f32x16 o, int idx) {
    const f32x8 lo = __builtin_shufflevector(p, p, 0, 1, 2, 3, 4, 5, 6, 7), hi = __builtin_shufflevector(p, p, 8, 9, 10, 11, 12, 13, 14, 15);
    o = __builtin_amdgcn_smfmac_f32_32x32x32_bf16(v0, __builtin_bit_cast(bf16x16, lo), o, idx, 0, 0);
    o = __builtin_amdgcn_smfmac_f32_32x32x32_bf16(v1, __builtin_bit_cast(bf16x16, hi), o, idx, 0, 0);
    return o; }
DI float rowmax16(const f32x16& s) {
    float m = __builtin_fmaxf(__builtin_fmaxf(s[0], s[1]), s[2]);
#pragma unroll
    for (int i = 3; i < 15; i += 2) m = __builtin_fmaxf(__builtin_fmaxf(m, s[i]), s[i + 1]);
    return __builtin_fmaxf(m, s[15]); }
DI float xhalf_max(float m) { auto r = __builtin_amdgcn_permlane32_swap(F2U(m), F2U(m), false, false); return __builtin_fmaxf(U2F(r[0]), U2F(r[1])); }

__global__ __launch_bounds__(768, 3) NOPK void gated_mix_fused(const unsigned short* __restrict__ x, const unsigned short* __restrict__ wqkv, const unsigned short* __restrict__ wo,
                                                         const unsigned short* __restrict__ w1, const unsigned short* __restrict__ b1, const unsigned short* __restrict__ w2,
                                                         const unsigned short* __restrict__ b2, const unsigned short* __restrict__ gate_w, unsigned short* __restrict__ out, char* ws) {
    __shared__ __attribute__((aligned(1024))) char smem[163840];
    LAS char* lds = (LAS char*)smem;
    const unsigned lds0 = (unsigned)(uintptr_t)lds;
    const int wid = __builtin_amdgcn_readfirstlane(__builtin_amdgcn_workitem_id_x() >> 6);
#define LANES() int lane = __builtin_amdgcn_mbcnt_hi(~0u, __builtin_amdgcn_mbcnt_lo(~0u, 0u)); PIN(lane); const int r32 = lane & 31, hh = lane >> 5, g4 = lane >> 4, r16 = lane & 15, q4 = r16 >> 2, p4 = lane & 3; (void)r32; (void)hh; (void)g4; (void)q4; (void)p4
    const int bx = __builtin_amdgcn_workgroup_id_x(), hd = 8 * (bx & 3) + ((bx >> 3) & 7), t = 4 * ((bx >> 2) & 1) + (bx >> 6);
    const __amdgpu_buffer_rsrc_t rs = __builtin_amdgcn_make_buffer_rsrc(ws, 0, WS_END, 0x00020000);
    LAS unsigned* sy = (LAS unsigned*)(lds + L_SYNC);
    asm volatile("" :: "s"(x), "s"(wqkv), "s"(wo), "s"(w1), "s"(b1), "s"(w2), "s"(b2), "s"(gate_w), "s"(out), "s"(ws));
    u64 tag; u32x2 ep;
#define EPOCH_LOAD() asm volatile("global_load_dwordx2 %0, %1, %2" : "=v"(ep) : "v"(0), "s"(ws) : "memory")
#define EPOCH_TAKE() do { PIN(ep); tag = (((u64)(unsigned)__builtin_amdgcn_readfirstlane(ep[1]) << 32) | (unsigned)__builtin_amdgcn_readfirstlane(ep[0])) + 1ull; } while (0)
    const int cp = hd & 7, rq = hd >> 3;
    const bool hasF = cp != 7; const int fb = hd - rq;
    const int rot = ROTX;
    STAMP(0);
#define CS(c) (((c) + rot) & 7)
#define BUFOF(c) ((((c) + 1) % 3) * BUFB)
#define CHUNK_SYNC(c, n) do { if ((c) < 7) asm volatile("s_waitcnt vmcnt(" #n ") lgkmcnt(0)\n\ts_barrier" ::: "memory"); else asm volatile("s_waitcnt vmcnt(0) lgkmcnt(0)\n\ts_barrier" ::: "memory"); } while (0)

    if (__builtin_expect(wid < 8, 1)) {
        f32x4 aq[2] = {}, ak[2] = {}, av[2] = {};
        { LANES(); const int xsw = g4 ^ (r16 >> 1);
          const int c0 = (lane & 7) ^ (lane >> 4), rho = lane >> 1, r8 = lane >> 3;
          const unsigned dvo0 = r8 * 1024 + c0 * 16, dvo1 = r8 * 1024 + (c0 ^ 4) * 16;
          const unsigned dvq = (8 * ((rho >> 2) & 3) + 4 * (rho >> 4) + (rho & 3)) * 3072 + (lane & 1) * 16;
          const unsigned dvf = (8 * (r8 >> 2) + (r8 & 3)) * 3584 + (((lane & 7) ^ (r8 & 6)) * 16);
          const char* xbase = (const char*)x + (size_t)(256 * t + 32 * wid) * 1024;
          const bool hq = wid < 6, hf = hasF;
          const char* qbase = (const char*)wqkv + hd * 32 + (hq ? (wid >> 1) * 1024 + (wid & 1) * 32 * 3072 : 0);
          const char* fbase = (const char*)w1 + (hf ? fb * 128 : 0) + (32 * (wid >> 2) + 16 * (wid & 1) + 4 * ((wid >> 1) & 1)) * 3584;
          const unsigned vq_ = hq ? dvq : 0u, vf_ = hf ? dvf : 0u;
#define DMA_X(c) do { _Pragma("unroll") for (int i = 0; i < 4; ++i) glds16((i & 1) ? dvo1 : dvo0, xbase + CS(c) * 128 + i * 8192, lds0 + BUFOF(c) + wid * 4096 + i * 1024); } while (0)
#define DMA_ALL(c) do { glds16(vq_, qbase + CS(c) * 64 * 3072, hq ? lds0 + BUFOF(c) + XB + wid * 1024 : lds0 + L_DUMMY); \
              glds16(vf_, fbase + CS(c) * 64 * 3584, hf ? lds0 + BUFOF(c) + XB + 6144 + wid * 1024 : lds0 + L_DUMMY); DMA_X(c); } while (0)
          DMA_ALL(0);
          EPOCH_LOAD();
          if (wid == 0) {
              if (lane < SY_N) sy[lane] = 0;
              if (lane < 8) { const unsigned one = lane < 4 ? 0x3F803F80u : 0u; *(LAS u32x4*)(lds + L_MISC + (lane & 3) * 512 + (lane >> 2) * 496) = (u32x4){one, one, one, one}; }
          }
          __builtin_amdgcn_s_sleep(SLP1);
          DMA_ALL(1);
          const int xo = (32 * wid + r16) * 128, wo_ = XB + (4 * g4 + q4) * 32 + p4 * 8;
          bf16x8 xa[2], xb[2], wq[2], wk[2], wv[2];
#define RX(d, c, ks, rt) d[ks] = ldsr(lds + BUFOF(c) + xo + (rt) * 2048 + ((xsw ^ (4 * (ks))) * 16))
#define RW(d, c, ks, im) d[ks] = ldtr(lds + BUFOF(c) + wo_ + (im) * 2048 + (ks) * 1024, lds + BUFOF(c) + wo_ + (im) * 2048 + (ks) * 1024 + 512)
#define M2(A, W, ks) do { A[0] = MFMA16(W[ks], xa[ks], A[0]); A[1] = MFMA16(W[ks], xb[ks], A[1]); PIN(A[0]); PIN(A[1]); SBAR(); } while (0)
#define MV(ks) do { av[0] = MFMA16(xa[ks], wv[ks], av[0]); av[1] = MFMA16(xb[ks], wv[ks], av[1]); PIN(av[0]); PIN(av[1]); SBAR(); } while (0)
#pragma unroll
          for (int c = 0; c < 8; ++c) {
              CHUNK_SYNC(c, 6);
              if (c == 0 || c == 1 || c == 7) STAMP(12 + c);
              if (c == 7) { EPOCH_TAKE(); __builtin_amdgcn_s_setprio(3); }
              if (c + 2 < 8 && wid >= DSPLIT) DMA_ALL(c + 2);
              if (c > 0) M2(aq, wq, 1);
              RX(xa, c, 0, 0); RX(xb, c, 0, 1); RW(wq, c, 0, 0); SBAR();
              if (c > 0) M2(ak, wk, 1);
              RW(wk, c, 0, 1); RW(wv, c, 0, 2); SBAR();
              if (c > 0) MV(1);
              RX(xa, c, 1, 0); RX(xb, c, 1, 1); RW(wq, c, 1, 0); SBAR();
              M2(aq, wq, 0);
              RW(wk, c, 1, 1); RW(wv, c, 1, 2); SBAR();
              M2(ak, wk, 0);
              MV(0);
              if (c + 2 < 8 && wid < DSPLIT) DMA_ALL(c + 2);
          }
          M2(aq, wq, 1); M2(ak, wk, 1); MV(1);
#undef RX
#undef RW
#undef M2
#undef MV
#undef DMA_ALL
#undef DMA_X
        }
        STAMP(1);
        LANES();
        constexpr float QS = 0.25f * 1.4426950408889634f;
        const bf16x8 qf = __builtin_bit_cast(bf16x8, join16(aq[0], aq[1], QS, QS));
        const unsigned kvg = WS_KV + (unsigned)(hd * 8 + t) * 16384u, og = WS_O + (unsigned)((8 * t + wid) * 32 + hd) * 1024u;
        { const u32x4 kk = join16(ak[0], ak[1], 1.f, 1.f), vv = join16(av[0], av[1], 1.f, 1.f);
          const int ok = wid * 1024 + lane * 16, ov = wid * 1024 + (g4 & 1) * 512 + hh * 256 + r16 * 16;
          *(LAS u32x4*)(lds + ok) = kk; *(LAS u32x4*)(lds + L_V + ov) = vv;
          asm volatile("s_waitcnt lgkmcnt(0)" ::: "memory");
          if (lane == 0) __hip_atomic_fetch_add(sy + SY_AW, 1u, RLX_WG);
          st1(kk, rs, ok, kvg); st1(vv, rs, ov + 8192, kvg); }
        lds_wait_ge<1>(sy + SY_AW, 8);
        STAMP(2);
        drain();
        STAMP(22);
        if (lds_arrive(sy + SY_KV, lane) == 7 && lane == 0) __hip_atomic_store((GAS u64*)(ws + flag_at(0, hd, t)), tag, RLX_AGENT);
        const int tc = XORD ? ((t ^ wid) & 4) | ((t + wid) & 3) : (t + wid) & 7;
        const unsigned rkv = WS_KV + (unsigned)(hd * 8 + tc) * 16384u;
#define FETCH(NB) do { poll_tags(ws, flag_at(0, hd, tc), lane == 0, tag); STAMP(23); \
            _Pragma("unroll") for (int b_ = 0; b_ < 16 / (NB); ++b_) { u32x4 stg[NB]; \
                _Pragma("unroll") for (int i = 0; i < (NB); ++i) stg[i] = ld1(rs, lane * 16 + (b_ * (NB) + i) * 1024, rkv); \
                if (b_ == 0 && wid > 2) lds_wait_ge(sy + SY_FA, 4);              \
                _Pragma("unroll") for (int i = 0; i < (NB); ++i) *(LAS u32x4*)(lds + ((b_ * (NB) + i) >> 3) * L_V + wid * 8192 + ((b_ * (NB) + i) & 7) * 1024 + lane * 16) = stg[i]; } \
            asm volatile("s_waitcnt lgkmcnt(0)" ::: "memory"); \
            if (lane == 0) __hip_atomic_store(sy + SY_READY + wid, 1u, RLX_WG); } while (0)
        if (wid > 0 && wid < EARLYW) FETCH(16);
        const int idx = 0xDDDD;
        const LAS char* kp = lds + lane * 16;
        const bool vl = r32 < 16;
        const LAS char* vp = lds + (vl ? L_V + 512 + hh * 256 + r32 * 16 : (r32 & 11) == 0 ? L_MISC : L_MISC + 496);
        const int vstep = vl ? 2048 : 0;
        f32x16 o = (f32x16){}, negm, sA, sB = (f32x16){}; float gate = 0.f;
        bf16x8 kf = ldsr(kp), v0 = ldsr(vl ? vp - 512 : vp), v1 = (bf16x8){}, w0 = (bf16x8){};
        { const f32x16 s0 = MFMA(kf, qf, (f32x16){}); const float m0 = -xhalf_max(rowmax16(s0));
#pragma unroll
          for (int i = 0; i < 16; ++i) negm[i] = m0; }
        sA = MFMA(kf, qf, negm);
        kf = ldsr(kp + 1024);
        STAMP(10);
        __builtin_amdgcn_s_setprio(0);
#define EX(S, a, b) do { _Pragma("unroll") for (int i_ = (a); i_ < (b); ++i_) S[i_] = __builtin_amdgcn_exp2f(S[i_]); PIN(S); SBAR(); } while (0)
#define LO8(S) __builtin_bit_cast(bf16x16, __builtin_shufflevector(S, S, 0, 1, 2, 3, 4, 5, 6, 7))
#define HI8(S) __builtin_bit_cast(bf16x16, __builtin_shufflevector(S, S, 8, 9, 10, 11, 12, 13, 14, 15))
#define SM(a, b) do { o = __builtin_amdgcn_smfmac_f32_32x32x32_bf16(a, b, o, idx, 0, 0); PIN(o); SBAR(); } while (0)
#define TILE(CUR, OTH, KO, VO, VC, VN) do { SBAR(); EX(CUR, 0, 2); SM(v1, HI8(OTH)); v1 = ldsr(vp + (VO)); SBAR(); EX(CUR, 2, 6); \
            OTH = MFMA(kf, qf, negm); PIN(OTH); SBAR(); kf = ldsr(kp + (KO)); SBAR(); EX(CUR, 6, 10); SM(v0, LO8(CUR)); v0 = ldsr(vp + (VO) + 512); SBAR(); EX(CUR, 10, 16); } while (0)
#define LEAPFROG(pos) do { if (lane == 0) __hip_atomic_store(sy + SY_PROG + wid, (unsigned)(pos), RLX_WG); \
            if ((unsigned)__builtin_amdgcn_readfirstlane(__hip_atomic_load(sy + SY_PROG + (wid ^ 4), RLX_WG)) >= (unsigned)(pos)) __builtin_amdgcn_s_setprio(1); else __builtin_amdgcn_s_setprio(0); } while (0)
#define PAIR(KO) do { TILE(sA, sB, (KO) + 2048, 0, v0, w0); TILE(sB, sA, (KO) + 3072, 1024, w0, v0); vp += vstep; } while (0)
        PAIR(0);
        if (LATEPOS == 2 && wid >= EARLYW) { __builtin_amdgcn_s_setprio(3); FETCH(8); __builtin_amdgcn_s_setprio(0); }
        PAIR(2048);
        if (LATEPOS == 1 && wid >= EARLYW) { __builtin_amdgcn_s_setprio(3); FETCH(8); __builtin_amdgcn_s_setprio(0); }
        PAIR(4096);
        if (LATEPOS == 3 && wid >= EARLYW) { __builtin_amdgcn_s_setprio(3); FETCH(8); __builtin_amdgcn_s_setprio(0); }
        lds_wait_ge(sy + SY_READY + 1, 1);
        PAIR(6144);
        if (LATEPOS == 0 && wid >= EARLYW) { __builtin_amdgcn_s_setprio(3); FETCH(8); __builtin_amdgcn_s_setprio(YP); }
#undef FETCH
        STAMP(3);
        for (int j = 1; j < 8; ++j) {
            kp += 8192;
            if (LEAP) LEAPFROG(2 * j);
            PAIR(0); PAIR(2048);
            if (LEAP == 2) LEAPFROG(2 * j + 1);
            PAIR(4096);
            if (j < 7) lds_wait_ge(sy + SY_READY + j + 1, 1);
            PAIR(6144);
            if (j == 4 && lane == 0) __hip_atomic_fetch_add(sy + SY_PASS, 1u, RLX_WG);
            if (j == TRIGJ && lane == 0) __hip_atomic_fetch_add(sy + SY_TRIG, 1u, RLX_WG);
            if (j == 6) { lds_wait_ge(sy + SY_FSEEN, 4); gate = *(const LAS float*)(lds + g_at(32 * wid + r32)); }
        }
        SM(v1, HI8(sB));
#undef PAIR
#undef LEAPFROG
#undef TILE
#undef SM
#undef EX
        asm volatile("s_waitcnt lgkmcnt(0)" ::: "memory");
        if (lane == 0) __hip_atomic_fetch_add(sy + SY_DONE, 1u, RLX_WG);
        STAMP(4);
        { float chk = o[8] * 0.f;
#pragma unroll
          for (int i = 0; i < 8; ++i) chk = __builtin_fmaf(o[i], 0.f, chk);
          if (__builtin_expect(0 != __builtin_amdgcn_ballot_w64(!(chk == 0.f) || o[8] > 1.8e19f), 0)) {
              float m = -__builtin_inff();
#pragma nounroll
              for (int tl = 0; tl < 64; ++tl) { const bf16x8 kg = __builtin_bit_cast(bf16x8, ld1(rs, lane * 16, WS_KV + (unsigned)(hd * 8 + (tl >> 3)) * 16384u + (tl & 7) * 1024));
                  m = __builtin_fmaxf(m, rowmax16(MFMA(kg, qf, (f32x16){}))); }
              m = -xhalf_max(m);
#pragma unroll
              for (int i = 0; i < 16; ++i) negm[i] = m;
              o = (f32x16){};
              const u32x4 ones = {0x3F803F80u, 0x3F803F80u, 0x3F803F80u, 0x3F803F80u};
#pragma nounroll
              for (int tl = 0; tl < 64; ++tl) { const unsigned so = WS_KV + (unsigned)(hd * 8 + (tl >> 3)) * 16384u + (tl & 7) * 1024;
                  const bf16x8 kg = __builtin_bit_cast(bf16x8, ld1(rs, lane * 16, so));
                  const u32x4 a0 = ld1(rs, 8192 + hh * 256 + r16 * 16, so), a1 = ld1(rs, 8192 + 512 + hh * 256 + r16 * 16, so);
                  o = pv(__builtin_bit_cast(bf16x8, vl ? a0 : ones), __builtin_bit_cast(bf16x8, vl ? a1 : ones), exp16(MFMA(kg, qf, negm)), o, idx); }
          } }
        { const u32x4 ov = pack8(o, 0, gate * __builtin_amdgcn_rcpf(o[8]));
          st1(unmark(ov), rs, lane * 16, og); }
        STAMP(5);
        { const int rt = wid & 1, kq = wid >> 1, kreg = (kq >> 1) * L_V + (kq & 1) * 16384;
          const unsigned ob = WS_O + (unsigned)((8 * t + 2 * rq + rt) * 32 + 8 * kq) * 1024u;
#pragma nounroll
          for (unsigned s = 0; s < SPIN_CAP; ++s) {
              if (__builtin_popcountll(__builtin_amdgcn_ballot_w64(__builtin_amdgcn_raw_buffer_load_b32(rs, (lane & 7) * 1024 + 1020, ob, 16) == SENT)) <= 8 * LATE_OK) break;
              __builtin_amdgcn_s_sleep(1); CFENCE(); }
          u32x4 of[8];
#pragma unroll
          for (int i = 0; i < 8; ++i) of[i] = ld1(rs, lane * 16, ob + i * 1024);
          lds_wait_ge<1>(sy + SY_FP + kq, 1);
          f32x16 c[2];
#pragma unroll
          for (int ct = 0; ct < 2; ++ct)
#pragma unroll
              for (int g = 0; g < 4; ++g) { const f32x4 v = *(const LAS f32x4*)(lds + kreg + 32768 + (2 * rt + ct) * 4096 + g * 1024 + lane * 16);
#pragma unroll
                  for (int i = 0; i < 4; ++i) c[ct][4 * g + i] = v[i]; }
          PIN(c[0]); PIN(c[1]);
#pragma nounroll
          for (unsigned s = 0; s < SPIN_CAP; ++s) {
              unsigned miss = 0;
#pragma unroll
              for (int i = 0; i < 8; ++i) if (marked(of[i])) miss |= 1u << i;
              if (miss == 0) break;
              CFENCE();
#pragma unroll
              for (int i = 0; i < 8; ++i) if ((miss >> i) & 1) of[i] = ld1(rs, lane * 16, ob + i * 1024);
          }
          STAMP(11);
#pragma unroll
          for (int n = 0; n < 8; ++n) { const LAS char* p = lds + kreg + n * 2048 + lane * 16;
              c[0] = MFMA(ldsr(p), __builtin_bit_cast(bf16x8, of[n]), c[0]); c[1] = MFMA(ldsr(p + 1024), __builtin_bit_cast(bf16x8, of[n]), c[1]); }
          f32x8 own;
#pragma unroll
          for (int pc = 0; pc < 4; ++pc) {
              if (pc == kq) {
#pragma unroll
                  for (int i = 0; i < 8; ++i) own[i] = c[pc >> 1][8 * (pc & 1) + i];
              } else {
#pragma unroll
                  for (int g = 0; g < 2; ++g) { const int r = 8 * (pc & 1) + 4 * g; *(LAS f32x4*)(lds + kreg + 32768 + rt * 8192 + pc * 2048 + g * 1024 + lane * 16) = (f32x4){c[pc >> 1][r], c[pc >> 1][r + 1], c[pc >> 1][r + 2], c[pc >> 1][r + 3]}; }
              } }
          asm volatile("s_waitcnt lgkmcnt(0)" ::: "memory");
          if (lane == 0) __hip_atomic_fetch_add(sy + SY_RED + rt, 1u, RLX_WG);
          lds_wait_ge<0>(sy + SY_RED + rt, 4);
          STAMP(21);
#pragma unroll
          for (int k = 1; k < 4; ++k) { const int w = (kq + k) & 3;
#pragma unroll
              for (int g = 0; g < 2; ++g) { const f32x4 v = *(const LAS f32x4*)(lds + (w >> 1) * L_V + (w & 1) * 16384 + 32768 + rt * 8192 + kq * 2048 + g * 1024 + lane * 16);
#pragma unroll
                  for (int i = 0; i < 4; ++i) own[4 * g + i] += v[i]; } }
          auto a = __builtin_amdgcn_permlane32_swap(pack2(own[0], own[1]), pack2(own[4], own[5]), false, false), b = __builtin_amdgcn_permlane32_swap(pack2(own[2], own[3]), pack2(own[6], own[7]), false, false);
          *(u32x4*)(out + (size_t)(256 * t + 64 * rq + 32 * rt + r32) * DM + 64 * cp + 16 * kq + 8 * hh) = (u32x4){a[0], b[0], a[1], b[1]};
          if (bx == 0 && wid == 0 && lane == 0) __hip_atomic_store((GAS u64*)(ws + WS_FLAGS), tag, RLX_AGENT);
        }
        STAMP(9); STAMP_FLUSH();
    } else {
        const int f = wid - 8, i32 = rq + 4 * t;
        { LANES(); const int xsw = g4 ^ (r16 >> 1);
          EPOCH_LOAD();
          if (f == 1 && lane < 32) { if (hasF) glds4(lane * 4, (const char*)b1 + fb * 128, lds0 + L_B1); glds4(lane * 4, (const char*)b2 + cp * 128, lds0 + L_B2); }
          if (hd == 31) { glds16(lane * 16, gate_w, lds0 + L_GATE + f * 1024); asm volatile("s_waitcnt vmcnt(0)" ::: "memory"); }
          const int xo = (64 * f + r16) * 128, fo = XB + 6144 + (4 * g4 + q4) * 128 + p4 * 8, fsw = (2 * g4 + (q4 >> 1)) & 3;
          bf16x8 xf[2][4], wf[2][4];
#define RX(c, ks, rt) xf[ks][rt] = ldsr(lds + BUFOF(c) + xo + (rt) * 2048 + ((xsw ^ (4 * (ks))) * 16))
#define RW(c, ks, cb) wf[ks][cb] = ldtr(lds + BUFOF(c) + fo + (ks) * 4096 + (((cb) ^ fsw) * 32), lds + BUFOF(c) + fo + (ks) * 4096 + 2048 + (((cb) ^ fsw) * 32))
#define M2(ks, cb, r0) do { af[cb][r0] = MFMA16(wf[ks][cb], xf[ks][r0], af[cb][r0]); af[cb][r0 + 1] = MFMA16(wf[ks][cb], xf[ks][r0 + 1], af[cb][r0 + 1]); PIN(af[cb][r0]); PIN(af[cb][r0 + 1]); SBAR(); } while (0)
#define F_SYNC(c) do { if ((c) != RTC && (c) != 0) asm volatile("s_waitcnt lgkmcnt(0)\n\ts_barrier" ::: "memory"); else asm volatile("s_waitcnt vmcnt(0) lgkmcnt(0)\n\ts_barrier" ::: "memory"); \
              if ((c) == 0 || (c) == 7) STAMP(12 + (c)); \
              if ((c) == RTC) { EPOCH_TAKE(); LANES();     \
                  const int u = i32 + 32 * f; const char* src = u < 112 ? (const char*)w2 + (size_t)u * 16384 : (const char*)wo + (size_t)(u - 112) * 16384; \
                  const unsigned vo = (lane >> 3) * 1024 + cp * 128 + (lane & 7) * 16; \
                  glds16(vo, src, lds0 + L_RT + f * 2048); glds16(vo, src + 8192, lds0 + L_RT + f * 2048 + 1024); \
                  if (f == 0 && i32 < 16) { const char* s2 = (const char*)wo + (size_t)(i32 + 16) * 16384; glds16(vo, s2, lds0 + L_RT + 8192); glds16(vo, s2 + 8192, lds0 + L_RT + 8192 + 1024); } } } while (0)
#define F_LEAVE() STAMP(1); LANES(); \
              asm volatile("s_waitcnt lgkmcnt(0)" ::: "memory"); \
              if (lane == 0) __hip_atomic_fetch_add(sy + SY_FA, 1u, RLX_WG); \
              __builtin_amdgcn_s_setprio(2);
#define F_RETILE() do { \
              _Pragma("unroll") for (int i = 0; i < 2; ++i)     \
                  st1((u32x4){SENT, SENT, SENT, SENT}, rs, lane * 16, WS_O + (unsigned)((8 * t + 2 * f + i) * 32 + hd) * 1024u); \
              for (int n = f; n < ((f == 0 && i32 < 16) ? 5 : 4); n += 4) {        \
                  const int u = i32 + 32 * n, r0 = (u < 112 ? 8 : 4) * hh + q4, r1 = u < 112 ? 512 : 1024; \
                  _Pragma("unroll") for (int ct = 0; ct < 2; ++ct) { const LAS char* s = lds + L_RT + n * 2048 + r0 * 128 + (g4 & 1) * 32 + p4 * 8 + ct * 64; \
                      st1(__builtin_bit_cast(u32x4, ldtr(s, s + r1)), rs, lane * 16, WS_W + (unsigned)((2 * cp + ct) * 144 + u) * 1024u); } } } while (0)
          if (hasF) {
              f32x4 af[4][4];
#pragma unroll
              for (int c = 0; c < 8; ++c) {
                  F_SYNC(c);
                  if (c == 0) {
#pragma unroll
                      for (int cb = 0; cb < 4; ++cb) { const u32x2 w = *(const LAS u32x2*)(lds + L_B1 + cb * 32 + g4 * 8);
#pragma unroll
                          for (int rt = 0; rt < 4; ++rt) af[cb][rt] = (f32x4){U2F(w[0] << 16), U2F(w[0] & 0xffff0000u), U2F(w[1] << 16), U2F(w[1] & 0xffff0000u)}; }
                  }
                  if (FDEFER && c == 7) lds_wait_ge<2>(sy + SY_AW, 8);
                  if (c > 0) M2(1, 0, 0);
                  RX(c, 0, 0); SBAR();
                  if (c > 0) M2(1, 0, 2);
                  RW(c, 0, 0); SBAR();
                  if (c > 0) M2(1, 1, 0);
                  RX(c, 0, 1); SBAR();
                  if (c > 0) M2(1, 1, 2);
                  RX(c, 0, 2); SBAR();
                  if (c > 0) M2(1, 2, 0);
                  RX(c, 0, 3); SBAR();
                  if (c > 0) M2(1, 2, 2);
                  RW(c, 0, 1); SBAR();
                  if (c > 0) M2(1, 3, 0);
                  RW(c, 0, 2); SBAR();
                  if (c > 0) M2(1, 3, 2);
                  RW(c, 0, 3); SBAR();
                  M2(0, 0, 0); RX(c, 1, 0); RW(c, 1, 0); SBAR();
                  M2(0, 0, 2); RX(c, 1, 1); RX(c, 1, 2); SBAR();
                  M2(0, 1, 0); RX(c, 1, 3); RW(c, 1, 1); SBAR();
                  M2(0, 1, 2); RW(c, 1, 2); RW(c, 1, 3); SBAR();
                  M2(0, 2, 0); M2(0, 2, 2); M2(0, 3, 0); M2(0, 3, 2);
              }
              M2(1, 0, 0); M2(1, 0, 2); M2(1, 1, 0); M2(1, 1, 2); M2(1, 2, 0); M2(1, 2, 2); M2(1, 3, 0); M2(1, 3, 2);
              { F_LEAVE();
                u32x4 hp[4][2];
#pragma unroll
                for (int cb = 0; cb < 4; ++cb) {
#pragma unroll
                    for (int rp = 0; rp < 2; ++rp) {
                        float v[8], e[8];
#pragma unroll
                        for (int i = 0; i < 8; ++i) { v[i] = af[cb][2 * rp + (i >> 2)][i & 3];
                            e[i] = v[i] * __builtin_fmaf(v[i] * v[i], -2.f * 0.7978845608028654f * 1.4426950408889634f * 0.044715f, -2.f * 0.7978845608028654f * 1.4426950408889634f); }
                        SBAR();
#pragma unroll
                        for (int i = 0; i < 8; ++i) e[i] = __builtin_amdgcn_exp2f(e[i]);
                        SBAR();
#pragma unroll
                        for (int i = 0; i < 8; ++i) e[i] = 1.f + e[i];
                        SBAR();
#pragma unroll
                        for (int i = 0; i < 8; ++i) e[i] = __builtin_amdgcn_rcpf(e[i]);
                        SBAR();
                        hp[cb][rp] = join16((f32x4){v[0] * e[0], v[1] * e[1], v[2] * e[2], v[3] * e[3]}, (f32x4){v[4] * e[4], v[5] * e[5], v[6] * e[6], v[7] * e[7]}, 1.f, 1.f);
                        if (!HLATE) st1(hp[cb][rp], rs, lane * 16, WS_H + (unsigned)((8 * t + 2 * f + rp) * 112 + 4 * fb + cb) * 1024u);
                        PIN(hp[cb][rp]); SBAR();
                    }
                }
                STAMP(2);
                if (HLATE) { drain();
#pragma unroll
                    for (int i = 0; i < 8; ++i) st1(hp[i >> 1][i & 1], rs, lane * 16, WS_H + (unsigned)((8 * t + 2 * f + (i & 1)) * 112 + 4 * fb + (i >> 1)) * 1024u);
                } else asm volatile("s_waitcnt vmcnt(8)" ::: "memory");
                F_RETILE(); }
          } else {
              f32x4 az[4] = {};
#pragma unroll
              for (int c = 0; c < 8; ++c) {
                  F_SYNC(c);
                  if (hd == 31) {
#pragma unroll
                      for (int ks = 0; ks < 2; ++ks) { const bf16x8 gf = ldsr(lds + L_GATE + f * 1024 + CS(c) * 128 + ks * 64 + g4 * 16);
#pragma unroll
                          for (int rt = 0; rt < 4; ++rt) { RX(c, ks, rt); az[rt] = MFMA16(gf, xf[ks][rt], az[rt]); } }
                  }
              }
              { F_LEAVE(); drain(); F_RETILE();
                if (hd == 31) {
                    const float z = bfround(g4 == 0 ? az[0][0] : g4 == 1 ? az[1][0] : g4 == 2 ? az[2][0] : az[3][0]);
                    __hip_atomic_store((GAS float*)(ws + WS_G + (256 * t + 64 * f + lane) * 4), bfround(__builtin_amdgcn_rcpf(1.f + __builtin_amdgcn_exp2f(-1.4426950408889634f * z))), RLX_AGENT);
                } }
          }
#undef F_SYNC
#undef F_LEAVE
#undef F_RETILE
#undef RX
#undef RW
#undef M2
        }
        LANES();
        const int freg = (f >> 1) * L_V + (f & 1) * 16384;
        drain();
        if (lds_arrive(sy + SY_F, lane) == 3 && lane == 0) __hip_atomic_store((GAS u64*)(ws + flag_at(1, hd, t)), tag, RLX_AGENT);
        STAMP(3);
        f32x16 acc[4] = {};
        poll_tags<4>(ws, hh ? flag_at(1, cp + 8 * (r32 & 3), r32 >> 2) : flag_at(1, r32, t), true, tag);
        *(LAS float*)(lds + g_at(64 * f + lane)) = __hip_atomic_load((GAS float*)(ws + WS_G + (256 * t + 64 * f + lane) * 4), RLX_AGENT);
        asm volatile("s_waitcnt lgkmcnt(0)" ::: "memory");
        if (lane == 0) __hip_atomic_fetch_add(sy + SY_FSEEN, 1u, RLX_WG);
        __builtin_amdgcn_s_setprio(F2_PRIO);
        STAMP(4);
        const unsigned hb = WS_H + (unsigned)((8 * t + 2 * rq) * 112 + 28 * f) * 1024u, wb = WS_W + (unsigned)(2 * cp * 144 + 28 * f) * 1024u;
        u32x4 fr[4][4];
#define LDK(slot, n) do { fr[slot][0] = ld1(rs, lane * 16, wb + (n) * 1024); fr[slot][1] = ld1(rs, lane * 16, wb + (144 + (n)) * 1024); \
                          fr[slot][2] = ld1(rs, lane * 16, hb + (n) * 1024); fr[slot][3] = ld1(rs, lane * 16, hb + (112 + (n)) * 1024); } while (0)
#define BC(v) __builtin_bit_cast(bf16x8, v)
#define MM4(w0, w1_, h0, h1) do { acc[0] = MFMA(w0, h0, acc[0]); acc[1] = MFMA(w0, h1, acc[1]); acc[2] = MFMA(w1_, h0, acc[2]); acc[3] = MFMA(w1_, h1, acc[3]); PIN(acc[0]); PIN(acc[1]); PIN(acc[2]); PIN(acc[3]); SBAR(); } while (0)
#define FFN2(n0, n1, pace) do { _Pragma("unroll") for (int n = (n0); n < (n1); ++n) { MM4(BC(fr[n & 3][0]), BC(fr[n & 3][1]), BC(fr[n & 3][2]), BC(fr[n & 3][3])); if (n + 4 < (n1)) LDK(n & 3, n + 4); if (pace) __builtin_amdgcn_s_sleep(pace); SBAR(); } } while (0)
#pragma unroll
        for (int n = 0; n < 4; ++n) LDK(n, n);
        FFN2(0, NP1, F2_PACE);
        STAMP(5);
        { u32x4 tmp[16];
#pragma unroll
          for (int i = 0; i < 16; ++i) tmp[i] = ld1(rs, lane * 16, WS_W + (unsigned)((2 * cp + (i & 1)) * 144 + 112 + 8 * f + (i >> 1)) * 1024u);
          __builtin_amdgcn_s_setprio(2);
          lds_wait_ge<16>(sy + SY_PASS, 8);
#pragma unroll
          for (int i = 0; i < 16; ++i) *(LAS u32x4*)(lds + freg + i * 1024 + lane * 16) = tmp[i]; }
        STAMP(6);
        if (NP1 < 28) {
#pragma unroll
            for (int n = NP1; n < NP1 + 4; ++n) LDK(n & 3, n);
            lds_wait_ge<8>(sy + SY_TRIG, 8);
            __builtin_amdgcn_s_setprio(F2_PRIO);
            FFN2(NP1, 28, 0);
        }
#undef LDK
#undef FFN2
        lds_wait_ge(sy + SY_FSEEN, 4);
        { const float og0 = bfround(1.f - *(const LAS float*)(lds + g_at(64 * rq + r32))), og1 = bfround(1.f - *(const LAS float*)(lds + g_at(64 * rq + 32 + r32)));
#pragma unroll
          for (int ct = 0; ct < 2; ++ct)
#pragma unroll
              for (int g = 0; g < 4; ++g) { u32x2 w = *(const LAS u32x2*)(lds + L_B2 + ct * 64 + g * 16 + hh * 8); if (f != 0) w = (u32x2){0u, 0u};
                  const float bb[4] = {U2F(w[0] << 16), U2F(w[0] & 0xffff0000u), U2F(w[1] << 16), U2F(w[1] & 0xffff0000u)};
#pragma unroll
                  for (int i = 0; i < 4; ++i) { acc[2 * ct][4 * g + i] = (acc[2 * ct][4 * g + i] + bb[i]) * og0; acc[2 * ct + 1][4 * g + i] = (acc[2 * ct + 1][4 * g + i] + bb[i]) * og1; } } }
#undef MM4
#undef BC
        STAMP(7);
        lds_wait_ge<1>(sy + SY_DONE, 8);
#pragma unroll
        for (int qd = 0; qd < 4; ++qd)
#pragma unroll
            for (int g = 0; g < 4; ++g) *(LAS f32x4*)(lds + freg + 32768 + (2 * (qd & 1) + (qd >> 1)) * 4096 + g * 1024 + lane * 16) = (f32x4){acc[qd][4 * g], acc[qd][4 * g + 1], acc[qd][4 * g + 2], acc[qd][4 * g + 3]};
        asm volatile("s_waitcnt lgkmcnt(0)" ::: "memory");
        if (lane == 0) __hip_atomic_store(sy + SY_FP + f, 1u, RLX_WG);
        STAMP(9); STAMP_FLUSH();
    }
}

extern "C" void kernel_launch(void* const* d_in, const int* in_sizes, int n_in, void* d_out, int out_size, void* d_ws, size_t ws_size, hipStream_t stream) {
    (void)in_sizes; (void)n_in; (void)out_size; (void)ws_size;
    hipLaunchKernelGGL(gated_mix_fused, dim3(256), dim3(768), 0, stream, (const unsigned short*)d_in[0], (const unsigned short*)d_in[1], (const unsigned short*)d_in[2],
                       (const unsigned short*)d_in[4], (const unsigned short*)d_in[5], (const unsigned short*)d_in[6], (const unsigned short*)d_in[7], (const unsigned short*)d_in[8],
                       (unsigned short*)d_out, (char*)d_ws);
}
```

```cpp
#include <hip/hip_runtime.h>
#include <hip/hip_bf16.h>
#include <stdint.h>

typedef __attribute__((ext_vector_type(8))) __bf16 bf16x8;
typedef __attribute__((ext_vector_type(16))) __bf16 bf16x16;
typedef __attribute__((ext_vector_type(2))) __bf16 bf16x2;
typedef __attribute__((ext_vector_type(4))) short s16x4;
typedef __attribute__((ext_vector_type(16))) float f32x16;
typedef __attribute__((ext_vector_type(8))) float f32x8;
typedef __attribute__((ext_vector_type(4))) float f32x4;
typedef __attribute__((ext_vector_type(2))) float f32x2;
typedef __attribute__((ext_vector_type(4))) unsigned u32x4;
typedef __attribute__((ext_vector_type(2))) unsigned u32x2;
typedef unsigned long long u64;
#define LAS __attribute__((address_space(3)))
#define GAS __attribute__((address_space(1)))
#if defined(__HIP_DEVICE_COMPILE__) && !defined(ALLOW_PK)
#define NOPK __attribute__((target("no-packed-fp32-ops")))
#else
#define NOPK
#endif
#define DI __device__ __forceinline__ NOPK
#define U2F(x) __builtin_bit_cast(float, (unsigned)(x))
#define F2U(x) __builtin_bit_cast(unsigned, (float)(x))
#define RLX_AGENT __ATOMIC_RELAXED, __HIP_MEMORY_SCOPE_AGENT
#define RLX_WG __ATOMIC_RELAXED, __HIP_MEMORY_SCOPE_WORKGROUP

constexpr int DM = 512, DFF = 1792;
constexpr unsigned WS_FLAGS = 0;
constexpr unsigned WS_FLAG0 = 4096, FLAG_STRIDE = 128;
constexpr unsigned WS_G = 106496;
constexpr unsigned WS_KV = 131072;
constexpr unsigned WS_H = WS_KV + 32u * 8u * 16384u;
constexpr unsigned WS_O = WS_H + 64u * 112u * 1024u;
constexpr unsigned WS_W = WS_O + 64u * 32u * 1024u;
constexpr unsigned WS_END = WS_W + 16u * 144u * 1024u;
constexpr unsigned WS_DIAG = WS_END;
constexpr int XB = 32768, BUFB = XB + 6144 + 8192;
constexpr int L_V = 65536;
constexpr int L_GATE = 3 * BUFB;
constexpr int L_RT = L_GATE + 4096;
constexpr int L_MISC = 163840 - 2048;
constexpr int L_DUMMY = L_RT + 10240;
constexpr int L_B1 = L_MISC + 272, L_B2 = L_MISC + 512 + 272, L_SYNC = L_MISC + 1024 + 272;
static_assert(L_DUMMY + 1024 <= L_MISC && L_V + 3 * 8192 <= 2 * BUFB && 3 * 8192 <= BUFB, "lds");
enum { SY_READY = 0  , SY_KV = 8, SY_DONE = 9, SY_F = 10, SY_PASS = 11, SY_TRIG = 13, SY_FSEEN = 14, SY_AW = 15, SY_FA = 16, SY_RED = 18  , SY_FP = 20  , SY_PROG = 24  , SY_N = 32 };
constexpr unsigned SENT = 0xFFFFFFFFu;
constexpr unsigned SPIN_CAP = 1u << 18;

#define MFMA(a, b, c) __builtin_amdgcn_mfma_f32_32x32x16_bf16((a), (b), (c), 0, 0, 0)
#define MFMA16(a, b, c) __builtin_amdgcn_mfma_f32_16x16x32_bf16((a), (b), (c), 0, 0, 0)
#define SBAR() __builtin_amdgcn_sched_barrier(0)
#define CFENCE() asm volatile("" ::: "memory")
#define PIN(x) asm volatile("" : "+v"(x))

#define F2_PRIO 0
#define DSPLIT 4
#define ROTX (2 * ((hd + t) & 3))
#define EARLYW 4
#define F2_PACE 0
#define FDEFER 1
#define RTC 7
#define HLATE 0
#define SLP1 20
#define LATE_OK 2
#define XORD 1
#define LATEPOS 0
#define YP 3
#define SCHED 0
#define CPF 1
#define LEAP 0
#define NP1 24
#define TRIGJ 6
#define STAMP(i) do {} while (0)
#define STAMP_FLUSH() do {} while (0)

DI unsigned pack2(float lo, float hi) { f32x2 v = {lo, hi}; return __builtin_bit_cast(unsigned, __builtin_convertvector(v, bf16x2)); }
DI u32x4 pack8(const f32x16& a, int o, float s) {
    u32x4 r; r[0] = pack2(a[o] * s, a[o + 1] * s); r[1] = pack2(a[o + 2] * s, a[o + 3] * s); r[2] = pack2(a[o + 4] * s, a[o + 5] * s); r[3] = pack2(a[o + 6] * s, a[o + 7] * s); return r; }
DI u32x4 pack8(const f32x16& a, int o) {
    u32x4 r; r[0] = pack2(a[o], a[o + 1]); r[1] = pack2(a[o + 2], a[o + 3]); r[2] = pack2(a[o + 4], a[o + 5]); r[3] = pack2(a[o + 6], a[o + 7]); return r; }
DI float bfround(float v) { return (float)(__bf16)v; }
DI float bf2f(unsigned short u) { return U2F((unsigned)u << 16); }
DI bf16x8 ldsr(const LAS char* p) { return *(const LAS bf16x8*)p; }
DI bf16x8 ldtr(const LAS char* p0, const LAS char* p1) {
    s16x4 a = __builtin_amdgcn_ds_read_tr16_b64_v4i16((LAS s16x4*)p0), b = __builtin_amdgcn_ds_read_tr16_b64_v4i16((LAS s16x4*)p1);
    return __builtin_bit_cast(bf16x8, __builtin_shufflevector(a, b, 0, 1, 2, 3, 4, 5, 6, 7)); }
DI void glds16(unsigned voff, const void* sbase, unsigned lds) {
    unsigned keep; asm volatile("s_mov_b32 %0, m0\n\ts_mov_b32 m0, %3\n\ts_nop 0\n\tglobal_load_lds_dwordx4 %1, %2\n\ts_mov_b32 m0, %0" : "=&s"(keep) : "v"(voff), "s"(sbase), "s"(lds) : "memory"); }
DI void glds4(unsigned voff, const void* sbase, unsigned lds) {
    unsigned keep; asm volatile("s_mov_b32 %0, m0\n\ts_mov_b32 m0, %3\n\ts_nop 0\n\tglobal_load_lds_dword %1, %2\n\ts_mov_b32 m0, %0" : "=&s"(keep) : "v"(voff), "s"(sbase), "s"(lds) : "memory"); }
DI u32x4 ld1(__amdgpu_buffer_rsrc_t rs, unsigned voff, unsigned soff) { return __builtin_amdgcn_raw_buffer_load_b128(rs, voff, soff, 16); }
DI void st1(u32x4 v, __amdgpu_buffer_rsrc_t rs, unsigned voff, unsigned soff) {
    __builtin_amdgcn_raw_buffer_store_b128(v, rs, voff, soff, 16); asm volatile("s_nop 1" :: "v"(v) : "memory"); }
DI u32x4 unmark(u32x4 v) {
#pragma unroll
    for (int i = 0; i < 4; ++i) v[i] = v[i] == SENT ? 0x7FFF7FFFu : v[i];
    return v; }
DI bool marked(const u32x4& d) { return __builtin_amdgcn_ballot_w64(__builtin_elementwise_max(__builtin_elementwise_max(d[0], d[1]), __builtin_elementwise_max(d[2], d[3])) == SENT) != 0; }
DI void drain() { asm volatile("s_waitcnt vmcnt(0)" ::: "memory"); }
DI unsigned lds_arrive(LAS unsigned* w, int lane) {
    unsigned old = 0; if (lane == 0) old = __hip_atomic_fetch_add(w, 1u, RLX_WG); return __builtin_amdgcn_readfirstlane(old); }
template <int SL = 1> DI void lds_wait_ge(LAS unsigned* w, unsigned want) {
    unsigned tv, ts, n;
    asm volatile("s_mov_b32 %2, 0x400000\n"
                 "1:\n\tds_read_b32 %0, %3\n\ts_waitcnt lgkmcnt(0)\n\tv_readfirstlane_b32 %1, %0\n\ts_nop 1\n\ts_cmp_ge_u32 %1, %4\n\ts_cbranch_scc1 2f\n\t"
                 "s_sleep %5\n\ts_sub_u32 %2, %2, 1\n\ts_cmp_lg_u32 %2, 0\n\ts_cbranch_scc1 1b\n"
                 "2:" : "=&v"(tv), "=&s"(ts), "=&s"(n) : "v"((unsigned)(uintptr_t)w), "s"(want), "i"(SL) : "memory", "scc"); }
template <int SL = 1> DI void poll_tags(const char* ws, unsigned flag_off, bool active, u64 tag) {
#pragma nounroll
    for (unsigned s = 0; s < SPIN_CAP; ++s) {
        u64 v = tag; if (active) v = __hip_atomic_load((GAS u64*)(ws + flag_off), RLX_AGENT);
        if (__builtin_amdgcn_ballot_w64(v != tag) == 0) break; __builtin_amdgcn_s_sleep(SL); }
    __builtin_amdgcn_fence(__ATOMIC_ACQUIRE, "wavefront"); CFENCE(); }
DI unsigned flag_at(int kind, int hd, int t) { return WS_FLAG0 + (unsigned)(kind * 256 + hd * 8 + t) * FLAG_STRIDE; }
DI int g_at(int i) { return L_MISC + (i >> 6) * 512 + 16 + (i & 63) * 4; }
DI u32x4 join16(const f32x4& a, const f32x4& b, float sa, float sb) {
    auto r0 = __builtin_amdgcn_permlane16_swap(pack2(a[0] * sa, a[1] * sa), pack2(b[0] * sb, b[1] * sb), false, false);
    auto r1 = __builtin_amdgcn_permlane16_swap(pack2(a[2] * sa, a[3] * sa), pack2(b[2] * sb, b[3] * sb), false, false);
    return (u32x4){r0[0], r1[0], r0[1], r1[1]}; }
DI f32x16 exp16(f32x16 s) {
#pragma unroll
    for (int i = 0; i < 16; ++i) s[i] = __builtin_amdgcn_exp2f(s[i]);
    return s; }
DI f32x16 pv(bf16x8 v0, bf16x8 v1, const f32x16& p, f32x16 o, int idx) {
    const f32x8 lo = __builtin_shufflevector(p, p, 0, 1, 2, 3, 4, 5, 6, 7), hi = __builtin_shufflevector(p, p, 8, 9, 10, 11, 12, 13, 14, 15);
    o = __builtin_amdgcn_smfmac_f32_32x32x32_bf16(v0, __builtin_bit_cast(bf16x16, lo), o, idx, 0, 0);
    o = __builtin_amdgcn_smfmac_f32_32x32x32_bf16(v1, __builtin_bit_cast(bf16x16, hi), o, idx, 0, 0);
    return o; }
DI float rowmax16(const f32x16& s) {
    float m = __builtin_fmaxf(__builtin_fmaxf(s[0], s[1]), s[2]);
#pragma unroll
    for (int i = 3; i < 15; i += 2) m = __builtin_fmaxf(__builtin_fmaxf(m, s[i]), s[i + 1]);
    return __builtin_fmaxf(m, s[15]); }
DI float xhalf_max(float m) { auto r = __builtin_amdgcn_permlane32_swap(F2U(m), F2U(m), false, false); return __builtin_fmaxf(U2F(r[0]), U2F(r[1])); }

__global__ __launch_bounds__(768, 3) NOPK void gated_mix_fused(const unsigned short* __restrict__ x, const unsigned short* __restrict__ wqkv, const unsigned short* __restrict__ wo,
                                                         const unsigned short* __restrict__ w1, const unsigned short* __restrict__ b1, const unsigned short* __restrict__ w2,
                                                         const unsigned short* __restrict__ b2, const unsigned short* __restrict__ gate_w, unsigned short* __restrict__ out, char* ws) {
    __shared__ __attribute__((aligned(1024))) char smem[163840];
    LAS char* lds = (LAS char*)smem;
    const unsigned lds0 = (unsigned)(uintptr_t)lds;
    const int wid = __builtin_amdgcn_readfirstlane(__builtin_amdgcn_workitem_id_x() >> 6);
#define LANES() int lane = __builtin_amdgcn_mbcnt_hi(~0u, __builtin_amdgcn_mbcnt_lo(~0u, 0u)); PIN(lane); const int r32 = lane & 31, hh = lane >> 5, g4 = lane >> 4, r16 = lane & 15, q4 = r16 >> 2, p4 = lane & 3; (void)r32; (void)hh; (void)g4; (void)q4; (void)p4
    const int bx = __builtin_amdgcn_workgroup_id_x(), hd = 8 * (bx & 3) + ((bx >> 3) & 7), t = 4 * ((bx >> 2) & 1) + (bx >> 6);
    const __amdgpu_buffer_rsrc_t rs = __builtin_amdgcn_make_buffer_rsrc(ws, 0, WS_END, 0x00020000);
    LAS unsigned* sy = (LAS unsigned*)(lds + L_SYNC);
    asm volatile("" :: "s"(x), "s"(wqkv), "s"(wo), "s"(w1), "s"(b1), "s"(w2), "s"(b2), "s"(gate_w), "s"(out), "s"(ws));
    const char* pc0; unsigned clen;
    asm volatile("s_getpc_b64 %0\n.Lgm_ref:\n\ts_mov_b32 %1, .Lgm_end-.Lgm_ref" : "=s"(pc0), "=s"(clen));
    u64 tag; u32x2 ep;
#define EPOCH_LOAD() asm volatile("global_load_dwordx2 %0, %1, %2" : "=v"(ep) : "v"(0), "s"(ws) : "memory")
#define EPOCH_TAKE() do { PIN(ep); tag = (((u64)(unsigned)__builtin_amdgcn_readfirstlane(ep[1]) << 32) | (unsigned)__builtin_amdgcn_readfirstlane(ep[0])) + 1ull; } while (0)
    const int cp = hd & 7, rq = hd >> 3;
    const bool hasF = cp != 7; const int fb = hd - rq;
    const int rot = ROTX;
    STAMP(0);
#define CS(c) (((c) + rot) & 7)
#define BUFOF(c) ((((c) + 1) % 3) * BUFB)
#define CHUNK_SYNC(c, n) do { if ((c) < 7) asm volatile("s_waitcnt vmcnt(" #n ") lgkmcnt(0)\n\ts_barrier" ::: "memory"); else asm volatile("s_waitcnt vmcnt(0) lgkmcnt(0)\n\ts_barrier" ::: "memory"); } while (0)

    if (__builtin_expect(wid < 8, 1)) {
        f32x4 aq[2] = {}, ak[2] = {}, av[2] = {};
        { LANES(); const int xsw = g4 ^ (r16 >> 1);
          const int c0 = (lane & 7) ^ (lane >> 4), rho = lane >> 1, r8 = lane >> 3;
          const unsigned dvo0 = r8 * 1024 + c0 * 16, dvo1 = r8 * 1024 + (c0 ^ 4) * 16;
          const unsigned dvq = (8 * ((rho >> 2) & 3) + 4 * (rho >> 4) + (rho & 3)) * 3072 + (lane & 1) * 16;
          const unsigned dvf = (8 * (r8 >> 2) + (r8 & 3)) * 3584 + (((lane & 7) ^ (r8 & 6)) * 16);
          const char* xbase = (const char*)x + (size_t)(256 * t + 32 * wid) * 1024;
          const bool hq = wid < 6, hf = hasF;
          const char* qbase = (const char*)wqkv + hd * 32 + (hq ? (wid >> 1) * 1024 + (wid & 1) * 32 * 3072 : 0);
          const char* fbase = (const char*)w1 + (hf ? fb * 128 : 0) + (32 * (wid >> 2) + 16 * (wid & 1) + 4 * ((wid >> 1) & 1)) * 3584;
          const unsigned vq_ = hq ? dvq : 0u, vf_ = hf ? dvf : 0u;
#define DMA_X(c) do { _Pragma("unroll") for (int i = 0; i < 4; ++i) glds16((i & 1) ? dvo1 : dvo0, xbase + CS(c) * 128 + i * 8192, lds0 + BUFOF(c) + wid * 4096 + i * 1024); } while (0)
#define DMA_ALL(c) do { glds16(vq_, qbase + CS(c) * 64 * 3072, hq ? lds0 + BUFOF(c) + XB + wid * 1024 : lds0 + L_DUMMY); \
              glds16(vf_, fbase + CS(c) * 64 * 3584, hf ? lds0 + BUFOF(c) + XB + 6144 + wid * 1024 : lds0 + L_DUMMY); DMA_X(c); } while (0)
          DMA_ALL(0);
          EPOCH_LOAD();
          if (wid == 0) {
              if (lane < SY_N) sy[lane] = 0;
              if (lane < 8) { const unsigned one = lane < 4 ? 0x3F803F80u : 0u; *(LAS u32x4*)(lds + L_MISC + (lane & 3) * 512 + (lane >> 2) * 496) = (u32x4){one, one, one, one}; }
          }
          __builtin_amdgcn_s_sleep(SLP1);
          DMA_ALL(1);
          const int xo = (32 * wid + r16) * 128, wo_ = XB + (4 * g4 + q4) * 32 + p4 * 8;
          bf16x8 xa[2], xb[2], wq[2], wk[2], wv[2];
#define RX(d, c, ks, rt) d[ks] = ldsr(lds + BUFOF(c) + xo + (rt) * 2048 + ((xsw ^ (4 * (ks))) * 16))
#define RW(d, c, ks, im) d[ks] = ldtr(lds + BUFOF(c) + wo_ + (im) * 2048 + (ks) * 1024, lds + BUFOF(c) + wo_ + (im) * 2048 + (ks) * 1024 + 512)
#define M2(A, W, ks) do { A[0] = MFMA16(W[ks], xa[ks], A[0]); A[1] = MFMA16(W[ks], xb[ks], A[1]); PIN(A[0]); PIN(A[1]); SBAR(); } while (0)
#define MV(ks) do { av[0] = MFMA16(xa[ks], wv[ks], av[0]); av[1] = MFMA16(xb[ks], wv[ks], av[1]); PIN(av[0]); PIN(av[1]); SBAR(); } while (0)
#pragma unroll
          for (int c = 0; c < 8; ++c) {
              CHUNK_SYNC(c, 6);
              if (c == 0 || c == 1 || c == 7) STAMP(12 + c);
              if (c == 7) { EPOCH_TAKE(); __builtin_amdgcn_s_setprio(3); }
              if (c + 2 < 8 && wid >= DSPLIT) DMA_ALL(c + 2);
              if (c > 0) M2(aq, wq, 1);
              RX(xa, c, 0, 0); RX(xb, c, 0, 1); RW(wq, c, 0, 0); SBAR();
              if (c > 0) M2(ak, wk, 1);
              RW(wk, c, 0, 1); RW(wv, c, 0, 2); SBAR();
              if (c > 0) MV(1);
              RX(xa, c, 1, 0); RX(xb, c, 1, 1); RW(wq, c, 1, 0); SBAR();
              M2(aq, wq, 0);
              RW(wk, c, 1, 1); RW(wv, c, 1, 2); SBAR();
              M2(ak, wk, 0);
              MV(0);
              if (c + 2 < 8 && wid < DSPLIT) DMA_ALL(c + 2);
          }
          M2(aq, wq, 1); M2(ak, wk, 1); MV(1);
#undef RX
#undef RW
#undef M2
#undef MV
#undef DMA_ALL
#undef DMA_X
        }
        STAMP(1);
        LANES();
        constexpr float QS = 0.25f * 1.4426950408889634f;
        const bf16x8 qf = __builtin_bit_cast(bf16x8, join16(aq[0], aq[1], QS, QS));
        const unsigned kvg = WS_KV + (unsigned)(hd * 8 + t) * 16384u, og = WS_O + (unsigned)((8 * t + wid) * 32 + hd) * 1024u;
        { const u32x4 kk = join16(ak[0], ak[1], 1.f, 1.f), vv = join16(av[0], av[1], 1.f, 1.f);
          const int ok = wid * 1024 + lane * 16, ov = wid * 1024 + (g4 & 1) * 512 + hh * 256 + r16 * 16;
          *(LAS u32x4*)(lds + ok) = kk; *(LAS u32x4*)(lds + L_V + ov) = vv;
          asm volatile("s_waitcnt lgkmcnt(0)" ::: "memory");
          if (lane == 0) __hip_atomic_fetch_add(sy + SY_AW, 1u, RLX_WG);
          st1(kk, rs, ok, kvg); st1(vv, rs, ov + 8192, kvg); }
        lds_wait_ge<1>(sy + SY_AW, 8);
        STAMP(2);
        drain();
        STAMP(22);
        if (lds_arrive(sy + SY_KV, lane) == 7 && lane == 0) __hip_atomic_store((GAS u64*)(ws + flag_at(0, hd, t)), tag, RLX_AGENT);
        const int tc = XORD ? ((t ^ wid) & 4) | ((t + wid) & 3) : (t + wid) & 7;
        const unsigned rkv = WS_KV + (unsigned)(hd * 8 + tc) * 16384u;
#define FETCH(NB) do { poll_tags(ws, flag_at(0, hd, tc), lane == 0, tag); STAMP(23); \
            _Pragma("unroll") for (int b_ = 0; b_ < 16 / (NB); ++b_) { u32x4 stg[NB]; \
                _Pragma("unroll") for (int i = 0; i < (NB); ++i) stg[i] = ld1(rs, lane * 16 + (b_ * (NB) + i) * 1024, rkv); \
                if (b_ == 0 && wid > 2) lds_wait_ge(sy + SY_FA, 4);              \
                _Pragma("unroll") for (int i = 0; i < (NB); ++i) *(LAS u32x4*)(lds + ((b_ * (NB) + i) >> 3) * L_V + wid * 8192 + ((b_ * (NB) + i) & 7) * 1024 + lane * 16) = stg[i]; } \
            asm volatile("s_waitcnt lgkmcnt(0)" ::: "memory"); \
            if (lane == 0) __hip_atomic_store(sy + SY_READY + wid, 1u, RLX_WG); } while (0)
        if (wid > 0 && wid < EARLYW) FETCH(16);
        const int idx = 0xDDDD;
        const LAS char* kp = lds + lane * 16;
        const bool vl = r32 < 16;
        const LAS char* vp = lds + (vl ? L_V + 512 + hh * 256 + r32 * 16 : (r32 & 11) == 0 ? L_MISC : L_MISC + 496);
        const int vstep = vl ? 2048 : 0;
        f32x16 o = (f32x16){}, negm, sA, sB = (f32x16){}; float gate = 0.f;
        bf16x8 kf = ldsr(kp), v0 = ldsr(vl ? vp - 512 : vp), v1 = (bf16x8){}, w0 = (bf16x8){};
        { const f32x16 s0 = MFMA(kf, qf, (f32x16){}); const float m0 = -xhalf_max(rowmax16(s0));
#pragma unroll
          for (int i = 0; i < 16; ++i) negm[i] = m0; }
        sA = MFMA(kf, qf, negm);
        kf = ldsr(kp + 1024);
        STAMP(10);
        __builtin_amdgcn_s_setprio(0);
#define EX(S, a, b) do { _Pragma("unroll") for (int i_ = (a); i_ < (b); ++i_) S[i_] = __builtin_amdgcn_exp2f(S[i_]); PIN(S); SBAR(); } while (0)
#define LO8(S) __builtin_bit_cast(bf16x16, __builtin_shufflevector(S, S, 0, 1, 2, 3, 4, 5, 6, 7))
#define HI8(S) __builtin_bit_cast(bf16x16, __builtin_shufflevector(S, S, 8, 9, 10, 11, 12, 13, 14, 15))
#define SM(a, b) do { o = __builtin_amdgcn_smfmac_f32_32x32x32_bf16(a, b, o, idx, 0, 0); PIN(o); SBAR(); } while (0)
#define TILE(CUR, OTH, KO, VO, VC, VN) do { SBAR(); EX(CUR, 0, 2); SM(v1, HI8(OTH)); v1 = ldsr(vp + (VO)); SBAR(); EX(CUR, 2, 6); \
            OTH = MFMA(kf, qf, negm); PIN(OTH); SBAR(); kf = ldsr(kp + (KO)); SBAR(); EX(CUR, 6, 10); SM(v0, LO8(CUR)); v0 = ldsr(vp + (VO) + 512); SBAR(); EX(CUR, 10, 16); } while (0)
#define LEAPFROG(pos) do { if (lane == 0) __hip_atomic_store(sy + SY_PROG + wid, (unsigned)(pos), RLX_WG); \
            if ((unsigned)__builtin_amdgcn_readfirstlane(__hip_atomic_load(sy + SY_PROG + (wid ^ 4), RLX_WG)) >= (unsigned)(pos)) __builtin_amdgcn_s_setprio(1); else __builtin_amdgcn_s_setprio(0); } while (0)
#define PAIR(KO) do { TILE(sA, sB, (KO) + 2048, 0, v0, w0); TILE(sB, sA, (KO) + 3072, 1024, w0, v0); vp += vstep; } while (0)
        PAIR(0);
        if (LATEPOS == 2 && wid >= EARLYW) { __builtin_amdgcn_s_setprio(3); FETCH(8); __builtin_amdgcn_s_setprio(0); }
        PAIR(2048);
        if (LATEPOS == 1 && wid >= EARLYW) { __builtin_amdgcn_s_setprio(3); FETCH(8); __builtin_amdgcn_s_setprio(0); }
        PAIR(4096);
        if (LATEPOS == 3 && wid >= EARLYW) { __builtin_amdgcn_s_setprio(3); FETCH(8); __builtin_amdgcn_s_setprio(0); }
        lds_wait_ge(sy + SY_READY + 1, 1);
        PAIR(6144);
        if (LATEPOS == 0 && wid >= EARLYW) { __builtin_amdgcn_s_setprio(3); FETCH(8); __builtin_amdgcn_s_setprio(YP); }
#undef FETCH
        STAMP(3);
        for (int j = 1; j < 8; ++j) {
            kp += 8192;
            if (LEAP) LEAPFROG(2 * j);
            PAIR(0); PAIR(2048);
            if (LEAP == 2) LEAPFROG(2 * j + 1);
            PAIR(4096);
            if (j < 7) lds_wait_ge(sy + SY_READY + j + 1, 1);
            PAIR(6144);
            if (j == 4 && lane == 0) __hip_atomic_fetch_add(sy + SY_PASS, 1u, RLX_WG);
            if (j == TRIGJ && lane == 0) __hip_atomic_fetch_add(sy + SY_TRIG, 1u, RLX_WG);
            if (j == 6) { lds_wait_ge(sy + SY_FSEEN, 4); gate = *(const LAS float*)(lds + g_at(32 * wid + r32)); }
        }
        SM(v1, HI8(sB));
#undef PAIR
#undef LEAPFROG
#undef TILE
#undef SM
#undef EX
        asm volatile("s_waitcnt lgkmcnt(0)" ::: "memory");
        if (lane == 0) __hip_atomic_fetch_add(sy + SY_DONE, 1u, RLX_WG);
        STAMP(4);
        { float chk = o[8] * 0.f;
#pragma unroll
          for (int i = 0; i < 8; ++i) chk = __builtin_fmaf(o[i], 0.f, chk);
          if (__builtin_expect(0 != __builtin_amdgcn_ballot_w64(!(chk == 0.f) || o[8] > 1.8e19f), 0)) {
              float m = -__builtin_inff();
#pragma nounroll
              for (int tl = 0; tl < 64; ++tl) { const bf16x8 kg = __builtin_bit_cast(bf16x8, ld1(rs, lane * 16, WS_KV + (unsigned)(hd * 8 + (tl >> 3)) * 16384u + (tl & 7) * 1024));
                  m = __builtin_fmaxf(m, rowmax16(MFMA(kg, qf, (f32x16){}))); }
              m = -xhalf_max(m);
#pragma unroll
              for (int i = 0; i < 16; ++i) negm[i] = m;
              o = (f32x16){};
              const u32x4 ones = {0x3F803F80u, 0x3F803F80u, 0x3F803F80u, 0x3F803F80u};
#pragma nounroll
              for (int tl = 0; tl < 64; ++tl) { const unsigned so = WS_KV + (unsigned)(hd * 8 + (tl >> 3)) * 16384u + (tl & 7) * 1024;
                  const bf16x8 kg = __builtin_bit_cast(bf16x8, ld1(rs, lane * 16, so));
                  const u32x4 a0 = ld1(rs, 8192 + hh * 256 + r16 * 16, so), a1 = ld1(rs, 8192 + 512 + hh * 256 + r16 * 16, so);
                  o = pv(__builtin_bit_cast(bf16x8, vl ? a0 : ones), __builtin_bit_cast(bf16x8, vl ? a1 : ones), exp16(MFMA(kg, qf, negm)), o, idx); }
          } }
        { const u32x4 ov = pack8(o, 0, gate * __builtin_amdgcn_rcpf(o[8]));
          st1(unmark(ov), rs, lane * 16, og); }
        STAMP(5);
        { const int rt = wid & 1, kq = wid >> 1, kreg = (kq >> 1) * L_V + (kq & 1) * 16384;
          const unsigned ob = WS_O + (unsigned)((8 * t + 2 * rq + rt) * 32 + 8 * kq) * 1024u;
#pragma nounroll
          for (unsigned s = 0; s < SPIN_CAP; ++s) {
              if (__builtin_popcountll(__builtin_amdgcn_ballot_w64(__builtin_amdgcn_raw_buffer_load_b32(rs, (lane & 7) * 1024 + 1020, ob, 16) == SENT)) <= 8 * LATE_OK) break;
              __builtin_amdgcn_s_sleep(1); CFENCE(); }
          u32x4 of[8];
#pragma unroll
          for (int i = 0; i < 8; ++i) of[i] = ld1(rs, lane * 16, ob + i * 1024);
          lds_wait_ge<1>(sy + SY_FP + kq, 1);
          f32x16 c[2];
#pragma unroll
          for (int ct = 0; ct < 2; ++ct)
#pragma unroll
              for (int g = 0; g < 4; ++g) { const f32x4 v = *(const LAS f32x4*)(lds + kreg + 32768 + (2 * rt + ct) * 4096 + g * 1024 + lane * 16);
#pragma unroll
                  for (int i = 0; i < 4; ++i) c[ct][4 * g + i] = v[i]; }
          PIN(c[0]); PIN(c[1]);
#pragma nounroll
          for (unsigned s = 0; s < SPIN_CAP; ++s) {
              unsigned miss = 0;
#pragma unroll
              for (int i = 0; i < 8; ++i) if (marked(of[i])) miss |= 1u << i;
              if (miss == 0) break;
              CFENCE();
#pragma unroll
              for (int i = 0; i < 8; ++i) if ((miss >> i) & 1) of[i] = ld1(rs, lane * 16, ob + i * 1024);
          }
          STAMP(11);
#pragma unroll
          for (int n = 0; n < 8; ++n) { const LAS char* p = lds + kreg + n * 2048 + lane * 16;
              c[0] = MFMA(ldsr(p), __builtin_bit_cast(bf16x8, of[n]), c[0]); c[1] = MFMA(ldsr(p + 1024), __builtin_bit_cast(bf16x8, of[n]), c[1]); }
          f32x8 own;
#pragma unroll
          for (int pc = 0; pc < 4; ++pc) {
              if (pc == kq) {
#pragma unroll
                  for (int i = 0; i < 8; ++i) own[i] = c[pc >> 1][8 * (pc & 1) + i];
              } else {
#pragma unroll
                  for (int g = 0; g < 2; ++g) { const int r = 8 * (pc & 1) + 4 * g; *(LAS f32x4*)(lds + kreg + 32768 + rt * 8192 + pc * 2048 + g * 1024 + lane * 16) = (f32x4){c[pc >> 1][r], c[pc >> 1][r + 1], c[pc >> 1][r + 2], c[pc >> 1][r + 3]}; }
              } }
          asm volatile("s_waitcnt lgkmcnt(0)" ::: "memory");
          if (lane == 0) __hip_atomic_fetch_add(sy + SY_RED + rt, 1u, RLX_WG);
          lds_wait_ge<0>(sy + SY_RED + rt, 4);
          STAMP(21);
#pragma unroll
          for (int k = 1; k < 4; ++k) { const int w = (kq + k) & 3;
#pragma unroll
              for (int g = 0; g < 2; ++g) { const f32x4 v = *(const LAS f32x4*)(lds + (w >> 1) * L_V + (w & 1) * 16384 + 32768 + rt * 8192 + kq * 2048 + g * 1024 + lane * 16);
#pragma unroll
                  for (int i = 0; i < 4; ++i) own[4 * g + i] += v[i]; } }
          auto a = __builtin_amdgcn_permlane32_swap(pack2(own[0], own[1]), pack2(own[4], own[5]), false, false), b = __builtin_amdgcn_permlane32_swap(pack2(own[2], own[3]), pack2(own[6], own[7]), false, false);
          *(u32x4*)(out + (size_t)(256 * t + 64 * rq + 32 * rt + r32) * DM + 64 * cp + 16 * kq + 8 * hh) = (u32x4){a[0], b[0], a[1], b[1]};
          if (bx == 0 && wid == 0 && lane == 0) __hip_atomic_store((GAS u64*)(ws + WS_FLAGS), tag, RLX_AGENT);
        }
        STAMP(9); STAMP_FLUSH();
    } else {
        const int f = wid - 8, i32 = rq + 4 * t;
        { LANES(); const int xsw = g4 ^ (r16 >> 1);
          unsigned cs_[3] = {0, 0, 0};
          EPOCH_LOAD();
          if (CPF) {
#pragma unroll
              for (int k = 0; k < 3; ++k) { unsigned o_ = (unsigned)((bx >> 3) + 32 * (3 * f + k)) * 128u, d_; o_ = o_ < clen ? o_ : 0u;
                  asm volatile("s_load_dword %0, %1, %2" : "=s"(d_) : "s"(pc0), "s"(o_) : "memory"); cs_[k] = d_; }
          }
          if (f == 1 && lane < 32) { if (hasF) glds4(lane * 4, (const char*)b1 + fb * 128, lds0 + L_B1); glds4(lane * 4, (const char*)b2 + cp * 128, lds0 + L_B2); }
          if (hd == 31) { glds16(lane * 16, gate_w, lds0 + L_GATE + f * 1024); asm volatile("s_waitcnt vmcnt(0)" ::: "memory"); }
          const int xo = (64 * f + r16) * 128, fo = XB + 6144 + (4 * g4 + q4) * 128 + p4 * 8, fsw = (2 * g4 + (q4 >> 1)) & 3;
          bf16x8 xf[2][4], wf[2][4];
#define RX(c, ks, rt) xf[ks][rt] = ldsr(lds + BUFOF(c) + xo + (rt) * 2048 + ((xsw ^ (4 * (ks))) * 16))
#define RW(c, ks, cb) wf[ks][cb] = ldtr(lds + BUFOF(c) + fo + (ks) * 4096 + (((cb) ^ fsw) * 32), lds + BUFOF(c) + fo + (ks) * 4096 + 2048 + (((cb) ^ fsw) * 32))
#define M2(ks, cb, r0) do { af[cb][r0] = MFMA16(wf[ks][cb], xf[ks][r0], af[cb][r0]); af[cb][r0 + 1] = MFMA16(wf[ks][cb], xf[ks][r0 + 1], af[cb][r0 + 1]); PIN(af[cb][r0]); PIN(af[cb][r0 + 1]); SBAR(); } while (0)
#define F_SYNC(c) do { if ((c) != RTC && (c) != 0) asm volatile("s_waitcnt lgkmcnt(0)\n\ts_barrier" ::: "memory"); else asm volatile("s_waitcnt vmcnt(0) lgkmcnt(0)\n\ts_barrier" ::: "memory"); \
              if ((c) == 0) asm volatile("" :: "s"(cs_[0]), "s"(cs_[1]), "s"(cs_[2])); \
              if ((c) == 0 || (c) == 7) STAMP(12 + (c)); \
              if ((c) == RTC) { EPOCH_TAKE(); LANES();     \
                  const int u = i32 + 32 * f; const char* src = u < 112 ? (const char*)w2 + (size_t)u * 16384 : (const char*)wo + (size_t)(u - 112) * 16384; \
                  const unsigned vo = (lane >> 3) * 1024 + cp * 128 + (lane & 7) * 16; \
                  glds16(vo, src, lds0 + L_RT + f * 2048); glds16(vo, src + 8192, lds0 + L_RT + f * 2048 + 1024); \
                  if (f == 0 && i32 < 16) { const char* s2 = (const char*)wo + (size_t)(i32 + 16) * 16384; glds16(vo, s2, lds0 + L_RT + 8192); glds16(vo, s2 + 8192, lds0 + L_RT + 8192 + 1024); } } } while (0)
#define F_LEAVE() STAMP(1); LANES(); \
              asm volatile("s_waitcnt lgkmcnt(0)" ::: "memory"); \
              if (lane == 0) __hip_atomic_fetch_add(sy + SY_FA, 1u, RLX_WG); \
              __builtin_amdgcn_s_setprio(2);
#define F_RETILE() do { \
              _Pragma("unroll") for (int i = 0; i < 2; ++i)     \
                  st1((u32x4){SENT, SENT, SENT, SENT}, rs, lane * 16, WS_O + (unsigned)((8 * t + 2 * f + i) * 32 + hd) * 1024u); \
              for (int n = f; n < ((f == 0 && i32 < 16) ? 5 : 4); n += 4) {        \
                  const int u = i32 + 32 * n, r0 = (u < 112 ? 8 : 4) * hh + q4, r1 = u < 112 ? 512 : 1024; \
                  _Pragma("unroll") for (int ct = 0; ct < 2; ++ct) { const LAS char* s = lds + L_RT + n * 2048 + r0 * 128 + (g4 & 1) * 32 + p4 * 8 + ct * 64; \
                      st1(__builtin_bit_cast(u32x4, ldtr(s, s + r1)), rs, lane * 16, WS_W + (unsigned)((2 * cp + ct) * 144 + u) * 1024u); } } } while (0)
          if (hasF) {
              f32x4 af[4][4];
#pragma unroll
              for (int c = 0; c < 8; ++c) {
                  F_SYNC(c);
                  if (c == 0) {
#pragma unroll
                      for (int cb = 0; cb < 4; ++cb) { const u32x2 w = *(const LAS u32x2*)(lds + L_B1 + cb * 32 + g4 * 8);
#pragma unroll
                          for (int rt = 0; rt < 4; ++rt) af[cb][rt] = (f32x4){U2F(w[0] << 16), U2F(w[0] & 0xffff0000u), U2F(w[1] << 16), U2F(w[1] & 0xffff0000u)}; }
                  }
                  if (FDEFER && c == 7) lds_wait_ge<2>(sy + SY_AW, 8);
                  if (c > 0) M2(1, 0, 0);
                  RX(c, 0, 0); SBAR();
                  if (c > 0) M2(1, 0, 2);
                  RW(c, 0, 0); SBAR();
                  if (c > 0) M2(1, 1, 0);
                  RX(c, 0, 1); SBAR();
                  if (c > 0) M2(1, 1, 2);
                  RX(c, 0, 2); SBAR();
                  if (c > 0) M2(1, 2, 0);
                  RX(c, 0, 3); SBAR();
                  if (c > 0) M2(1, 2, 2);
                  RW(c, 0, 1); SBAR();
                  if (c > 0) M2(1, 3, 0);
                  RW(c, 0, 2); SBAR();
                  if (c > 0) M2(1, 3, 2);
                  RW(c, 0, 3); SBAR();
                  M2(0, 0, 0); RX(c, 1, 0); RW(c, 1, 0); SBAR();
                  M2(0, 0, 2); RX(c, 1, 1); RX(c, 1, 2); SBAR();
                  M2(0, 1, 0); RX(c, 1, 3); RW(c, 1, 1); SBAR();
                  M2(0, 1, 2); RW(c, 1, 2); RW(c, 1, 3); SBAR();
                  M2(0, 2, 0); M2(0, 2, 2); M2(0, 3, 0); M2(0, 3, 2);
              }
              M2(1, 0, 0); M2(1, 0, 2); M2(1, 1, 0); M2(1, 1, 2); M2(1, 2, 0); M2(1, 2, 2); M2(1, 3, 0); M2(1, 3, 2);
              { F_LEAVE();
                u32x4 hp[4][2];
#pragma unroll
                for (int cb = 0; cb < 4; ++cb) {
#pragma unroll
                    for (int rp = 0; rp < 2; ++rp) {
                        float v[8], e[8];
#pragma unroll
                        for (int i = 0; i < 8; ++i) { v[i] = af[cb][2 * rp + (i >> 2)][i & 3];
                            e[i] = v[i] * __builtin_fmaf(v[i] * v[i], -2.f * 0.7978845608028654f * 1.4426950408889634f * 0.044715f, -2.f * 0.7978845608028654f * 1.4426950408889634f); }
                        SBAR();
#pragma unroll
                        for (int i = 0; i < 8; ++i) e[i] = __builtin_amdgcn_exp2f(e[i]);
                        SBAR();
#pragma unroll
                        for (int i = 0; i < 8; ++i) e[i] = 1.f + e[i];
                        SBAR();
#pragma unroll
                        for (int i = 0; i < 8; ++i) e[i] = __builtin_amdgcn_rcpf(e[i]);
                        SBAR();
                        hp[cb][rp] = join16((f32x4){v[0] * e[0], v[1] * e[1], v[2] * e[2], v[3] * e[3]}, (f32x4){v[4] * e[4], v[5] * e[5], v[6] * e[6], v[7] * e[7]}, 1.f, 1.f);
                        if (!HLATE) st1(hp[cb][rp], rs, lane * 16, WS_H + (unsigned)((8 * t + 2 * f + rp) * 112 + 4 * fb + cb) * 1024u);
                        PIN(hp[cb][rp]); SBAR();
                    }
                }
                STAMP(2);
                if (HLATE) { drain();
#pragma unroll
                    for (int i = 0; i < 8; ++i) st1(hp[i >> 1][i & 1], rs, lane * 16, WS_H + (unsigned)((8 * t + 2 * f + (i & 1)) * 112 + 4 * fb + (i >> 1)) * 1024u);
                } else asm volatile("s_waitcnt vmcnt(8)" ::: "memory");
                F_RETILE(); }
          } else {
              f32x4 az[4] = {};
#pragma unroll
              for (int c = 0; c < 8; ++c) {
                  F_SYNC(c);
                  if (hd == 31) {
#pragma unroll
                      for (int ks = 0; ks < 2; ++ks) { const bf16x8 gf = ldsr(lds + L_GATE + f * 1024 + CS(c) * 128 + ks * 64 + g4 * 16);
#pragma unroll
                          for (int rt = 0; rt < 4; ++rt) { RX(c, ks, rt); az[rt] = MFMA16(gf, xf[ks][rt], az[rt]); } }
                  }
              }
              { F_LEAVE(); drain(); F_RETILE();
                if (hd == 31) {
                    const float z = bfround(g4 == 0 ? az[0][0] : g4 == 1 ? az[1][0] : g4 == 2 ? az[2][0] : az[3][0]);
                    __hip_atomic_store((GAS float*)(ws + WS_G + (256 * t + 64 * f + lane) * 4), bfround(__builtin_amdgcn_rcpf(1.f + __builtin_amdgcn_exp2f(-1.4426950408889634f * z))), RLX_AGENT);
                } }
          }
#undef F_SYNC
#undef F_LEAVE
#undef F_RETILE
#undef RX
#undef RW
#undef M2
        }
        LANES();
        const int freg = (f >> 1) * L_V + (f & 1) * 16384;
        drain();
        if (lds_arrive(sy + SY_F, lane) == 3 && lane == 0) __hip_atomic_store((GAS u64*)(ws + flag_at(1, hd, t)), tag, RLX_AGENT);
        STAMP(3);
        f32x16 acc[4] = {};
        poll_tags<4>(ws, hh ? flag_at(1, cp + 8 * (r32 & 3), r32 >> 2) : flag_at(1, r32, t), true, tag);
        *(LAS float*)(lds + g_at(64 * f + lane)) = __hip_atomic_load((GAS float*)(ws + WS_G + (256 * t + 64 * f + lane) * 4), RLX_AGENT);
        asm volatile("s_waitcnt lgkmcnt(0)" ::: "memory");
        if (lane == 0) __hip_atomic_fetch_add(sy + SY_FSEEN, 1u, RLX_WG);
        __builtin_amdgcn_s_setprio(F2_PRIO);
        STAMP(4);
        const unsigned hb = WS_H + (unsigned)((8 * t + 2 * rq) * 112 + 28 * f) * 1024u, wb = WS_W + (unsigned)(2 * cp * 144 + 28 * f) * 1024u;
        u32x4 fr[4][4];
#define LDK(slot, n) do { fr[slot][0] = ld1(rs, lane * 16, wb + (n) * 1024); fr[slot][1] = ld1(rs, lane * 16, wb + (144 + (n)) * 1024); \
                          fr[slot][2] = ld1(rs, lane * 16, hb + (n) * 1024); fr[slot][3] = ld1(rs, lane * 16, hb + (112 + (n)) * 1024); } while (0)
#define BC(v) __builtin_bit_cast(bf16x8, v)
#define MM4(w0, w1_, h0, h1) do { acc[0] = MFMA(w0, h0, acc[0]); acc[1] = MFMA(w0, h1, acc[1]); acc[2] = MFMA(w1_, h0, acc[2]); acc[3] = MFMA(w1_, h1, acc[3]); PIN(acc[0]); PIN(acc[1]); PIN(acc[2]); PIN(acc[3]); SBAR(); } while (0)
#define FFN2(n0, n1, pace) do { _Pragma("unroll") for (int n = (n0); n < (n1); ++n) { MM4(BC(fr[n & 3][0]), BC(fr[n & 3][1]), BC(fr[n & 3][2]), BC(fr[n & 3][3])); if (n + 4 < (n1)) LDK(n & 3, n + 4); if (pace) __builtin_amdgcn_s_sleep(pace); SBAR(); } } while (0)
#pragma unroll
        for (int n = 0; n < 4; ++n) LDK(n, n);
        FFN2(0, NP1, F2_PACE);
        STAMP(5);
        { u32x4 tmp[16];
#pragma unroll
          for (int i = 0; i < 16; ++i) tmp[i] = ld1(rs, lane * 16, WS_W + (unsigned)((2 * cp + (i & 1)) * 144 + 112 + 8 * f + (i >> 1)) * 1024u);
          __builtin_amdgcn_s_setprio(2);
          lds_wait_ge<16>(sy + SY_PASS, 8);
#pragma unroll
          for (int i = 0; i < 16; ++i) *(LAS u32x4*)(lds + freg + i * 1024 + lane * 16) = tmp[i]; }
        STAMP(6);
        if (NP1 < 28) {
#pragma unroll
            for (int n = NP1; n < NP1 + 4; ++n) LDK(n & 3, n);
            lds_wait_ge<8>(sy + SY_TRIG, 8);
            __builtin_amdgcn_s_setprio(F2_PRIO);
            FFN2(NP1, 28, 0);
        }
#undef LDK
#undef FFN2
        lds_wait_ge(sy + SY_FSEEN, 4);
        { const float og0 = bfround(1.f - *(const LAS float*)(lds + g_at(64 * rq + r32))), og1 = bfround(1.f - *(const LAS float*)(lds + g_at(64 * rq + 32 + r32)));
#pragma unroll
          for (int ct = 0; ct < 2; ++ct)
#pragma unroll
              for (int g = 0; g < 4; ++g) { u32x2 w = *(const LAS u32x2*)(lds + L_B2 + ct * 64 + g * 16 + hh * 8); if (f != 0) w = (u32x2){0u, 0u};
                  const float bb[4] = {U2F(w[0] << 16), U2F(w[0] & 0xffff0000u), U2F(w[1] << 16), U2F(w[1] & 0xffff0000u)};
#pragma unroll
                  for (int i = 0; i < 4; ++i) { acc[2 * ct][4 * g + i] = (acc[2 * ct][4 * g + i] + bb[i]) * og0; acc[2 * ct + 1][4 * g + i] = (acc[2 * ct + 1][4 * g + i] + bb[i]) * og1; } } }
#undef MM4
#undef BC
        STAMP(7);
        lds_wait_ge<1>(sy + SY_DONE, 8);
#pragma unroll
        for (int qd = 0; qd < 4; ++qd)
#pragma unroll
            for (int g = 0; g < 4; ++g) *(LAS f32x4*)(lds + freg + 32768 + (2 * (qd & 1) + (qd >> 1)) * 4096 + g * 1024 + lane * 16) = (f32x4){acc[qd][4 * g], acc[qd][4 * g + 1], acc[qd][4 * g + 2], acc[qd][4 * g + 3]};
        asm volatile("s_waitcnt lgkmcnt(0)" ::: "memory");
        if (lane == 0) __hip_atomic_store(sy + SY_FP + f, 1u, RLX_WG);
        asm volatile(".Lgm_end:");
        STAMP(9); STAMP_FLUSH();
    }
}

extern "C" void kernel_launch(void* const* d_in, const int* in_sizes, int n_in, void* d_out, int out_size, void* d_ws, size_t ws_size, hipStream_t stream) {
    (void)in_sizes; (void)n_in; (void)out_size; (void)ws_size;
    hipLaunchKernelGGL(gated_mix_fused, dim3(256), dim3(768), 0, stream, (const unsigned short*)d_in[0], (const unsigned short*)d_in[1], (const unsigned short*)d_in[2],
                       (const unsigned short*)d_in[4], (const unsigned short*)d_in[5], (const unsigned short*)d_in[6], (const unsigned short*)d_in[7], (const unsigned short*)d_in[8],
                       (unsigned short*)d_out, (char*)d_ws);
}
```

```cpp
#include <hip/hip_runtime.h>
#include <hip/hip_bf16.h>
#include <stdint.h>

typedef __attribute__((ext_vector_type(8))) __bf16 bf16x8;
typedef __attribute__((ext_vector_type(16))) __bf16 bf16x16;
typedef __attribute__((ext_vector_type(2))) __bf16 bf16x2;
typedef __attribute__((ext_vector_type(4))) short s16x4;
typedef __attribute__((ext_vector_type(16))) float f32x16;
typedef __attribute__((ext_vector_type(8))) float f32x8;
typedef __attribute__((ext_vector_type(4))) float f32x4;
typedef __attribute__((ext_vector_type(2))) float f32x2;
typedef __attribute__((ext_vector_type(4))) unsigned u32x4;
typedef __attribute__((ext_vector_type(2))) unsigned u32x2;
typedef unsigned long long u64;
#define LAS __attribute__((address_space(3)))
#define GAS __attribute__((address_space(1)))
#if defined(__HIP_DEVICE_COMPILE__)
#define NOPK __attribute__((target("no-packed-fp32-ops")))
#else
#define NOPK
#endif
#define DI __device__ __forceinline__ NOPK
#define U2F(x) __builtin_bit_cast(float, (unsigned)(x))
#define F2U(x) __builtin_bit_cast(unsigned, (float)(x))
#define RLX_AGENT __ATOMIC_RELAXED, __HIP_MEMORY_SCOPE_AGENT
#define RLX_WG __ATOMIC_RELAXED, __HIP_MEMORY_SCOPE_WORKGROUP

constexpr int DM = 512;
constexpr unsigned WS_FLAGS = 0;
constexpr unsigned WS_FLAG0 = 4096, FLAG_STRIDE = 128;
constexpr unsigned WS_G = 106496;
constexpr unsigned WS_KV = 131072;
constexpr unsigned WS_H = WS_KV + 32u * 8u * 16384u;
constexpr unsigned WS_O = WS_H + 64u * 112u * 1024u;
constexpr unsigned WS_W = WS_O + 64u * 32u * 1024u;
constexpr unsigned WS_END = WS_W + 16u * 144u * 1024u;
constexpr int XB = 32768, BUFB = XB + 6144 + 8192;
constexpr int L_V = 65536;
constexpr int L_GATE = 3 * BUFB;
constexpr int L_RT = L_GATE + 1024;
constexpr int L_MISC = 163840 - 2048;
constexpr int L_DUMMY = L_RT + 10240;
constexpr int L_B1 = L_MISC + 272, L_B2 = L_MISC + 512 + 272, L_SYNC = L_MISC + 1024 + 272, L_EP = L_MISC + 1536 + 272;
static_assert(L_DUMMY + 1024 <= L_MISC && L_V + 3 * 8192 <= 2 * BUFB && 3 * 8192 <= BUFB, "lds");
enum { SY_READY = 0  , SY_KV = 8, SY_DONE = 9, SY_F = 10, SY_PASS = 11, SY_TRIG = 13, SY_FSEEN = 14, SY_AW = 15, SY_FA = 16, SY_RED = 18  , SY_FP = 20  , SY_N = 24 };
constexpr unsigned SENT = 0xFFFFFFFFu;
constexpr unsigned SPIN_CAP = 1u << 18;

#define MFMA(a, b, c) __builtin_amdgcn_mfma_f32_32x32x16_bf16((a), (b), (c), 0, 0, 0)
#define MFMA16(a, b, c) __builtin_amdgcn_mfma_f32_16x16x32_bf16((a), (b), (c), 0, 0, 0)
#define SBAR() __builtin_amdgcn_sched_barrier(0)
#define CFENCE() asm volatile("" ::: "memory")
#define PIN(x) asm volatile("" : "+v"(x))

constexpr int NP1 = 24;
constexpr int LATE_OK = 2;

DI unsigned pack2(float lo, float hi) { f32x2 v = {lo, hi}; return __builtin_bit_cast(unsigned, __builtin_convertvector(v, bf16x2)); }
DI u32x4 pack8(const f32x16& a, int o, float s) {
    u32x4 r; r[0] = pack2(a[o] * s, a[o + 1] * s); r[1] = pack2(a[o + 2] * s, a[o + 3] * s); r[2] = pack2(a[o + 4] * s, a[o + 5] * s); r[3] = pack2(a[o + 6] * s, a[o + 7] * s); return r; }
DI float bfround(float v) { return (float)(__bf16)v; }
DI bf16x8 ldsr(const LAS char* p) { return *(const LAS bf16x8*)p; }
DI bf16x8 ldtr(const LAS char* p0, const LAS char* p1) {
    s16x4 a = __builtin_amdgcn_ds_read_tr16_b64_v4i16((LAS s16x4*)p0), b = __builtin_amdgcn_ds_read_tr16_b64_v4i16((LAS s16x4*)p1);
    return __builtin_bit_cast(bf16x8, __builtin_shufflevector(a, b, 0, 1, 2, 3, 4, 5, 6, 7)); }
DI void glds16(unsigned voff, const void* sbase, unsigned lds) {
    unsigned keep; asm volatile("s_mov_b32 %0, m0\n\ts_mov_b32 m0, %3\n\ts_nop 0\n\tglobal_load_lds_dwordx4 %1, %2 sc1\n\ts_mov_b32 m0, %0" : "=&s"(keep) : "v"(voff), "s"(sbase), "s"(lds) : "memory"); }
DI void glds4(unsigned voff, const void* sbase, unsigned lds) {
    unsigned keep; asm volatile("s_mov_b32 %0, m0\n\ts_mov_b32 m0, %3\n\ts_nop 0\n\tglobal_load_lds_dword %1, %2\n\ts_mov_b32 m0, %0" : "=&s"(keep) : "v"(voff), "s"(sbase), "s"(lds) : "memory"); }
DI u32x4 ld1(__amdgpu_buffer_rsrc_t rs, unsigned voff, unsigned soff) { return __builtin_amdgcn_raw_buffer_load_b128(rs, voff, soff, 16); }
DI void st1(u32x4 v, __amdgpu_buffer_rsrc_t rs, unsigned voff, unsigned soff) {
    __builtin_amdgcn_raw_buffer_store_b128(v, rs, voff, soff, 16); asm volatile("s_nop 1" :: "v"(v) : "memory"); }
DI u32x4 unmark(u32x4 v) {
#pragma unroll
    for (int i = 0; i < 4; ++i) v[i] = v[i] == SENT ? 0x7FFF7FFFu : v[i];
    return v; }
DI bool marked(const u32x4& d) { return __builtin_amdgcn_ballot_w64(__builtin_elementwise_max(__builtin_elementwise_max(d[0], d[1]), __builtin_elementwise_max(d[2], d[3])) == SENT) != 0; }
DI void drain() { asm volatile("s_waitcnt vmcnt(0)" ::: "memory"); }
DI unsigned lds_arrive(LAS unsigned* w, int lane) {
    unsigned old = 0; if (lane == 0) old = __hip_atomic_fetch_add(w, 1u, RLX_WG); return __builtin_amdgcn_readfirstlane(old); }
template <int SL = 1> DI void lds_wait_ge(LAS unsigned* w, unsigned want) {
    unsigned tv, ts, n;
    asm volatile("s_mov_b32 %2, %6\n"
                 "1:\n\tds_read_b32 %0, %3\n\ts_waitcnt lgkmcnt(0)\n\tv_readfirstlane_b32 %1, %0\n\ts_nop 1\n\ts_cmp_ge_u32 %1, %4\n\ts_cbranch_scc1 2f\n\t"
                 "s_sleep %5\n\ts_sub_u32 %2, %2, 1\n\ts_cmp_lg_u32 %2, 0\n\ts_cbranch_scc1 1b\n"
                 "2:" : "=&v"(tv), "=&s"(ts), "=&s"(n) : "v"((unsigned)(uintptr_t)w), "s"(want), "i"(SL), "i"(0x400000 / (SL + 1)) : "memory", "scc"); }
template <int SL = 1> DI void poll_tags(const char* ws, unsigned flag_off, bool active, u64 tag) {
#pragma nounroll
    for (unsigned s = 0; s < SPIN_CAP; ++s) {
        u64 v = tag; if (active) v = __hip_atomic_load((GAS u64*)(ws + flag_off), RLX_AGENT);
        if (__builtin_amdgcn_ballot_w64(v != tag) == 0) break; __builtin_amdgcn_s_sleep(SL); }
    __builtin_amdgcn_fence(__ATOMIC_ACQUIRE, "wavefront"); CFENCE(); }
DI unsigned flag_at(int kind, int hd, int t) { return WS_FLAG0 + (unsigned)(kind * 256 + hd * 8 + t) * FLAG_STRIDE; }
DI int g_at(int i) { return L_MISC + (i >> 6) * 512 + 16 + (i & 63) * 4; }
DI u32x4 join16(const f32x4& a, const f32x4& b, float sa, float sb) {
    auto r0 = __builtin_amdgcn_permlane16_swap(pack2(a[0] * sa, a[1] * sa), pack2(b[0] * sb, b[1] * sb), false, false);
    auto r1 = __builtin_amdgcn_permlane16_swap(pack2(a[2] * sa, a[3] * sa), pack2(b[2] * sb, b[3] * sb), false, false);
    return (u32x4){r0[0], r1[0], r0[1], r1[1]}; }
DI f32x16 exp16(f32x16 s) {
#pragma unroll
    for (int i = 0; i < 16; ++i) s[i] = __builtin_amdgcn_exp2f(s[i]);
    return s; }
DI f32x16 pv(bf16x8 v0, bf16x8 v1, const f32x16& p, f32x16 o, int idx) {
    const f32x8 lo = __builtin_shufflevector(p, p, 0, 1, 2, 3, 4, 5, 6, 7), hi = __builtin_shufflevector(p, p, 8, 9, 10, 11, 12, 13, 14, 15);
    o = __builtin_amdgcn_smfmac_f32_32x32x32_bf16(v0, __builtin_bit_cast(bf16x16, lo), o, idx, 0, 0);
    o = __builtin_amdgcn_smfmac_f32_32x32x32_bf16(v1, __builtin_bit_cast(bf16x16, hi), o, idx, 0, 0);
    return o; }
DI float rowmax16(const f32x16& s) {
    float m = __builtin_fmaxf(__builtin_fmaxf(s[0], s[1]), s[2]);
#pragma unroll
    for (int i = 3; i < 15; i += 2) m = __builtin_fmaxf(__builtin_fmaxf(m, s[i]), s[i + 1]);
    return __builtin_fmaxf(m, s[15]); }
DI float xhalf_max(float m) { auto r = __builtin_amdgcn_permlane32_swap(F2U(m), F2U(m), false, false); return __builtin_fmaxf(U2F(r[0]), U2F(r[1])); }

__global__ __launch_bounds__(768, 3) NOPK void gated_mix_fused(const unsigned short* __restrict__ x, const unsigned short* __restrict__ wqkv, const unsigned short* __restrict__ wo,
                                                         const unsigned short* __restrict__ w1, const unsigned short* __restrict__ b1, const unsigned short* __restrict__ w2,
                                                         const unsigned short* __restrict__ b2, const unsigned short* __restrict__ gate_w, unsigned short* __restrict__ out, char* ws) {
    __shared__ __attribute__((aligned(1024))) char smem[163840];
    LAS char* lds = (LAS char*)smem;
    const unsigned lds0 = (unsigned)(uintptr_t)lds;
    const int wid = __builtin_amdgcn_readfirstlane(__builtin_amdgcn_workitem_id_x() >> 6);
#define LANES() int lane = __builtin_amdgcn_mbcnt_hi(~0u, __builtin_amdgcn_mbcnt_lo(~0u, 0u)); PIN(lane); const int r32 = lane & 31, hh = lane >> 5, g4 = lane >> 4, r16 = lane & 15, q4 = r16 >> 2, p4 = lane & 3; (void)r32; (void)hh; (void)g4; (void)q4; (void)p4
    const int bx = __builtin_amdgcn_workgroup_id_x(), hd = 8 * (bx & 3) + ((bx >> 3) & 7), t = 4 * ((bx >> 2) & 1) + (bx >> 6);
    const __amdgpu_buffer_rsrc_t rs = __builtin_amdgcn_make_buffer_rsrc(ws, 0, WS_END, 0x00020000);
    LAS unsigned* sy = (LAS unsigned*)(lds + L_SYNC);
    asm volatile("" :: "s"(x), "s"(wqkv), "s"(wo), "s"(w1), "s"(b1), "s"(w2), "s"(b2), "s"(gate_w), "s"(out), "s"(ws));
    u64 tag;
#define EPOCH_TAKE() do { const u32x2 ep = *(const LAS u32x2*)(lds + L_EP); tag = (((u64)(unsigned)__builtin_amdgcn_readfirstlane(ep[1]) << 32) | (unsigned)__builtin_amdgcn_readfirstlane(ep[0])) + 1ull; } while (0)
    const int cp = hd & 7, rq = hd >> 3;
    const bool hasF = cp != 7; const int fb = hd - rq;
    const int rot = 2 * ((hd + t) & 3);
#define CS(c) (((c) + rot) & 7)
#define BUFOF(c) ((((c) + 1) % 3) * BUFB)
#define CHUNK_SYNC(c, n) do { if ((c) < 7) asm volatile("s_waitcnt vmcnt(" #n ") lgkmcnt(0)\n\ts_barrier" ::: "memory"); else asm volatile("s_waitcnt vmcnt(0) lgkmcnt(0)\n\ts_barrier" ::: "memory"); } while (0)

    if (__builtin_expect(wid < 8, 1)) {
        f32x4 aq[2] = {}, ak[2] = {}, av[2] = {};
        { LANES(); const int xsw = g4 ^ (r16 >> 1);
          const int c0 = (lane & 7) ^ (lane >> 4), rho = lane >> 1, r8 = lane >> 3;
          const unsigned dvo0 = r8 * 1024 + c0 * 16, dvo1 = r8 * 1024 + (c0 ^ 4) * 16;
          const unsigned dvq = (8 * ((rho >> 2) & 3) + 4 * (rho >> 4) + (rho & 3)) * 3072 + (lane & 1) * 16;
          const unsigned dvf = (8 * (r8 >> 2) + (r8 & 3)) * 3584 + (((lane & 7) ^ (r8 & 6)) * 16);
          const char* xbase = (const char*)x + (size_t)(256 * t + 32 * wid) * 1024;
          const bool hq = wid < 6, hf = hasF;
          const char* qbase = (const char*)wqkv + hd * 32 + (hq ? (wid >> 1) * 1024 + (wid & 1) * 32 * 3072 : 0);
          const char* fbase = (const char*)w1 + (hf ? fb * 128 : 0) + (32 * (wid >> 2) + 16 * (wid & 1) + 4 * ((wid >> 1) & 1)) * 3584;
          const unsigned vq_ = hq ? dvq : 0u, vf_ = hf ? dvf : 0u;
#define DMA_X(c) do { _Pragma("unroll") for (int i = 0; i < 4; ++i) glds16((i & 1) ? dvo1 : dvo0, xbase + CS(c) * 128 + i * 8192, lds0 + BUFOF(c) + wid * 4096 + i * 1024); } while (0)
#define DMA_ALL(c) do { glds16(vq_, qbase + CS(c) * 64 * 3072, hq ? lds0 + BUFOF(c) + XB + wid * 1024 : lds0 + L_DUMMY); \
              glds16(vf_, fbase + CS(c) * 64 * 3584, hf ? lds0 + BUFOF(c) + XB + 6144 + wid * 1024 : lds0 + L_DUMMY); DMA_X(c); } while (0)
          DMA_ALL(0);
          if (wid == 1) {
              if (lane < 32) { if (hasF) glds4(lane * 4, (const char*)b1 + fb * 128, lds0 + L_B1); glds4(lane * 4, (const char*)b2 + cp * 128, lds0 + L_B2); }
              if (lane < 2) glds4(lane * 4, ws + WS_FLAGS, lds0 + L_EP);
              if (hd == 31) glds16(lane * 16, gate_w, lds0 + L_GATE);
          }
          if (wid == 0) {
              if (lane < SY_N) sy[lane] = 0;
              if (lane < 8) { const unsigned one = lane < 4 ? 0x3F803F80u : 0u; *(LAS u32x4*)(lds + L_MISC + (lane & 3) * 512 + (lane >> 2) * 496) = (u32x4){one, one, one, one}; }
          }
          __builtin_amdgcn_s_sleep(20);
          DMA_ALL(1);
          const int xo = (32 * wid + r16) * 128, wo_ = XB + (4 * g4 + q4) * 32 + p4 * 8;
          bf16x8 xa[2], xb[2], wq[2], wk[2], wv[2];
#define RX(d, c, ks, rt) d[ks] = ldsr(lds + BUFOF(c) + xo + (rt) * 2048 + ((xsw ^ (4 * (ks))) * 16))
#define RW(d, c, ks, im) d[ks] = ldtr(lds + BUFOF(c) + wo_ + (im) * 2048 + (ks) * 1024, lds + BUFOF(c) + wo_ + (im) * 2048 + (ks) * 1024 + 512)
#define M2(A, W, ks) do { A[0] = MFMA16(W[ks], xa[ks], A[0]); A[1] = MFMA16(W[ks], xb[ks], A[1]); PIN(A[0]); PIN(A[1]); SBAR(); } while (0)
#define MV(ks) do { av[0] = MFMA16(xa[ks], wv[ks], av[0]); av[1] = MFMA16(xb[ks], wv[ks], av[1]); PIN(av[0]); PIN(av[1]); SBAR(); } while (0)
#pragma unroll
          for (int c = 0; c < 8; ++c) {
              CHUNK_SYNC(c, 6);
              if (c == 7) __builtin_amdgcn_s_setprio(3);
              if (c + 2 < 8 && wid >= 5) DMA_ALL(c + 2);
              if (c > 0) M2(aq, wq, 1);
              RX(xa, c, 0, 0); RX(xb, c, 0, 1); RW(wq, c, 0, 0); SBAR();
              if (c > 0) M2(ak, wk, 1);
              RW(wk, c, 0, 1); RW(wv, c, 0, 2); SBAR();
              if (c > 0) MV(1);
              RX(xa, c, 1, 0); RX(xb, c, 1, 1); RW(wq, c, 1, 0); SBAR();
              M2(aq, wq, 0);
              RW(wk, c, 1, 1); RW(wv, c, 1, 2); SBAR();
              M2(ak, wk, 0);
              MV(0);
              if (c + 2 < 8 && wid < 5) DMA_ALL(c + 2);
          }
          M2(aq, wq, 1); M2(ak, wk, 1); MV(1);
#undef RX
#undef RW
#undef M2
#undef MV
#undef DMA_ALL
#undef DMA_X
        }
        LANES();
        constexpr float QS = 0.25f * 1.4426950408889634f;
        const bf16x8 qf = __builtin_bit_cast(bf16x8, join16(aq[0], aq[1], QS, QS));
        const unsigned kvg = WS_KV + (unsigned)(hd * 8 + t) * 16384u, og = WS_O + (unsigned)((8 * t + wid) * 32 + hd) * 1024u;
        { const u32x4 kk = join16(ak[0], ak[1], 1.f, 1.f), vv = join16(av[0], av[1], 1.f, 1.f);
          const int ok = wid * 1024 + lane * 16, ov = wid * 1024 + (g4 & 1) * 512 + hh * 256 + r16 * 16;
          *(LAS u32x4*)(lds + ok) = kk; *(LAS u32x4*)(lds + L_V + ov) = vv;
          asm volatile("s_waitcnt lgkmcnt(0)" ::: "memory");
          if (lane == 0) __hip_atomic_fetch_add(sy + SY_AW, 1u, RLX_WG);
          st1(kk, rs, ok, kvg); st1(vv, rs, ov + 8192, kvg); }
        lds_wait_ge<1>(sy + SY_AW, 8);
        EPOCH_TAKE();
        drain();
        if (lds_arrive(sy + SY_KV, lane) == 7 && lane == 0) __hip_atomic_store((GAS u64*)(ws + flag_at(0, hd, t)), tag, RLX_AGENT);
        const int tc = ((t ^ wid) & 4) | ((t + wid) & 3);
        const unsigned rkv = WS_KV + (unsigned)(hd * 8 + tc) * 16384u;
#define FETCH(NB) do { poll_tags(ws, flag_at(0, hd, tc), lane == 0, tag); \
            _Pragma("unroll") for (int b_ = 0; b_ < 16 / (NB); ++b_) { u32x4 stg[NB]; \
                _Pragma("unroll") for (int i = 0; i < (NB); ++i) stg[i] = ld1(rs, lane * 16 + (b_ * (NB) + i) * 1024, rkv); \
                if (b_ == 0 && wid > 2) lds_wait_ge(sy + SY_FA, 4);              \
                _Pragma("unroll") for (int i = 0; i < (NB); ++i) *(LAS u32x4*)(lds + ((b_ * (NB) + i) >> 3) * L_V + wid * 8192 + ((b_ * (NB) + i) & 7) * 1024 + lane * 16) = stg[i]; } \
            asm volatile("s_waitcnt lgkmcnt(0)" ::: "memory"); \
            if (lane == 0) __hip_atomic_store(sy + SY_READY + wid, 1u, RLX_WG); } while (0)
        if (wid > 0 && wid < 4) FETCH(16);
        const int idx = 0xDDDD;
        const LAS char* kp = lds + lane * 16;
        const bool vl = r32 < 16;
        const LAS char* vp = lds + (vl ? L_V + 512 + hh * 256 + r32 * 16 : (r32 & 11) == 0 ? L_MISC : L_MISC + 496);
        const int vstep = vl ? 2048 : 0;
        f32x16 o = (f32x16){}, negm, sA, sB = (f32x16){}; float gate = 0.f;
        bf16x8 kf = ldsr(kp), v0 = ldsr(vl ? vp - 512 : vp), v1 = (bf16x8){};
        { const f32x16 s0 = MFMA(kf, qf, (f32x16){}); const float m0 = -xhalf_max(rowmax16(s0));
#pragma unroll
          for (int i = 0; i < 16; ++i) negm[i] = m0; }
        sA = MFMA(kf, qf, negm);
        kf = ldsr(kp + 1024);
        __builtin_amdgcn_s_setprio(0);
#define EX(S, a, b) do { _Pragma("unroll") for (int i_ = (a); i_ < (b); ++i_) S[i_] = __builtin_amdgcn_exp2f(S[i_]); PIN(S); SBAR(); } while (0)
#define LO8(S) __builtin_bit_cast(bf16x16, __builtin_shufflevector(S, S, 0, 1, 2, 3, 4, 5, 6, 7))
#define HI8(S) __builtin_bit_cast(bf16x16, __builtin_shufflevector(S, S, 8, 9, 10, 11, 12, 13, 14, 15))
#define SM(a, b) do { o = __builtin_amdgcn_smfmac_f32_32x32x32_bf16(a, b, o, idx, 0, 0); PIN(o); SBAR(); } while (0)
#define TILE(CUR, OTH, KO, VO) do { SBAR(); EX(CUR, 0, 2); SM(v1, HI8(OTH)); v1 = ldsr(vp + (VO)); SBAR(); EX(CUR, 2, 6); \
            OTH = MFMA(kf, qf, negm); PIN(OTH); SBAR(); kf = ldsr(kp + (KO)); SBAR(); EX(CUR, 6, 10); SM(v0, LO8(CUR)); v0 = ldsr(vp + (VO) + 512); SBAR(); EX(CUR, 10, 16); } while (0)
#define PAIR(KO) do { TILE(sA, sB, (KO) + 2048, 0); TILE(sB, sA, (KO) + 3072, 1024); vp += vstep; } while (0)
        PAIR(0); PAIR(2048); PAIR(4096);
        lds_wait_ge(sy + SY_READY + 1, 1);
        PAIR(6144);
        if (wid >= 4) { __builtin_amdgcn_s_setprio(3); FETCH(8); }
#undef FETCH
        for (int j = 1; j < 8; ++j) {
            kp += 8192;
            PAIR(0); PAIR(2048); PAIR(4096);
            if (j < 7) lds_wait_ge(sy + SY_READY + j + 1, 1);
            PAIR(6144);
            if (j == 4 && lane == 0) __hip_atomic_fetch_add(sy + SY_PASS, 1u, RLX_WG);
            if (j == 6 && lane == 0) __hip_atomic_fetch_add(sy + SY_TRIG, 1u, RLX_WG);
            if (j == 6) { lds_wait_ge(sy + SY_FSEEN, 4); gate = *(const LAS float*)(lds + g_at(32 * wid + r32)); }
        }
        SM(v1, HI8(sB));
#undef PAIR
#undef TILE
#undef SM
#undef EX
        asm volatile("s_waitcnt lgkmcnt(0)" ::: "memory");
        if (lane == 0) __hip_atomic_fetch_add(sy + SY_DONE, 1u, RLX_WG);
        { float chk = o[8] * 0.f;
#pragma unroll
          for (int i = 0; i < 8; ++i) chk = __builtin_fmaf(o[i], 0.f, chk);
          if (__builtin_expect(0 != __builtin_amdgcn_ballot_w64(!(chk == 0.f) || o[8] > 1.0e30f), 0)) {
              float m = -__builtin_inff();
#pragma nounroll
              for (int tl = 0; tl < 64; ++tl) { const bf16x8 kg = __builtin_bit_cast(bf16x8, ld1(rs, lane * 16, WS_KV + (unsigned)(hd * 8 + (tl >> 3)) * 16384u + (tl & 7) * 1024));
                  m = __builtin_fmaxf(m, rowmax16(MFMA(kg, qf, (f32x16){}))); }
              m = -xhalf_max(m);
#pragma unroll
              for (int i = 0; i < 16; ++i) negm[i] = m;
              o = (f32x16){};
              const u32x4 ones = {0x3F803F80u, 0x3F803F80u, 0x3F803F80u, 0x3F803F80u};
#pragma nounroll
              for (int tl = 0; tl < 64; ++tl) { const unsigned so = WS_KV + (unsigned)(hd * 8 + (tl >> 3)) * 16384u + (tl & 7) * 1024;
                  const bf16x8 kg = __builtin_bit_cast(bf16x8, ld1(rs, lane * 16, so));
                  const u32x4 a0 = ld1(rs, 8192 + hh * 256 + r16 * 16, so), a1 = ld1(rs, 8192 + 512 + hh * 256 + r16 * 16, so);
                  o = pv(__builtin_bit_cast(bf16x8, vl ? a0 : ones), __builtin_bit_cast(bf16x8, vl ? a1 : ones), exp16(MFMA(kg, qf, negm)), o, idx); }
          } }
        { const u32x4 ov = pack8(o, 0, gate * __builtin_amdgcn_rcpf(o[8]));
          st1(unmark(ov), rs, lane * 16, og); }
        { const int rt = wid & 1, kq = wid >> 1, kreg = (kq >> 1) * L_V + (kq & 1) * 16384;
          const unsigned ob = WS_O + (unsigned)((8 * t + 2 * rq + rt) * 32 + 8 * kq) * 1024u;
#pragma nounroll
          for (unsigned s = 0; s < SPIN_CAP; ++s) {
              if (__builtin_popcountll(__builtin_amdgcn_ballot_w64(__builtin_amdgcn_raw_buffer_load_b32(rs, (lane & 7) * 1024 + 1020, ob, 16) == SENT)) <= 8 * LATE_OK) break;
              __builtin_amdgcn_s_sleep(1); CFENCE(); }
          u32x4 of[8];
#pragma unroll
          for (int i = 0; i < 8; ++i) of[i] = ld1(rs, lane * 16, ob + i * 1024);
          lds_wait_ge<1>(sy + SY_FP + kq, 1);
          f32x16 c[2];
#pragma unroll
          for (int ct = 0; ct < 2; ++ct)
#pragma unroll
              for (int g = 0; g < 4; ++g) { const f32x4 v = *(const LAS f32x4*)(lds + kreg + 32768 + (2 * rt + ct) * 4096 + g * 1024 + lane * 16);
#pragma unroll
                  for (int i = 0; i < 4; ++i) c[ct][4 * g + i] = v[i]; }
          PIN(c[0]); PIN(c[1]);
#pragma nounroll
          for (unsigned s = 0; s < SPIN_CAP; ++s) {
              unsigned miss = 0;
#pragma unroll
              for (int i = 0; i < 8; ++i) if (marked(of[i])) miss |= 1u << i;
              if (miss == 0) break;
              CFENCE();
#pragma unroll
              for (int i = 0; i < 8; ++i) if ((miss >> i) & 1) of[i] = ld1(rs, lane * 16, ob + i * 1024);
          }
#pragma unroll
          for (int n = 0; n < 8; ++n) { const LAS char* p = lds + kreg + n * 2048 + lane * 16;
              c[0] = MFMA(ldsr(p), __builtin_bit_cast(bf16x8, of[n]), c[0]); c[1] = MFMA(ldsr(p + 1024), __builtin_bit_cast(bf16x8, of[n]), c[1]); }
          f32x8 own;
#pragma unroll
          for (int pc = 0; pc < 4; ++pc) {
              if (pc == kq) {
#pragma unroll
                  for (int i = 0; i < 8; ++i) own[i] = c[pc >> 1][8 * (pc & 1) + i];
              } else {
#pragma unroll
                  for (int g = 0; g < 2; ++g) { const int r = 8 * (pc & 1) + 4 * g; *(LAS f32x4*)(lds + kreg + 32768 + rt * 8192 + pc * 2048 + g * 1024 + lane * 16) = (f32x4){c[pc >> 1][r], c[pc >> 1][r + 1], c[pc >> 1][r + 2], c[pc >> 1][r + 3]}; }
              } }
          asm volatile("s_waitcnt lgkmcnt(0)" ::: "memory");
          if (lane == 0) __hip_atomic_fetch_add(sy + SY_RED + rt, 1u, RLX_WG);
          lds_wait_ge<0>(sy + SY_RED + rt, 4);
#pragma unroll
          for (int k = 1; k < 4; ++k) { const int w = (kq + k) & 3;
#pragma unroll
              for (int g = 0; g < 2; ++g) { const f32x4 v = *(const LAS f32x4*)(lds + (w >> 1) * L_V + (w & 1) * 16384 + 32768 + rt * 8192 + kq * 2048 + g * 1024 + lane * 16);
#pragma unroll
                  for (int i = 0; i < 4; ++i) own[4 * g + i] += v[i]; } }
          auto a = __builtin_amdgcn_permlane32_swap(pack2(own[0], own[1]), pack2(own[4], own[5]), false, false), b = __builtin_amdgcn_permlane32_swap(pack2(own[2], own[3]), pack2(own[6], own[7]), false, false);
          *(u32x4*)(out + (size_t)(256 * t + 64 * rq + 32 * rt + r32) * DM + 64 * cp + 16 * kq + 8 * hh) = (u32x4){a[0], b[0], a[1], b[1]};
          if (bx == 0 && wid == 0 && lane == 0) __hip_atomic_store((GAS u64*)(ws + WS_FLAGS), tag, RLX_AGENT);
        }
    } else {
        const int f = wid - 8, i32 = rq + 4 * t;
        { LANES(); const int xsw = g4 ^ (r16 >> 1);
          const int xo = (64 * f + r16) * 128, fo = XB + 6144 + (4 * g4 + q4) * 128 + p4 * 8, fsw = (2 * g4 + (q4 >> 1)) & 3;
          bf16x8 xf[2][4], wf[2][4];
#define RX(c, ks, rt) xf[ks][rt] = ldsr(lds + BUFOF(c) + xo + (rt) * 2048 + ((xsw ^ (4 * (ks))) * 16))
#define RW(c, ks, cb) wf[ks][cb] = ldtr(lds + BUFOF(c) + fo + (ks) * 4096 + (((cb) ^ fsw) * 32), lds + BUFOF(c) + fo + (ks) * 4096 + 2048 + (((cb) ^ fsw) * 32))
#define M2(ks, cb, r0) do { af[cb][r0] = MFMA16(wf[ks][cb], xf[ks][r0], af[cb][r0]); af[cb][r0 + 1] = MFMA16(wf[ks][cb], xf[ks][r0 + 1], af[cb][r0 + 1]); PIN(af[cb][r0]); PIN(af[cb][r0 + 1]); SBAR(); } while (0)
#define F_SYNC(c) do { asm volatile("s_waitcnt lgkmcnt(0)\n\ts_barrier" ::: "memory"); \
              if ((c) == 7) { LANES();     \
                  const int u = i32 + 32 * f; const char* src = u < 112 ? (const char*)w2 + (size_t)u * 16384 : (const char*)wo + (size_t)(u - 112) * 16384; \
                  const unsigned vo = (lane >> 3) * 1024 + cp * 128 + (lane & 7) * 16; \
                  glds16(vo, src, lds0 + L_RT + f * 2048); glds16(vo, src + 8192, lds0 + L_RT + f * 2048 + 1024); \
                  if (f == 0 && i32 < 16) { const char* s2 = (const char*)wo + (size_t)(i32 + 16) * 16384; glds16(vo, s2, lds0 + L_RT + 8192); glds16(vo, s2 + 8192, lds0 + L_RT + 8192 + 1024); } } } while (0)
#define F_LEAVE() LANES(); \
              asm volatile("s_waitcnt lgkmcnt(0)" ::: "memory"); \
              if (lane == 0) __hip_atomic_fetch_add(sy + SY_FA, 1u, RLX_WG); \
              __builtin_amdgcn_s_setprio(2);
#define F_RETILE() do { \
              _Pragma("unroll") for (int i = 0; i < 2; ++i)     \
                  st1((u32x4){SENT, SENT, SENT, SENT}, rs, lane * 16, WS_O + (unsigned)((8 * t + 2 * f + i) * 32 + hd) * 1024u); \
              for (int n = f; n < ((f == 0 && i32 < 16) ? 5 : 4); n += 4) {        \
                  const int u = i32 + 32 * n, r0 = (u < 112 ? 8 : 4) * hh + q4, r1 = u < 112 ? 512 : 1024; \
                  _Pragma("unroll") for (int ct = 0; ct < 2; ++ct) { const LAS char* s = lds + L_RT + n * 2048 + r0 * 128 + (g4 & 1) * 32 + p4 * 8 + ct * 64; \
                      st1(__builtin_bit_cast(u32x4, ldtr(s, s + r1)), rs, lane * 16, WS_W + (unsigned)((2 * cp + ct) * 144 + u) * 1024u); } } } while (0)
          if (hasF) {
              f32x4 af[4][4];
#pragma unroll
              for (int c = 0; c < 8; ++c) {
                  F_SYNC(c);
                  if (c == 0) {
#pragma unroll
                      for (int cb = 0; cb < 4; ++cb) { const u32x2 w = *(const LAS u32x2*)(lds + L_B1 + cb * 32 + g4 * 8);
#pragma unroll
                          for (int rt = 0; rt < 4; ++rt) af[cb][rt] = (f32x4){U2F(w[0] << 16), U2F(w[0] & 0xffff0000u), U2F(w[1] << 16), U2F(w[1] & 0xffff0000u)}; }
                  }
                  if (c == 7) lds_wait_ge<2>(sy + SY_AW, 8);
                  if (c > 0) M2(1, 0, 0);
                  RX(c, 0, 0); SBAR();
                  if (c > 0) M2(1, 0, 2);
                  RW(c, 0, 0); SBAR();
                  if (c > 0) M2(1, 1, 0);
                  RX(c, 0, 1); SBAR();
                  if (c > 0) M2(1, 1, 2);
                  RX(c, 0, 2); SBAR();
                  if (c > 0) M2(1, 2, 0);
                  RX(c, 0, 3); SBAR();
                  if (c > 0) M2(1, 2, 2);
                  RW(c, 0, 1); SBAR();
                  if (c > 0) M2(1, 3, 0);
                  RW(c, 0, 2); SBAR();
                  if (c > 0) M2(1, 3, 2);
                  RW(c, 0, 3); SBAR();
                  M2(0, 0, 0); RX(c, 1, 0); RW(c, 1, 0); SBAR();
                  M2(0, 0, 2); RX(c, 1, 1); RX(c, 1, 2); SBAR();
                  M2(0, 1, 0); RX(c, 1, 3); RW(c, 1, 1); SBAR();
                  M2(0, 1, 2); RW(c, 1, 2); RW(c, 1, 3); SBAR();
                  M2(0, 2, 0); M2(0, 2, 2); M2(0, 3, 0); M2(0, 3, 2);
              }
              M2(1, 0, 0); M2(1, 0, 2); M2(1, 1, 0); M2(1, 1, 2); M2(1, 2, 0); M2(1, 2, 2); M2(1, 3, 0); M2(1, 3, 2);
              { F_LEAVE();
#pragma unroll
                for (int cb = 0; cb < 4; ++cb) {
#pragma unroll
                    for (int rp = 0; rp < 2; ++rp) {
                        float v[8], e[8];
#pragma unroll
                        for (int i = 0; i < 8; ++i) { v[i] = af[cb][2 * rp + (i >> 2)][i & 3];
                            e[i] = v[i] * __builtin_fmaf(v[i] * v[i], -2.f * 0.7978845608028654f * 1.4426950408889634f * 0.044715f, -2.f * 0.7978845608028654f * 1.4426950408889634f); }
                        SBAR();
#pragma unroll
                        for (int i = 0; i < 8; ++i) e[i] = __builtin_amdgcn_exp2f(e[i]);
                        SBAR();
#pragma unroll
                        for (int i = 0; i < 8; ++i) e[i] = 1.f + e[i];
                        SBAR();
#pragma unroll
                        for (int i = 0; i < 8; ++i) e[i] = __builtin_amdgcn_rcpf(e[i]);
                        SBAR();
                        st1(join16((f32x4){v[0] * e[0], v[1] * e[1], v[2] * e[2], v[3] * e[3]}, (f32x4){v[4] * e[4], v[5] * e[5], v[6] * e[6], v[7] * e[7]}, 1.f, 1.f), rs, lane * 16,
                            WS_H + (unsigned)((8 * t + 2 * f + rp) * 112 + 4 * fb + cb) * 1024u);
                        SBAR();
                    }
                }
                asm volatile("s_waitcnt vmcnt(8)" ::: "memory");
                F_RETILE(); }
          } else {
              f32x4 az[4] = {};
#pragma unroll
              for (int c = 0; c < 8; ++c) {
                  F_SYNC(c);
                  if (hd == 31) {
#pragma unroll
                      for (int ks = 0; ks < 2; ++ks) { const bf16x8 gf = ldsr(lds + L_GATE + CS(c) * 128 + ks * 64 + g4 * 16);
#pragma unroll
                          for (int rt = 0; rt < 4; ++rt) { RX(c, ks, rt); az[rt] = MFMA16(gf, xf[ks][rt], az[rt]); } }
                  }
              }
              { F_LEAVE(); drain(); F_RETILE();
                if (hd == 31) {
                    const float z = bfround(g4 == 0 ? az[0][0] : g4 == 1 ? az[1][0] : g4 == 2 ? az[2][0] : az[3][0]);
                    __hip_atomic_store((GAS float*)(ws + WS_G + (256 * t + 64 * f + lane) * 4), bfround(__builtin_amdgcn_rcpf(1.f + __builtin_amdgcn_exp2f(-1.4426950408889634f * z))), RLX_AGENT);
                } }
          }
#undef F_SYNC
#undef F_LEAVE
#undef F_RETILE
#undef RX
#undef RW
#undef M2
        }
        LANES();
        const int freg = (f >> 1) * L_V + (f & 1) * 16384;
        EPOCH_TAKE();
        drain();
        if (lds_arrive(sy + SY_F, lane) == 3 && lane == 0) __hip_atomic_store((GAS u64*)(ws + flag_at(1, hd, t)), tag, RLX_AGENT);
        f32x16 acc[4] = {};
        poll_tags<4>(ws, hh ? flag_at(1, cp + 8 * (r32 & 3), r32 >> 2) : flag_at(1, r32, t), true, tag);
        *(LAS float*)(lds + g_at(64 * f + lane)) = __hip_atomic_load((GAS float*)(ws + WS_G + (256 * t + 64 * f + lane) * 4), RLX_AGENT);
        asm volatile("s_waitcnt lgkmcnt(0)" ::: "memory");
        if (lane == 0) __hip_atomic_fetch_add(sy + SY_FSEEN, 1u, RLX_WG);
        __builtin_amdgcn_s_setprio(0);
        const unsigned hb = WS_H + (unsigned)((8 * t + 2 * rq) * 112 + 28 * f) * 1024u, wb = WS_W + (unsigned)(2 * cp * 144 + 28 * f) * 1024u;
        u32x4 fr[4][4];
#define LDK(slot, n) do { fr[slot][0] = ld1(rs, lane * 16, wb + (n) * 1024); fr[slot][1] = ld1(rs, lane * 16, wb + (144 + (n)) * 1024); \
                          fr[slot][2] = ld1(rs, lane * 16, hb + (n) * 1024); fr[slot][3] = ld1(rs, lane * 16, hb + (112 + (n)) * 1024); } while (0)
#define BC(v) __builtin_bit_cast(bf16x8, v)
#define MM4(w0, w1_, h0, h1) do { acc[0] = MFMA(w0, h0, acc[0]); acc[1] = MFMA(w0, h1, acc[1]); acc[2] = MFMA(w1_, h0, acc[2]); acc[3] = MFMA(w1_, h1, acc[3]); PIN(acc[0]); PIN(acc[1]); PIN(acc[2]); PIN(acc[3]); SBAR(); } while (0)
#define FFN2(n0, n1) do { _Pragma("unroll") for (int n = (n0); n < (n1); ++n) { MM4(BC(fr[n & 3][0]), BC(fr[n & 3][1]), BC(fr[n & 3][2]), BC(fr[n & 3][3])); if (n + 4 < (n1)) LDK(n & 3, n + 4); SBAR(); } } while (0)
#pragma unroll
        for (int n = 0; n < 4; ++n) LDK(n, n);
        FFN2(0, NP1);
        { u32x4 tmp[16];
#pragma unroll
          for (int i = 0; i < 16; ++i) tmp[i] = ld1(rs, lane * 16, WS_W + (unsigned)((2 * cp + (i & 1)) * 144 + 112 + 8 * f + (i >> 1)) * 1024u);
          __builtin_amdgcn_s_setprio(2);
          lds_wait_ge<16>(sy + SY_PASS, 8);
#pragma unroll
          for (int i = 0; i < 16; ++i) *(LAS u32x4*)(lds + freg + i * 1024 + lane * 16) = tmp[i]; }
        if (NP1 < 28) {
#pragma unroll
            for (int n = NP1; n < NP1 + 4; ++n) LDK(n & 3, n);
            lds_wait_ge<8>(sy + SY_TRIG, 8);
            __builtin_amdgcn_s_setprio(0);
            FFN2(NP1, 28);
        }
#undef LDK
#undef FFN2
        lds_wait_ge(sy + SY_FSEEN, 4);
        { const float og0 = bfround(1.f - *(const LAS float*)(lds + g_at(64 * rq + r32))), og1 = bfround(1.f - *(const LAS float*)(lds + g_at(64 * rq + 32 + r32)));
#pragma unroll
          for (int ct = 0; ct < 2; ++ct)
#pragma unroll
              for (int g = 0; g < 4; ++g) { u32x2 w = *(const LAS u32x2*)(lds + L_B2 + ct * 64 + g * 16 + hh * 8); if (f != 0) w = (u32x2){0u, 0u};
                  const float bb[4] = {U2F(w[0] << 16), U2F(w[0] & 0xffff0000u), U2F(w[1] << 16), U2F(w[1] & 0xffff0000u)};
#pragma unroll
                  for (int i = 0; i < 4; ++i) { acc[2 * ct][4 * g + i] = (acc[2 * ct][4 * g + i] + bb[i]) * og0; acc[2 * ct + 1][4 * g + i] = (acc[2 * ct + 1][4 * g + i] + bb[i]) * og1; } } }
#undef MM4
#undef BC
        lds_wait_ge<1>(sy + SY_DONE, 8);
#pragma unroll
        for (int qd = 0; qd < 4; ++qd)
#pragma unroll
            for (int g = 0; g < 4; ++g) *(LAS f32x4*)(lds + freg + 32768 + (2 * (qd & 1) + (qd >> 1)) * 4096 + g * 1024 + lane * 16) = (f32x4){acc[qd][4 * g], acc[qd][4 * g + 1], acc[qd][4 * g + 2], acc[qd][4 * g + 3]};
        asm volatile("s_waitcnt lgkmcnt(0)" ::: "memory");
        if (lane == 0) __hip_atomic_store(sy + SY_FP + f, 1u, RLX_WG);
    }
}

extern "C" void kernel_launch(void* const* d_in, const int* in_sizes, int n_in, void* d_out, int out_size, void* d_ws, size_t ws_size, hipStream_t stream) {
    (void)in_sizes; (void)n_in; (void)out_size; (void)ws_size;
    hipLaunchKernelGGL(gated_mix_fused, dim3(256), dim3(768), 0, stream, (const unsigned short*)d_in[0], (const unsigned short*)d_in[1], (const unsigned short*)d_in[2],
                       (const unsigned short*)d_in[4], (const unsigned short*)d_in[5], (const unsigned short*)d_in[6], (const unsigned short*)d_in[7], (const unsigned short*)d_in[8],
                       (unsigned short*)d_out, (char*)d_ws);
}
```
